```python
import torch

def _softmax(x, axis=-1):
    return torch.softmax(x, dim=axis)

def _gelu(x):
    return torch.nn.functional.gelu(x, approximate='tanh')

def _silu(x):
    return torch.nn.functional.silu(x)

def _sigmoid(x):
    return torch.sigmoid(x)

def _layernorm(x, axis=-1, eps=1e-05):
    m = x.mean(dim=axis, keepdim=True)
    v = x.var(dim=axis, keepdim=True, unbiased=False)
    return (x - m) / torch.sqrt(v + eps)

def _scatter_add(tgt, idx, src, axis=0):
    return tgt.clone().index_add_(axis, idx, src)

def _segment_sum(src, seg, n):
    out = torch.zeros((n,) + src.shape[1:], dtype=src.dtype)
    return out.index_add_(0, seg, src)

def _pad(x, pw):
    flat = tuple((v for pair in reversed(pw) for v in pair))
    return torch.nn.functional.pad(x, flat)
kernel_name = 'parallel_sum__memory_aug_recurrent_mem_tr__dense_activation_squared_relu__prompt'

def setup_inputs():

    _w = lambda a: (lambda t: t.to(torch.bfloat16) if t.is_floating_point() else t)(torch.as_tensor(a))

    def _A_setup():
        B, S, Dm = (1, 2048, 640)
        H, Dh = (32, 192)
        M, seg = (8, 32)
        seg = min(seg, S)
        S = S // seg * seg
        return {'x': _w((lambda *s: torch.randn(*s, dtype=torch.bfloat16) * 0.05)(B, S, Dm)), 'mem0': _w((lambda *s: torch.randn(*s, dtype=torch.bfloat16) * 1.6)(B, M, Dm)), 'Wq': _w((lambda *s: torch.randn(*s, dtype=torch.bfloat16) * 0.8)(Dm, H, Dh) * Dm ** (-0.5)), 'Wk': _w((lambda *s: torch.randn(*s, dtype=torch.bfloat16) * 0.8)(Dm, H, Dh) * Dm ** (-0.5)), 'Wv': _w((lambda *s: torch.randn(*s, dtype=torch.bfloat16) * 1.6)(Dm, H, Dh) * Dm ** (-0.5)), 'Wo': _w((lambda *s: torch.randn(*s, dtype=torch.bfloat16) * 1.6)(H, Dh, Dm) * (H * Dh) ** (-0.5)), 'seg_len': seg}
    _A_in = _A_setup()

    def _B_setup():
        B, S, D, F = (1, 2048, 640, 1536)
        return {'x': _w((lambda *s: torch.randn(*s, dtype=torch.bfloat16) * 0.05)(B, S, D)), 'w1': _w((lambda *s: torch.randn(*s, dtype=torch.bfloat16) * 0.05)(D, F)), 'b1': _w((lambda *s: torch.randn(*s, dtype=torch.bfloat16) * 0.05)(F)), 'w2': _w((lambda *s: torch.randn(*s, dtype=torch.bfloat16) * 0.05)(F, D)), 'b2': _w((lambda *s: torch.randn(*s, dtype=torch.bfloat16) * 0.05)(D))}
    _B_in = _B_setup()
    return {'A_x': _A_in['x'], 'A_mem0': _A_in['mem0'], 'A_Wq': _A_in['Wq'], 'A_Wk': _A_in['Wk'], 'A_Wv': _A_in['Wv'], 'A_Wo': _A_in['Wo'], 'A_seg_len': _A_in['seg_len'], 'B_x': _B_in['x'], 'B_w1': _B_in['w1'], 'B_b1': _B_in['b1'], 'B_w2': _B_in['w2'], 'B_b2': _B_in['b2']}

def reference(A_x, A_mem0, A_Wq, A_Wk, A_Wv, A_Wo, A_seg_len, B_x, B_w1, B_b1, B_w2, B_b2):

    def _A_ref(x, mem0, Wq, Wk, Wv, Wo, seg_len):
        B, S, Dm = x.shape
        H, Dh = (Wq.shape[1], Wq.shape[2])
        M = mem0.shape[1]
        scale = Dh ** (-0.5)
        n_seg = S // seg_len
        L = M + seg_len
        idx = torch.arange(L)
        mask = (idx[:, None] < M) | (idx[:, None] >= idx[None, :])
        mem = mem0
        outs = []
        for s in range(n_seg):
            xs = x[:, s * seg_len:(s + 1) * seg_len, :].clone()
            h = torch.concatenate([mem, xs], axis=1)
            q = torch.einsum('btd,dhe->bhte', h, Wq)
            k = torch.einsum('btd,dhe->bhte', h, Wk)
            v = torch.einsum('btd,dhe->bhte', h, Wv)
            scores = torch.einsum('bhqe,bhke->bhqk', q, k) * scale
            scores = torch.where(mask[None, None], scores, -1000000000.0)
            p = _softmax(scores, axis=-1)
            o = torch.einsum('bhqk,bhke->bhqe', p, v)
            o = torch.einsum('bhte,hed->btd', o, Wo)
            mem = o[:, :M, :].clone()
            outs.append(o[:, M:, :])
        return torch.concatenate(outs, axis=1)

    def _B_ref(x, w1, b1, w2, b2):
        h = torch.einsum('bsd,df->bsf', x, w1) + b1
        zero = torch.zeros_like(h)
        r = torch.where(h > zero, h, zero)
        h = r * r
        out = torch.einsum('bsf,fd->bsd', h, w2) + b2
        return out
    x = A_x
    xn = _layernorm(x, axis=-1)
    return x + _A_ref(xn, A_mem0, A_Wq, A_Wk, A_Wv, A_Wo, A_seg_len) + _B_ref(xn, B_w1, B_b1, B_w2, B_b2)
```

```cpp
#include <hip/hip_runtime.h>
#include <stdint.h>
#include <utility>

typedef __bf16 bf16x2 __attribute__((ext_vector_type(2)));
typedef __bf16 bf16x8 __attribute__((ext_vector_type(8)));
typedef __bf16 bf16x16 __attribute__((ext_vector_type(16)));
typedef float f32x2 __attribute__((ext_vector_type(2)));
typedef float f32x4 __attribute__((ext_vector_type(4)));
typedef float f32x16 __attribute__((ext_vector_type(16)));
typedef unsigned u32x2 __attribute__((ext_vector_type(2)));
typedef unsigned u32x4 __attribute__((ext_vector_type(4)));
typedef unsigned u32x8 __attribute__((ext_vector_type(8)));
typedef short s16x4 __attribute__((ext_vector_type(4)));
typedef unsigned short u16;
typedef __amdgpu_buffer_rsrc_t rsrc_t;
#define DI __device__ __forceinline__
#define LAS __attribute__((address_space(3)))
typedef LAS unsigned char* ldsp;

constexpr int NSEG = 64;
constexpr unsigned SENT = 0xFFFFFFFFu;
constexpr unsigned OOB = 0x7FFFFFF0u;
constexpr unsigned SPINCAP = 1u << 16;
constexpr int SC1 = 16;
constexpr unsigned OFF_FLAG1 = 0, OFF_FLAG2 = 16384, OFF_RING = 32768;
constexpr unsigned MEM_B = 10240, PM_B = 24576, PX_B = 81920, Y_B = 327680;
constexpr unsigned OFF_MEM = OFF_RING, OFF_PM = OFF_MEM + 4 * MEM_B, OFF_PX = OFF_PM + 4 * PM_B, OFF_Y = OFF_PX + 4 * PX_B, RING_END = OFF_Y + 4 * Y_B;
constexpr unsigned OFF_XNF = 2u << 20, OFF_T1 = 5u << 20, T1_SEG = 104 * 12288, OFF_U = OFF_T1 + NSEG * T1_SEG, U_SEG = 20 * 40 * 2048;
constexpr unsigned WS_END = OFF_U + NSEG * U_SEG;
constexpr unsigned OFF_DBG = 200u << 20;
static_assert(RING_END <= OFF_XNF && OFF_XNF + NSEG * 40960 <= OFF_T1 && WS_END <= OFF_DBG, "ws layout");
constexpr unsigned WS_RANGE = 256u << 20;
constexpr int LDS_B = 163840, WREG = 36864, OFF_IMG = 4 * WREG;

struct Args {
  const u16 *x, *mem0, *Wq, *Wk, *Wv, *Wo, *w1, *b1, *w2, *b2;
  u16* out;
  unsigned char* ws;
};

template <int... Is, class F> DI void sfor_(std::integer_sequence<int, Is...>, F&& f) { (f(std::integral_constant<int, Is>{}), ...); }
template <int N, class F> DI void sfor(F&& f) { sfor_(std::make_integer_sequence<int, N>{}, f); }

DI unsigned rfl(unsigned x) { return __builtin_amdgcn_readfirstlane(x); }
DI float bf2f(u16 b) { return __uint_as_float((unsigned)b << 16); }
DI unsigned cvtpk(float lo, float hi) { f32x2 v = {lo, hi}; bf16x2 b = __builtin_convertvector(v, bf16x2); return __builtin_bit_cast(unsigned, b); }
DI float rbf(float f) { return __uint_as_float(cvtpk(f, 0.f) << 16); }
DI unsigned canon(unsigned x) { return x < 0xFFFFFFFEu ? x : 0xFFFFFFFEu; }
DI rsrc_t mkrs(const void* p, unsigned range) { return __builtin_amdgcn_make_buffer_rsrc((void*)p, 0, range, 0x00020000); }
DI u32x4 ld4(rsrc_t r, unsigned vo, unsigned so) { return __builtin_amdgcn_raw_buffer_load_b128(r, vo, so, SC1); }
DI u32x2 ld2(rsrc_t r, unsigned vo, unsigned so) { return __builtin_amdgcn_raw_buffer_load_b64(r, vo, so, SC1); }
DI unsigned ld1(rsrc_t r, unsigned vo, unsigned so) { return __builtin_amdgcn_raw_buffer_load_b32(r, vo, so, SC1); }
DI u32x4 ld4p(rsrc_t r, unsigned vo, unsigned so) { return __builtin_amdgcn_raw_buffer_load_b128(r, vo, so, 0); }
DI void st4(u32x4 v, rsrc_t r, unsigned vo, unsigned so) { __builtin_amdgcn_raw_buffer_store_b128(v, r, vo + so, 0, SC1); }
DI void st1(unsigned v, rsrc_t r, unsigned vo, unsigned so) { __builtin_amdgcn_raw_buffer_store_b32(v, r, vo, so, SC1); }
template <class T> DI void pinv(T& v) { asm volatile("" : "+v"(v)); }
template <class T> DI void pina(T& v) { asm volatile("" : "+a"(v)); }
DI f32x4 zero4() { f32x4 z = {0.f, 0.f, 0.f, 0.f}; pinv(z); return z; }
DI void memfence() { asm volatile("" ::: "memory"); }
template <int N> DI void wait_vm() { asm volatile("s_waitcnt vmcnt(%0)" ::"i"(N) : "memory"); }
DI void wait_lgkm() { asm volatile("s_waitcnt lgkmcnt(0)" ::: "memory"); }
DI void bar() { asm volatile("s_waitcnt lgkmcnt(0)\n\ts_barrier" ::: "memory"); }
DI void dma16(rsrc_t r, unsigned vo, unsigned so, unsigned ldsa) {
  unsigned keep;
  asm volatile("s_mov_b32 %0, m0\n\ts_mov_b32 m0, %1\n\ts_nop 0\n\tbuffer_load_dwordx4 %2, %3, %4 offen lds\n\ts_mov_b32 m0, %0" : "=&s"(keep) : "s"(ldsa), "v"(vo), "s"(r), "s"(so) : "memory");
}
DI u32x2 trd(ldsp p) { return __builtin_bit_cast(u32x2, __builtin_amdgcn_ds_read_tr16_b64_v4i16((LAS s16x4*)p)); }
DI u32x4 lds4(ldsp p) { return *(LAS u32x4*)p; }
DI void sts4(ldsp p, u32x4 v) { *(LAS u32x4*)p = v; }
DI unsigned umax4(u32x4 v) { return max(max(v.x, v.y), max(v.z, v.w)); }
DI bf16x8 b8(u32x4 v) { return __builtin_bit_cast(bf16x8, v); }
DI bf16x16 b16(u32x4 a, u32x4 b) { return __builtin_bit_cast(bf16x16, __builtin_shufflevector(a, b, 0, 1, 2, 3, 4, 5, 6, 7)); }
DI f32x4 smf(u32x4 a, bf16x16 b, f32x4 c, int idx) { return __builtin_amdgcn_smfmac_f32_16x16x64_bf16(b8(a), b, c, idx, 0, 0); }
DI f32x4 mf16(u32x4 a, u32x4 b, f32x4 c) { return __builtin_amdgcn_mfma_f32_16x16x32_bf16(b8(a), b8(b), c, 0, 0, 0); }
DI f32x16 mf32(u32x4 a, u32x4 b, f32x16 c) { return __builtin_amdgcn_mfma_f32_32x32x16_bf16(b8(a), b8(b), c, 0, 0, 0); }
template <int RB> DI bf16x16 rd_sp(ldsp p) {
  u32x2 a = trd(p), b = trd(p + 2 * RB), c = trd(p + 32 * RB), d = trd(p + 34 * RB);
  u32x8 r = {a.x, a.y, b.x, b.y, c.x, c.y, d.x, d.y};
  return __builtin_bit_cast(bf16x16, r);
}
template <int RB> DI u32x4 rd_dn(ldsp p) { u32x2 a = trd(p), b = trd(p + 4 * RB); return (u32x4){a.x, a.y, b.x, b.y}; }
DI void wait_flag(rsrc_t rs, unsigned vo, unsigned need, unsigned& seen, bool& dead) {
  if (seen >= need) return;
  for (unsigned sp = 0; !dead; ++sp) {
    memfence();
    unsigned v = ld1(rs, vo, 0);
    for (int m = 32; m >= 1; m >>= 1) v = min(v, (unsigned)__shfl_xor((int)v, m));
    seen = rfl(v);
    if (seen >= need) return;
    if (sp > SPINCAP) dead = true;
    __builtin_amdgcn_s_sleep(2);
  }
}
#define STAMP(s, k)

__global__ void __launch_bounds__(256) k_pre(Args a) {
  const int tid = threadIdx.x, lane = tid & 63, row = blockIdx.x * 4 + (tid >> 6);
  rsrc_t rs = mkrs(a.ws, WS_RANGE);
  {
    unsigned o = (blockIdx.x * 256 + tid) * 16u;
    if (o < RING_END) {
      u32x4 v = {SENT, SENT, SENT, SENT};
      if (o < OFF_RING) v = (u32x4){0u, 0u, 0u, 0u};
      else if (o < OFF_MEM + MEM_B) {
        unsigned l = o - OFF_MEM, c = l >> 9, i = (l >> 6) & 7, cc = (l & 63) >> 1;
        v = *(const u32x4*)(a.mem0 + i * 640 + c * 32 + cc);
        v.x = canon(v.x); v.y = canon(v.y); v.z = canon(v.z); v.w = canon(v.w);
      }
      st4(v, rs, o, 0);
    }
  }
  const u32x4* xr = (const u32x4*)(a.x + (size_t)row * 640);
  u32x4 p0 = xr[lane], p1 = {0u, 0u, 0u, 0u};
  if (lane < 16) p1 = xr[64 + lane];
  float v[16];
#pragma unroll
  for (int i = 0; i < 4; ++i) {
    v[2 * i] = __uint_as_float(p0[i] << 16); v[2 * i + 1] = __uint_as_float(p0[i] & 0xFFFF0000u);
    v[8 + 2 * i] = __uint_as_float(p1[i] << 16); v[9 + 2 * i] = __uint_as_float(p1[i] & 0xFFFF0000u);
  }
  float s = 0.f;
#pragma unroll
  for (int i = 0; i < 16; ++i) s += v[i];
  for (int m = 32; m >= 1; m >>= 1) s += __shfl_xor(s, m);
  const float mean = s * (1.f / 640.f);
  float q = 0.f;
#pragma unroll
  for (int i = 0; i < 8; ++i) { float d = v[i] - mean; q += d * d; }
  if (lane < 16) {
#pragma unroll
    for (int i = 8; i < 16; ++i) { float d = v[i] - mean; q += d * d; }
  }
  for (int m = 32; m >= 1; m >>= 1) q += __shfl_xor(q, m);
  const float mb = rbf(mean), vb = rbf(q * (1.f / 640.f));
  const float den = rbf(sqrtf(rbf(vb + 1e-5f)));
  u32x4 o0, o1;
#pragma unroll
  for (int i = 0; i < 4; ++i) {
    o0[i] = cvtpk(rbf(v[2 * i] - mb) / den, rbf(v[2 * i + 1] - mb) / den);
    o1[i] = cvtpk(rbf(v[8 + 2 * i] - mb) / den, rbf(v[9 + 2 * i] - mb) / den);
  }
  unsigned char* dst = a.ws + OFF_XNF + (row >> 5) * 40960 + (row & 31) * 16;
  *(u32x4*)(dst + lane * 512) = o0;
  if (lane < 16) *(u32x4*)(dst + (64 + lane) * 512) = o1;
}

DI void role_t1(const Args& a, const int i1, ldsp lds, const int lane, const int wv) {
  rsrc_t rs = mkrs(a.ws, WS_RANGE);
  const int kind = i1 >> 5, h = i1 & 31, kh = wv >> 1, nh = wv & 1;
  const u16* W = kind == 0 ? a.Wq : kind == 1 ? a.Wk : kind == 2 ? a.Wv : a.w1;
  const unsigned stride = kind < 3 ? 12288u : 3072u;
  rsrc_t rw = mkrs(W, 640u * stride);
  const unsigned wbase = (h * 192 + nh * 96) * 2 + kh * 320 * stride;
  ldsp reg = lds + wv * WREG;
  const unsigned rega = rfl((unsigned)(uintptr_t)reg);
  unsigned voff[3];
#pragma unroll
  for (int r = 0; r < 3; ++r) { unsigned L = r * 64 + lane; voff[r] = (L / 12) * stride + (L % 12) * 16; }
  auto issue = [&](int ch) {
#pragma unroll
    for (int n = 0; n < 12; ++n) dma16(rw, voff[n % 3], wbase + (ch * 64 + (n / 3) * 16) * stride, rega + (ch % 3) * 12288 + n * 1024);
  };
  issue(0); issue(1); issue(2);
  const int gm = lane >> 4, q = (lane >> 2) & 3, p = lane & 3;
  ldsp tb = reg + (8 * (gm >> 1) + q) * 192 + (4 * (p & 1) + ((2 * (gm & 1) + (p >> 1)) ^ (2 * kh))) * 8;
  u32x4 w[20][3];
  sfor<5>([&](auto CH) {
    constexpr int ch = CH;
    wait_vm<(ch < 3 ? 24 : ch == 3 ? 12 : 0)>();
#pragma unroll
    for (int kk = 0; kk < 4; ++kk)
#pragma unroll
      for (int t = 0; t < 3; ++t) w[ch * 4 + kk][t] = rd_dn<192>(tb + (ch % 3) * 12288 + kk * 16 * 192 + t * 64);
    wait_lgkm();
    if constexpr (ch + 3 < 5) issue(ch + 3);
  });
  const int n = lane & 31, hi = lane >> 5;
  float bias[3][8];
#pragma unroll
  for (int t = 0; t < 3; ++t)
#pragma unroll
    for (int j = 0; j < 8; ++j) bias[t][j] = kind == 3 ? bf2f(a.b1[h * 192 + 96 * nh + 32 * t + 16 * hi + 8 * kh + j]) : 0.f;
  unsigned so, tstr;
  if (kind < 2) { so = (n >> 4) * 6144 + 3 * nh * 1024 + (2 * hi + kh) * 256 + (n & 15) * 16; tstr = 1024; }
  else { so = (6 * nh + hi) * 1024 + kh * 512 + (8 * ((n >> 2) & 3) + 4 * (n >> 4) + (n & 3)) * 16; tstr = 2048; }
  const unsigned xo = lane * 16, xs = OFF_XNF + kh * 20480;
  u32x4 x[20];
#pragma unroll
  for (int t = 0; t < 20; ++t) x[t] = ld4p(rs, xo, xs + t * 1024);
  ldsp xw = reg + lane * 16, xr = lds + (wv ^ 2) * WREG + lane * 16;
  for (int s = 0; s < NSEG; ++s) {
    f32x16 acc[3];
#pragma unroll
    for (int t = 0; t < 3; ++t)
#pragma unroll
      for (int j = 0; j < 16; ++j) acc[t][j] = 0.f;
    const unsigned sn = (s + 1 < NSEG ? s + 1 : s) * 40960 + xs;
#pragma unroll
    for (int t = 0; t < 20; ++t) {
#pragma unroll
      for (int u = 0; u < 3; ++u) acc[u] = mf32(w[t][u], x[t], acc[u]);
      x[t] = ld4p(rs, xo + (t & 3) * 1024, sn + (t >> 2) * 4096);
    }
    const int par = (s & 1) * 6144;
#pragma unroll
    for (int t = 0; t < 3; ++t)
#pragma unroll
      for (int u = 0; u < 2; ++u) {
        f32x4 v = {acc[t][8 + 4 * u], acc[t][9 + 4 * u], acc[t][10 + 4 * u], acc[t][11 + 4 * u]};
        sts4(xw + par + (2 * t + u) * 1024, __builtin_bit_cast(u32x4, v));
      }
    wait_vm<20>();
    bar();
    if (s >= 5 && threadIdx.x == 0) st1((unsigned)s, rs, OFF_FLAG1 + i1 * 128, 0);
    const unsigned ob = OFF_T1 + s * T1_SEG + i1 * 12288;
#pragma unroll
    for (int t = 0; t < 3; ++t) {
      f32x4 r0 = __builtin_bit_cast(f32x4, lds4(xr + par + (2 * t) * 1024)), r1 = __builtin_bit_cast(f32x4, lds4(xr + par + (2 * t + 1) * 1024));
      float v[8];
#pragma unroll
      for (int j = 0; j < 4; ++j) { v[j] = acc[t][j] + r0[j]; v[4 + j] = acc[t][4 + j] + r1[j]; }
      if (kind == 3) {
#pragma unroll
        for (int j = 0; j < 8; ++j) { float z = rbf(rbf(v[j]) + bias[t][j]); z = z > 0.f ? z : 0.f; v[j] = z * z; }
      }
      u32x4 o = {cvtpk(v[0], v[1]), cvtpk(v[2], v[3]), cvtpk(v[4], v[5]), cvtpk(v[6], v[7])};
      st4(o, rs, so, ob + t * tstr);
    }
    memfence();
    if (s < 4 || s == NSEG - 1) {
      wait_vm<0>();
      bar();
      if (threadIdx.x == 0) st1((unsigned)s + 1, rs, OFF_FLAG1 + i1 * 128, 0);
    }
    STAMP(s, 0);
  }
}

DI void role_t2(const Args& a, const int i2, ldsp lds, const int lane, const int wv) {
  rsrc_t rs = mkrs(a.ws, WS_RANGE);
  const u16* W = i2 < 32 ? a.Wo + (size_t)i2 * 192 * 640 : a.w2 + (size_t)(i2 - 32) * 192 * 640;
  rsrc_t rw = mkrs(W, 192u * 1280u);
  ldsp reg = lds + wv * WREG;
  const unsigned rega = rfl((unsigned)(uintptr_t)reg);
  unsigned voff[5];
#pragma unroll
  for (int r = 0; r < 5; ++r) { unsigned L = r * 64 + lane; voff[r] = (L / 20) * 1280 + (L % 20) * 16; }
  auto issue = [&](int ch) {
#pragma unroll
    for (int n = 0; n < 10; ++n) dma16(rw, voff[n % 5], wv * 320 + (ch * 32 + (n / 5) * 16) * 1280, rega + (ch % 3) * 10240 + n * 1024);
  };
  issue(0); issue(1); issue(2);
  const int gm = lane >> 4, q = (lane >> 2) & 3, p = lane & 3;
  ldsp tb = reg + (8 * (gm >> 1) + q) * 320 + (4 * (gm & 1) + p) * 8;
  u32x4 w[12][5];
  sfor<6>([&](auto CH) {
    constexpr int ch = CH;
    wait_vm<(ch < 4 ? 20 : ch == 4 ? 10 : 0)>();
#pragma unroll
    for (int kk = 0; kk < 2; ++kk)
#pragma unroll
      for (int t = 0; t < 5; ++t) w[ch * 2 + kk][t] = rd_dn<320>(tb + (ch % 3) * 10240 + kk * 16 * 320 + t * 64);
    wait_lgkm();
    if constexpr (ch + 3 < 6) issue(ch + 3);
  });
  bool dead = false;
  unsigned seen = 0;
  const unsigned fo = OFF_FLAG1 + (64 + i2) * 128;
  const unsigned vo = lane * 16, vs = OFF_T1 + (64 + i2) * 12288;
  const int c32 = lane & 31, hi = lane >> 5;
  const unsigned so = (c32 >> 4) * 1024 + hi * 512 + (c32 & 15) * 16, ss = OFF_U + (wv * 5 * 40 + i2) * 2048;
  u32x4 cur[12], nxt[12];
  wait_flag(rs, fo, 1, seen, dead);
  memfence();
#pragma unroll
  for (int t = 0; t < 12; ++t) cur[t] = ld4(rs, vo + (t & 3) * 1024, vs + (t >> 2) * 4096);
  for (int s = 0; s < NSEG; ++s) {
    const int s1 = s + 1 < NSEG ? s + 1 : s;
    if (seen < (unsigned)s1 + 1) { memfence(); seen = rfl(ld1(rs, fo, 0)); }
    const bool pre = seen >= (unsigned)s1 + 1;
    memfence();
    if (pre) {
#pragma unroll
      for (int t = 0; t < 12; ++t) nxt[t] = ld4(rs, vo + (t & 3) * 1024, vs + (t >> 2) * 4096 + s1 * T1_SEG);
    }
    f32x16 acc[5];
#pragma unroll
    for (int t = 0; t < 5; ++t)
#pragma unroll
      for (int j = 0; j < 16; ++j) acc[t][j] = 0.f;
#pragma unroll
    for (int t = 0; t < 12; ++t)
#pragma unroll
      for (int u = 0; u < 5; ++u) acc[u] = mf32(cur[t], w[t][u], acc[u]);
    if (s >= 5) {
      wait_vm<0>();
      bar();
      if (threadIdx.x == 0) st1((unsigned)s, rs, OFF_FLAG2 + i2 * 4, 0);
    }
#pragma unroll
    for (int t = 0; t < 5; ++t)
#pragma unroll
      for (int u = 0; u < 2; ++u) {
        u32x4 o = {cvtpk(acc[t][8 * u], acc[t][8 * u + 1]), cvtpk(acc[t][8 * u + 2], acc[t][8 * u + 3]), cvtpk(acc[t][8 * u + 4], acc[t][8 * u + 5]), cvtpk(acc[t][8 * u + 6], acc[t][8 * u + 7])};
        st4(o, rs, so + u * 256, ss + t * 81920 + s * U_SEG);
      }
    if (s < 4 || s == NSEG - 1) {
      wait_vm<0>();
      bar();
      if (threadIdx.x == 0) st1((unsigned)s + 1, rs, OFF_FLAG2 + i2 * 4, 0);
    }
    if (!pre) {
      wait_flag(rs, fo, (unsigned)s1 + 1, seen, dead);
      memfence();
#pragma unroll
      for (int t = 0; t < 12; ++t) nxt[t] = ld4(rs, vo + (t & 3) * 1024, vs + (t >> 2) * 4096 + s1 * T1_SEG);
    }
#pragma unroll
    for (int t = 0; t < 12; ++t) cur[t] = nxt[t];
    STAMP(s, 0);
  }
}

template <int N, class F> DI void poll4(u32x4 (&v)[N], F&& ld, bool& dead) {
  for (unsigned sp = 0;; ++sp) {
    memfence();
    unsigned m = 0;
#pragma unroll
    for (int i = 0; i < N; ++i) v[i] = ld(i);
#pragma unroll
    for (int i = 0; i < N; ++i) m = max(m, umax4(v[i]));
    if (!__any(m == SENT) || dead) return;
    if (sp > SPINCAP) dead = true;
  }
}

DI void role_qk(const Args& a, const int h, ldsp lds, const int lane, const int wv) {
  rsrc_t rs = mkrs(a.ws, WS_RANGE);
  const int mat = wv >> 1, hf = wv & 1;
  rsrc_t rw = mkrs(mat ? a.Wk : a.Wq, 640u * 12288u);
  const unsigned wbase = (h * 192 + hf * 96) * 2;
  ldsp reg = lds + wv * WREG;
  const unsigned rega = rfl((unsigned)(uintptr_t)reg);
  const int g = lane >> 4, c = lane & 15;
  bf16x16 w[8][6];
  {
    unsigned voff[3];
#pragma unroll
    for (int r = 0; r < 3; ++r) { unsigned L = r * 64 + lane; voff[r] = (L / 12) * 12288 + (L % 12) * 16; }
    auto issue = [&](int ch) {
      const int t = (ch + 2) % 10;
#pragma unroll
      for (int n = 0; n < 12; ++n) dma16(rw, voff[n % 3], wbase + (t * 64 + (n / 3) * 16) * 12288, rega + (ch % 3) * 12288 + n * 1024);
    };
    issue(0); issue(1); issue(2);
    const int q = c >> 2, p = c & 3;
    ldsp tb = reg + (16 * (g >> 1) + 4 * (g & 1) + 8 * (q >> 1) + (q & 1)) * 192 + p * 8;
    sfor<10>([&](auto CH) {
      constexpr int ch = CH;
      wait_vm<(ch < 8 ? 24 : ch == 8 ? 12 : 0)>();
      if constexpr (ch < 8) {
#pragma unroll
        for (int t = 0; t < 6; ++t) w[ch][t] = rd_sp<192>(tb + (ch % 3) * 12288 + t * 32);
#pragma unroll
        for (int t = 0; t < 6; ++t) if (ch * 6 + t < 32) pina(w[ch][t]);
        wait_lgkm();
        if constexpr (ch + 3 < 10) issue(ch + 3);
      } else {
        bf16x16 tmp[6];
#pragma unroll
        for (int t = 0; t < 6; ++t) tmp[t] = rd_sp<192>(tb + (ch % 3) * 12288 + t * 32);
        wait_lgkm();
#pragma unroll
        for (int t = 0; t < 6; ++t) {
          u32x8 v = __builtin_bit_cast(u32x8, tmp[t]);
          sts4(reg + (ch % 3) * 12288 + t * 2048 + lane * 16, __builtin_shufflevector(v, v, 0, 1, 2, 3));
          sts4(reg + (ch % 3) * 12288 + t * 2048 + 1024 + lane * 16, __builtin_shufflevector(v, v, 4, 5, 6, 7));
        }
      }
    });
  }
  bool dead = false;
  unsigned seen = 0;
  const unsigned fo = OFF_FLAG1 + (h + 32 * (lane & 1)) * 128;
  ldsp kxs = lds + 12288, qxs = lds + WREG + 12288, qimg = lds + OFF_IMG, kimg = lds + OFF_IMG + 6144;
  const unsigned sgo = lane * 16, sgs = OFF_T1 + (wv < 2 ? 32 + h : h) * 12288 + (wv & 1) * 6144;
  ldsp sgw = (wv < 2 ? kxs : qxs) + (wv & 1) * 6144 + ((wv == 1 ? lane ^ 12 : lane) * 16);
  const int idx = (lane & 1) ? 0xEEEE : 0x4444;
  const unsigned mo = (g >> 1) * 512 + (c >> 3) * 256 + ((c >> 1) & 3) * 64 + (g & 1) * 32 + (c & 1) * 16;
  ldsp rw0 = reg + 2 * 12288 + lane * 16, rw1 = reg + lane * 16;
  ldsp iw0, iw1;
  if (mat == 0) { iw0 = qimg + hf * 3072 + (c >> 3) * 256 + 2 * g * 16 + (c & 7) * 2; iw1 = iw0 + 16; }
  else { iw0 = kimg + hf * 3072 + (4 * g + ((c >> 1) & 1)) * 16 + ((c >> 2) * 2 + (c & 1)) * 2; iw1 = iw0 + 32; }
  ldsp qb = wv == 0 ? qimg + g * 256 + (c & 7) * 16 : qxs + ((wv - 1) & 1) * 6144 + g * 256 + c * 16;
  ldsp kb = kimg + g * 512 + c * 16;
  ldsp kx0, kx1;
  {
    const int rg = c >> 2, rr = c & 3;
    int k0 = wv == 0 ? 16 * (rg >> 1) + 4 * (rr >> 1) + 2 * (rg & 1) + (rr & 1) : 8 * rg + rr;
    int k1 = k0 + (wv == 0 ? 8 : 4);
    kx0 = kxs + (k0 >> 4) * 6144 + g * 256 + ((k0 & 15) ^ ((k0 >> 4) * 12)) * 16;
    kx1 = kxs + (k1 >> 4) * 6144 + g * 256 + ((k1 & 15) ^ ((k1 >> 4) * 12)) * 16;
  }
  const int qlim = wv == 0 ? 1000 : 16 * (wv - 1) + c - 8 * g;
  unsigned pxo, pmo, pstr;
  if (wv == 0) {
    unsigned b = OFF_PM + (c >> 2) * 12288 + h * 384;
    pxo = c < 8 ? b + (g >> 1) * 128 + (c & 3) * 32 + (g & 1) * 16 : OOB;
    pmo = c < 8 ? b + 256 + ((c & 3) * 2 + (g & 1)) * 8 + (g >> 1) * 4 : OOB;
    pstr = PM_B;
  } else {
    unsigned b = OFF_PX + ((wv - 1) * 32 + h) * 1280;
    pxo = b + g * 256 + c * 16; pmo = b + 1024 + c * 16 + g * 4; pstr = PX_B;
  }
  const unsigned ra0 = lane < 48 ? OFF_PM + (lane / 24) * 12288 + h * 384 + (lane % 24) * 16 : OOB;
  const unsigned ra2 = lane < 32 ? OFF_PX + (lane >> 4) * 40960 + h * 1280 + 1024 + (lane & 15) * 16 : OOB;
  {
    wait_flag(rs, fo, 1, seen, dead);
    memfence();
    u32x4 sg[6];
#pragma unroll
    for (int i = 0; i < 6; ++i) sg[i] = ld4(rs, sgo + (i & 3) * 1024, sgs + (i >> 2) * 4096);
    bar();
#pragma unroll
    for (int i = 0; i < 6; ++i) sts4(sgw + i * 1024, sg[i]);
  }
  for (int s = 0; s < NSEG; ++s) {
    const int slot = s & 3;
    u32x4 A[10];
    poll4<10>(A, [&](int i) { return ld4(rs, mo + (i & 3) * 1024, OFF_MEM + (i >> 2) * 4096 + slot * MEM_B); }, dead);
    STAMP(s, 0);
    f32x4 acc[6];
#pragma unroll
    for (int t = 0; t < 6; ++t) acc[t] = zero4();
    sfor<8>([&](auto T) {
      constexpr int t = T;
      constexpr int r0 = (3 * t) / 2, r1 = (3 * (t + 1)) / 2;
      bf16x16 rb[2];
#pragma unroll
      for (int r = r0; r < r1; ++r) { ldsp pb = (r < 6 ? rw0 : rw1) + (r % 6) * 2048; rb[r - r0] = b16(lds4(pb), lds4(pb + 1024)); }
#pragma unroll
      for (int u = 0; u < 6; ++u) acc[u] = smf(A[t + 2], w[t][u], acc[u], idx);
#pragma unroll
      for (int r = r0; r < r1; ++r) acc[r % 6] = smf(A[r / 6], rb[r - r0], acc[r % 6], idx);
    });
#pragma unroll
    for (int t = 0; t < 6; ++t) {
      unsigned d = cvtpk(acc[t][0] + acc[t][1], acc[t][2] + acc[t][3]);
      *(LAS u16*)(iw0 + t * 512) = (u16)d;
      *(LAS u16*)(iw1 + t * 512) = (u16)(d >> 16);
    }
    bar();
    if (wv < 3) {
      f32x4 s0 = zero4(), s1 = zero4(), s2 = zero4();
#pragma unroll
      for (int t = 0; t < 3; ++t) {
        u32x4 q0 = lds4(qb + t * 2048), q1 = lds4(qb + t * 2048 + 1024);
        s0 = smf(lds4(kb + t * 2048), b16(q0, q1), s0, idx);
        s1 = mf16(lds4(kx0 + t * 2048), q0, s1);
        s2 = mf16(lds4(kx1 + t * 2048), q0, s2);
        s1 = mf16(lds4(kx0 + t * 2048 + 1024), q1, s1);
        s2 = mf16(lds4(kx1 + t * 2048 + 1024), q1, s2);
      }
      const float cs = 0.07216878364870322f * 1.4426950408889634f, NEG = -1e9f * 1.4426950408889634f;
      float v[10];
#pragma unroll
      for (int r = 0; r < 4; ++r) { v[r] = r <= qlim ? s1[r] * cs : NEG; v[4 + r] = 4 + r <= qlim ? s2[r] * cs : NEG; }
      v[8] = (s0[0] + s0[1]) * cs; v[9] = (s0[2] + s0[3]) * cs;
      float mx = v[0];
#pragma unroll
      for (int r = 1; r < 10; ++r) mx = fmaxf(mx, v[r]);
      { auto t2 = __builtin_amdgcn_permlane16_swap(__float_as_uint(mx), __float_as_uint(mx), false, false); mx = fmaxf(__uint_as_float(t2[0]), __uint_as_float(t2[1])); }
      { auto t2 = __builtin_amdgcn_permlane32_swap(__float_as_uint(mx), __float_as_uint(mx), false, false); mx = fmaxf(__uint_as_float(t2[0]), __uint_as_float(t2[1])); }
      float sm = 0.f;
#pragma unroll
      for (int r = 0; r < 10; ++r) { v[r] = __builtin_amdgcn_exp2f(v[r] - mx); sm += v[r]; }
      { auto t2 = __builtin_amdgcn_permlane16_swap(__float_as_uint(sm), __float_as_uint(sm), false, false); sm = __uint_as_float(t2[0]) + __uint_as_float(t2[1]); }
      { auto t2 = __builtin_amdgcn_permlane32_swap(__float_as_uint(sm), __float_as_uint(sm), false, false); sm = __uint_as_float(t2[0]) + __uint_as_float(t2[1]); }
      const float rc = __builtin_amdgcn_rcpf(sm);
      u32x4 o = {canon(cvtpk(v[0] * rc, v[1] * rc)), canon(cvtpk(v[2] * rc, v[3] * rc)), canon(cvtpk(v[4] * rc, v[5] * rc)), canon(cvtpk(v[6] * rc, v[7] * rc))};
      st4(o, rs, pxo, slot * pstr);
      st1(canon(cvtpk(v[8] * rc, v[9] * rc)), rs, pmo, slot * pstr);
    } else if (s >= 2) {
      const int rsl = (s - 2) & 3;
      const u32x4 sv = {SENT, SENT, SENT, SENT};
      st4(sv, rs, ra0, rsl * PM_B);
      st4(sv, rs, sgo, OFF_PX + h * 1280 + rsl * PX_B);
      st4(sv, rs, sgo, OFF_PX + h * 1280 + 40960 + rsl * PX_B);
      st4(sv, rs, ra2, rsl * PX_B);
    }
    STAMP(s, 1);
    if (s + 1 < NSEG) {
      wait_flag(rs, fo, (unsigned)s + 2, seen, dead);
      memfence();
      u32x4 sg[6];
#pragma unroll
      for (int i = 0; i < 6; ++i) sg[i] = ld4(rs, sgo + (i & 3) * 1024, sgs + (i >> 2) * 4096 + (s + 1) * T1_SEG);
      bar();
#pragma unroll
      for (int i = 0; i < 6; ++i) sts4(sgw + i * 1024, sg[i]);
    }
  }
}

DI void role_vo(const Args& a, const int h, ldsp lds, const int lane, const int wv) {
  rsrc_t rs = mkrs(a.ws, WS_RANGE);
  rsrc_t rv = mkrs(a.Wv, 640u * 12288u), ro = mkrs(a.Wo + (size_t)h * 192 * 640, 192u * 1280u);
  ldsp reg = lds + wv * WREG;
  const unsigned rega = rfl((unsigned)(uintptr_t)reg);
  const int g = lane >> 4, c = lane & 15;
  bf16x16 w1[8][3], w2[5][5];
  {
    unsigned vo1[3], vo2[5];
#pragma unroll
    for (int r = 0; r < 3; ++r) { unsigned L = r * 64 + lane; vo1[r] = (L / 6) * 12288 + (L % 6) * 16; }
#pragma unroll
    for (int r = 0; r < 5; ++r) { unsigned L = r * 64 + lane; vo2[r] = (L / 10) * 1280 + (L % 10) * 16; }
    const unsigned b1 = (h * 192 + wv * 48) * 2, b2 = wv * 320;
    auto issue = [&](int nn) {
      if (nn < 4 || nn == 9) {
        const int ch = nn == 9 ? 0 : nn + 1;
#pragma unroll
        for (int n = 0; n < 12; ++n) dma16(rv, vo1[n % 3], b1 + (ch * 128 + (n / 3) * 32) * 12288, rega + (nn % 3) * 12288 + n * 1024);
      } else {
        const int ch = nn == 10 ? 0 : nn - 3;
#pragma unroll
        for (int n = 0; n < 10; ++n) dma16(ro, vo2[n % 5], b2 + (ch & 1) * 160 + ((ch >> 1) * 64 + (n / 5) * 32) * 1280, rega + (nn % 3) * 12288 + n * 1024);
      }
    };
    issue(0); issue(1); issue(2);
    const int q = c >> 2, p = c & 3, rl = 16 * (g >> 1) + 4 * (g & 1) + 8 * (q >> 1) + (q & 1);
    ldsp tb1 = reg + rl * 96 + p * 8, tb2 = reg + rl * 160 + p * 8;
    sfor<11>([&](auto NN) {
      constexpr int nn = NN;
      constexpr auto cnt = [](int k) { return k > 10 ? 0 : (k < 4 || k == 9) ? 12 : 10; };
      wait_vm<cnt(nn + 1) + cnt(nn + 2)>();
      constexpr int sl = (nn % 3) * 12288;
      if constexpr (nn < 4) {
#pragma unroll
        for (int kk = 0; kk < 2; ++kk)
#pragma unroll
          for (int t = 0; t < 3; ++t) { w1[2 * nn + kk][t] = rd_sp<96>(tb1 + sl + kk * 64 * 96 + t * 32); pina(w1[2 * nn + kk][t]); }
        wait_lgkm();
        issue(nn + 3);
      } else if constexpr (nn < 9) {
#pragma unroll
        for (int t = 0; t < 5; ++t) { w2[nn - 4][t] = rd_sp<160>(tb2 + sl + t * 32); if ((nn - 4) * 5 + t < 8) pina(w2[nn - 4][t]); }
        wait_lgkm();
        if constexpr (nn + 3 <= 10) issue(nn + 3);
      } else {
        constexpr int NT = nn == 9 ? 6 : 5;
        bf16x16 tmp[NT];
#pragma unroll
        for (int t = 0; t < NT; ++t) tmp[t] = nn == 9 ? rd_sp<96>(tb1 + sl + (t / 3) * 64 * 96 + (t % 3) * 32) : rd_sp<160>(tb2 + sl + t * 32);
        wait_lgkm();
#pragma unroll
        for (int t = 0; t < NT; ++t) {
          u32x8 v = __builtin_bit_cast(u32x8, tmp[t]);
          sts4(reg + sl + t * 2048 + lane * 16, __builtin_shufflevector(v, v, 0, 1, 2, 3));
          sts4(reg + sl + t * 2048 + 1024 + lane * 16, __builtin_shufflevector(v, v, 4, 5, 6, 7));
        }
      }
    });
  }
  bool dead = false;
  const int idx = (lane & 1) ? 0xEEEE : 0x4444;
  const unsigned mo = (g >> 1) * 512 + (c >> 3) * 256 + ((c >> 1) & 3) * 64 + (g & 1) * 32 + (c & 1) * 16;
  ldsp rw1 = reg + lane * 16, rw2 = reg + 12288 + lane * 16;
  ldsp vimg = lds + OFF_IMG;
  ldsp iw = vimg + wv * 768 + (4 * g + (c >> 3)) * 16 + (c & 7) * 2;
  unsigned yo[3];
#pragma unroll
  for (int k = 0; k < 3; ++k) { int T = 10 * wv + 4 * k + g; yo[k] = (k < 2 || g < 2) ? OFF_Y + (T >> 1) * 16384 + h * 512 + (T & 1) * 256 + c * 16 : OOB; }
  for (int s = 0; s < NSEG; ++s) {
    const int slot = s & 3;
    u32x4 A[10];
    poll4<10>(A, [&](int i) { return ld4(rs, mo + (i & 3) * 1024, OFF_MEM + (i >> 2) * 4096 + slot * MEM_B); }, dead);
    STAMP(s, 0);
    f32x4 acc[3];
#pragma unroll
    for (int t = 0; t < 3; ++t) acc[t] = zero4();
    sfor<8>([&](auto T) {
      constexpr int t = T;
      bf16x16 rb;
      if constexpr (t < 6) rb = b16(lds4(rw1 + t * 2048), lds4(rw1 + t * 2048 + 1024));
#pragma unroll
      for (int u = 0; u < 3; ++u) acc[u] = smf(A[t + 2], w1[t][u], acc[u], idx);
      if constexpr (t < 6) acc[t % 3] = smf(A[t / 3], rb, acc[t % 3], idx);
    });
    ldsp iwp = iw + (s & 1) * 3072;
#pragma unroll
    for (int t = 0; t < 3; ++t) {
      unsigned d = cvtpk(acc[t][0] + acc[t][1], acc[t][2] + acc[t][3]);
      *(LAS u16*)(iwp + t * 256) = (u16)d;
      *(LAS u16*)(iwp + t * 256 + 32) = (u16)(d >> 16);
    }
    bar();
    u32x4 V[3];
#pragma unroll
    for (int t = 0; t < 3; ++t) V[t] = lds4(vimg + (s & 1) * 3072 + t * 1024 + lane * 16);
    f32x4 y[10];
#pragma unroll
    for (int t = 0; t < 10; ++t) y[t] = zero4();
    sfor<5>([&](auto CH) {
      constexpr int ch = CH + 1;
      bf16x16 rb = b16(lds4(rw2 + CH * 2048), lds4(rw2 + CH * 2048 + 1024));
#pragma unroll
      for (int u = 0; u < 5; ++u) y[(ch & 1) * 5 + u] = smf(V[ch >> 1], w2[CH][u], y[(ch & 1) * 5 + u], idx);
      y[CH] = smf(V[0], rb, y[CH], idx);
    });
    unsigned d[12];
#pragma unroll
    for (int t = 0; t < 10; ++t) d[t] = canon(cvtpk(y[t][0] + y[t][1], y[t][2] + y[t][3]));
    d[10] = d[8]; d[11] = d[9];
#pragma unroll
    for (int k = 0; k < 3; ++k) {
      auto p02 = __builtin_amdgcn_permlane32_swap(d[4 * k], d[4 * k + 2], false, false);
      auto p13 = __builtin_amdgcn_permlane32_swap(d[4 * k + 1], d[4 * k + 3], false, false);
      auto q01 = __builtin_amdgcn_permlane16_swap(p02[0], p13[0], false, false);
      auto q23 = __builtin_amdgcn_permlane16_swap(p02[1], p13[1], false, false);
      st4((u32x4){q01[0], q01[1], q23[0], q23[1]}, rs, yo[k], slot * Y_B);
    }
    STAMP(s, 1);
    if (s >= 2) {
      const u32x4 sv = {SENT, SENT, SENT, SENT};
#pragma unroll
      for (int k = 0; k < 3; ++k) st4(sv, rs, yo[k], ((s - 2) & 3) * Y_B);
    }
  }
}

DI void role_red(const Args& a, const int r, ldsp lds, const int lane, const int wv) {
  rsrc_t rs = mkrs(a.ws, WS_RANGE);
  const int cg = r >> 1, rh = r & 1, g = lane >> 4, c = lane & 15, tid = threadIdx.x;
  bool dead = false;
  unsigned seen = 0;
  const unsigned fo = OFF_FLAG2 + (lane < 8 ? 8 * wv + lane : 32 + 2 * wv + (lane & 1)) * 4;
  const int idx = (lane & 1) ? 0xEEEE : 0x4444;
  const unsigned uo = lane * 16, us = rfl(OFF_U + (cg * 40 + 8 * wv) * 2048);
  const unsigned mlo = OFF_U + (cg * 40 + 32 + 2 * wv + (g >> 1)) * 2048 + (2 * rh + (g & 1)) * 256 + c * 16;
  const unsigned yo = g * 512 + c * 16, ys = rfl(OFF_Y + cg * 16384 + 8 * wv * 512);
  const unsigned pxo = c < 8 ? OFF_PM + rh * 12288 + (8 * wv + (g >> 1)) * 384 + (g & 1) * 128 + c * 16 : OOB;
  const unsigned pmo = c < 8 ? OFF_PM + rh * 12288 + (8 * wv + 2 * g) * 384 + 256 + c * 8 : OOB;
  const unsigned txs = rfl(OFF_PX + (rh * 32 + 8 * wv) * 1280);
  const unsigned tmo = OFF_PX + (rh * 32 + 8 * wv + g) * 1280 + 1024 + c * 16;
  u32x4 ident;
#pragma unroll
  for (int j = 0; j < 4; ++j) ident[j] = (8 * (g & 1) + 2 * j == c ? 0x3F80u : 0u) | (8 * (g & 1) + 2 * j + 1 == c ? 0x3F800000u : 0u);
  LAS float* red = (LAS float*)lds;
  LAS float* red2 = (LAS float*)(lds + 4096);
  const float bb0 = bf2f(a.b2[cg * 32 + 2 * (tid & 15)]), bb1 = bf2f(a.b2[cg * 32 + 2 * (tid & 15) + 1]);
  const unsigned oe = (16 * rh + (tid >> 4)) * 640 + cg * 32 + 2 * (tid & 15);
  u32x4 U[16], Un[16];
  wait_flag(rs, fo, 1, seen, dead);
  memfence();
#pragma unroll
  for (int i = 0; i < 16; ++i) U[i] = ld4(rs, uo + (i & 3) * 1024, us + (i >> 2) * 4096);
  for (int s = 0; s < NSEG; ++s) {
    const int slot = s & 3;
    u32x4 Y[4];
    poll4<4>(Y, [&](int i) { return ld4(rs, yo + (i >> 1) * 2048 + (i & 1) * 256, ys + slot * Y_B); }, dead);
    if (s + 1 < NSEG) {
      u32x4 P[5];
      poll4<5>(P, [&](int i) {
        if (i < 4) return ld4(rs, pxo, i * 768 + slot * PM_B);
        u32x2 lo = ld2(rs, pmo, slot * PM_B), hi = ld2(rs, pmo, 384 + slot * PM_B);
        return (u32x4){lo.x, lo.y, hi.x, hi.y};
      }, dead);
      STAMP(s, 0);
      f32x4 m0 = zero4(), m1 = zero4();
#pragma unroll
      for (int p = 0; p < 4; ++p) {
        m0 = smf(P[p], b16(U[4 * p], U[4 * p + 2]), m0, idx);
        m1 = smf(P[p], b16(U[4 * p + 1], U[4 * p + 3]), m1, idx);
      }
      m0 = smf(P[4], b16(Y[0], Y[2]), m0, idx);
      m1 = smf(P[4], b16(Y[1], Y[3]), m1, idx);
      if (g < 2) {
        LAS float* rp = red + wv * 128 + g * 64 + c;
        rp[0] = m0[0] + m0[1]; rp[32] = m0[2] + m0[3]; rp[16] = m1[0] + m1[1]; rp[48] = m1[2] + m1[3];
      }
    }
    bar();
    if (s + 1 < NSEG) {
      if (wv == 0) {
        LAS f32x2* rp = (LAS f32x2*)red + lane;
        f32x2 v = (rp[0] + rp[64]) + (rp[128] + rp[192]);
        unsigned d = canon(cvtpk(v.x, v.y));
        st1(d, rs, OFF_MEM + cg * 512 + rh * 256 + lane * 4, ((s + 1) & 3) * MEM_B);
      } else if (wv == 1) {
        st1(SENT, rs, OFF_MEM + cg * 512 + rh * 256 + lane * 4, slot * MEM_B);
      }
      STAMP(s, 1);
      wait_flag(rs, fo, (unsigned)s + 2, seen, dead);
      memfence();
#pragma unroll
      for (int i = 0; i < 16; ++i) Un[i] = ld4(rs, uo + (i & 3) * 1024, us + (i >> 2) * 4096 + (s + 1) * U_SEG);
    }
    u32x4 ml0 = ld4(rs, mlo, s * U_SEG), ml1 = ld4(rs, mlo, 1024 + s * U_SEG);
    const unsigned xv = *(const unsigned*)(a.x + (size_t)s * 32 * 640 + oe);
    u32x4 T[10];
    poll4<10>(T, [&](int i) { return i < 8 ? ld4(rs, uo, txs + i * 1280 + slot * PX_B) : ld4(rs, tmo, (i - 8) * 5120 + slot * PX_B); }, dead);
    f32x4 o0 = mf16(ident, ml0, (f32x4){0.f, 0.f, 0.f, 0.f}), o1 = mf16(ident, ml1, (f32x4){0.f, 0.f, 0.f, 0.f});
#pragma unroll
    for (int k = 0; k < 8; ++k) { o0 = mf16(T[k], U[2 * k], o0); o1 = mf16(T[k], U[2 * k + 1], o1); }
#pragma unroll
    for (int u = 0; u < 2; ++u) { o0 = mf16(T[8 + u], Y[2 * u], o0); o1 = mf16(T[8 + u], Y[2 * u + 1], o1); }
    {
      LAS float* rp = red2 + wv * 528 + 4 * g * 33 + c;
#pragma unroll
      for (int j = 0; j < 4; ++j) { rp[j * 33] = o0[j]; rp[j * 33 + 16] = o1[j]; }
    }
    bar();
    {
      LAS float* rp = red2 + (tid >> 4) * 33 + 2 * (tid & 15);
      float v0 = (rp[0] + rp[528]) + (rp[1056] + rp[1584]), v1 = (rp[1] + rp[529]) + (rp[1057] + rp[1585]);
      v0 += bb0 + __uint_as_float(xv << 16); v1 += bb1 + __uint_as_float(xv & 0xFFFF0000u);
      *(unsigned*)(a.out + (size_t)s * 32 * 640 + oe) = cvtpk(v0, v1);
    }
    STAMP(s, 2);
#pragma unroll
    for (int i = 0; i < 16; ++i) U[i] = Un[i];
  }
}

__global__ void __launch_bounds__(256, 1) k_main(Args a) {
  extern __shared__ __attribute__((aligned(16))) unsigned char smem[];
  ldsp lds = (ldsp)smem;
  const int lane = threadIdx.x & 63, wv = rfl(threadIdx.x >> 6);
  const int b = blockIdx.x;
  if (b < 32) role_qk(a, b, lds, lane, wv);
  else if (b < 64) role_vo(a, b - 32, lds, lane, wv);
  else if (b < 104) role_red(a, b - 64, lds, lane, wv);
  else if (b < 208) role_t1(a, b - 104, lds, lane, wv);
  else if (b < 248) role_t2(a, b - 208, lds, lane, wv);
}

extern "C" void kernel_launch(void* const* d_in, const int* in_sizes, int n_in, void* d_out, int out_size, void* d_ws, size_t ws_size, hipStream_t stream) {
  Args a;
  a.x = (const u16*)d_in[0]; a.mem0 = (const u16*)d_in[1];
  a.Wq = (const u16*)d_in[2]; a.Wk = (const u16*)d_in[3]; a.Wv = (const u16*)d_in[4]; a.Wo = (const u16*)d_in[5];
  a.w1 = (const u16*)d_in[8]; a.b1 = (const u16*)d_in[9]; a.w2 = (const u16*)d_in[10]; a.b2 = (const u16*)d_in[11];
  a.out = (u16*)d_out; a.ws = (unsigned char*)d_ws;
  (void)hipFuncSetAttribute((const void*)k_main, hipFuncAttributeMaxDynamicSharedMemorySize, LDS_B);
  hipLaunchKernelGGL(k_pre, dim3(512), dim3(256), 0, stream, a);
  hipLaunchKernelGGL(k_main, dim3(256), dim3(256), LDS_B, stream, a);
}
```

```cpp
#define DX 0
#define PF 0
#define PG 1
#define PH 2
#define MW 900
#define MW2 1300
#define MK 0
#define CP 0
#define PA 44
#define LP2 0
#define SWS 32
#define L2E 1
#define DIAG 0
#define ES 8
#define MQ 0
#define MAP 0
#define STMP 0
#define SW 54
#define XU 0
#define TX 0
#define TO 16
#define PW 1
#define LW 700
#define RW 400
#define XNT 2
#define HSL 16
#define QF 0
#define LP 0
#define RBN 2
#define CTH 90
#define FALLB 0
#define NA 0
#define NB 32
#define UF 0
#define UR 16
#define TK0 16
#define TK1 0
#define PC 0
#define QREC0 134348872u
#define PREF 0
#define RPF 0
#define RPA 0
#include <hip/hip_runtime.h>
#include <stdint.h>
#include <utility>

typedef __bf16 bf16x2 __attribute__((ext_vector_type(2)));
typedef __bf16 bf16x8 __attribute__((ext_vector_type(8)));
typedef __bf16 bf16x16 __attribute__((ext_vector_type(16)));
typedef float f32x2 __attribute__((ext_vector_type(2)));
typedef float f32x4 __attribute__((ext_vector_type(4)));
typedef float f32x16 __attribute__((ext_vector_type(16)));
typedef unsigned u32x2 __attribute__((ext_vector_type(2)));
typedef unsigned u32x4 __attribute__((ext_vector_type(4)));
typedef unsigned u32x8 __attribute__((ext_vector_type(8)));
typedef unsigned u32x16 __attribute__((ext_vector_type(16)));
typedef short s16x4 __attribute__((ext_vector_type(4)));
typedef unsigned short u16;
typedef unsigned short u16x2 __attribute__((ext_vector_type(2)));
typedef __amdgpu_buffer_rsrc_t rsrc_t;
#define DI __device__ __forceinline__
#define LAS __attribute__((address_space(3)))
#define SB() __builtin_amdgcn_sched_barrier(0)
typedef LAS unsigned char* ldsp;

constexpr int NSEG = 64;
constexpr unsigned SENT = 0xFFFFFFFFu;
constexpr unsigned OOB = 0x7FFFFFF0u;
constexpr unsigned SPINCAP = 1u << 16;
constexpr int SC1 = 16;
constexpr unsigned MiB = 1u << 20;
constexpr unsigned BLK = 4 * MiB, UN = 16384;
constexpr unsigned OFF_ARR = 3 * BLK - UN, OFF_CALL = OFF_ARR + 3968, OFF_LN = OFF_ARR + 4096, OFF_CAL = OFF_ARR + 12288;
constexpr unsigned OFF_DBG = 256 * MiB;
constexpr unsigned U_Q = 4 * UN + 2048, U_PMM = 6 * UN, U_PMX = 8 * UN, U_QM = 16 * UN, U_RM = 17 * UN;
constexpr unsigned WS_RANGE = 256u << 20;
constexpr int LDS_B = 163840, WREG = 36864, OFF_IMG = 4 * WREG;

struct Args {
  const u16 *x, *mem0, *Wq, *Wk, *Wv, *Wo, *w1, *b1, *w2, *b2;
  u16* out;
  unsigned char* ws;
};

template <int... Is, class F> DI void sfor_(std::integer_sequence<int, Is...>, F&& f) { (f(std::integral_constant<int, Is>{}), ...); }
template <int N, class F> DI void sfor(F&& f) { sfor_(std::make_integer_sequence<int, N>{}, f); }

DI unsigned rfl(unsigned x) { return __builtin_amdgcn_readfirstlane(x); }
DI float bf2f(u16 b) { return __uint_as_float((unsigned)b << 16); }
DI unsigned cvtpk(float lo, float hi) { f32x2 v = {lo, hi}; bf16x2 b = __builtin_convertvector(v, bf16x2); return __builtin_bit_cast(unsigned, b); }
DI float rbf(float f) { return __uint_as_float(cvtpk(f, 0.f) << 16); }
DI unsigned canon(unsigned x) { return x < 0xFFFFFFFEu ? x : 0xFFFFFFFEu; }
DI rsrc_t mkrs(const void* p, unsigned range) { return __builtin_amdgcn_make_buffer_rsrc((void*)p, 0, range, 0x00020000); }
DI u32x4 ld4(rsrc_t r, unsigned vo, unsigned so) { return __builtin_amdgcn_raw_buffer_load_b128(r, vo, so, SC1); }
DI unsigned ld1(rsrc_t r, unsigned vo, unsigned so) { return __builtin_amdgcn_raw_buffer_load_b32(r, vo, so, SC1); }
DI u32x4 ld4p(rsrc_t r, unsigned vo, unsigned so) { return __builtin_amdgcn_raw_buffer_load_b128(r, vo, so, 0); }
DI void st4(u32x4 v, rsrc_t r, unsigned vo, unsigned so) { __builtin_amdgcn_raw_buffer_store_b128(v, r, vo + so, 0, SC1); }
DI void st2(u32x2 v, rsrc_t r, unsigned vo, unsigned so) { __builtin_amdgcn_raw_buffer_store_b64(v, r, vo, so, SC1); }
DI void st1(unsigned v, rsrc_t r, unsigned vo, unsigned so) { __builtin_amdgcn_raw_buffer_store_b32(v, r, vo, so, SC1); }
template <class T> DI void pinv(T& v) { asm volatile("" : "+v"(v)); }
template <class T> DI void pina(T& v) { asm volatile("" : "+a"(v)); }
DI f32x4 zero4() { f32x2 a, b; asm volatile("v_mov_b64 %0, 0\n\tv_mov_b64 %1, 0" : "=v"(a), "=v"(b)); f32x4 z = __builtin_shufflevector(a, b, 0, 1, 2, 3); pinv(z); return z; }
DI f32x2 fold(f32x4 v) { return __builtin_shufflevector(v, v, 0, 1) + __builtin_shufflevector(v, v, 2, 3); }
DI int opq(int x) { asm volatile("" : "+v"(x)); return x; }
DI void memfence() { asm volatile("" ::: "memory"); }
template <int N> DI void wait_vm() { asm volatile("s_waitcnt vmcnt(%0)" ::"i"(N) : "memory"); }
DI void wait_lgkm() { asm volatile("s_waitcnt lgkmcnt(0)" ::: "memory"); }
DI void bar() { asm volatile("s_waitcnt lgkmcnt(0)\n\ts_barrier" ::: "memory"); }
DI void dma16(rsrc_t r, unsigned vo, unsigned so, unsigned ldsa) {
  unsigned keep;
  asm volatile("s_mov_b32 %0, m0\n\ts_mov_b32 m0, %1\n\ts_nop 0\n\tbuffer_load_dwordx4 %2, %3, %4 offen lds\n\ts_mov_b32 m0, %0" : "=&s"(keep) : "s"(ldsa), "v"(vo), "s"(r), "s"(so) : "memory");
}
DI u32x2 trd(ldsp p) { return __builtin_bit_cast(u32x2, __builtin_amdgcn_ds_read_tr16_b64_v4i16((LAS s16x4*)p)); }
DI u32x4 lds4(ldsp p) { return *(LAS u32x4*)p; }
DI void sts4(ldsp p, u32x4 v) { *(LAS u32x4*)p = v; }
DI void sts1(ldsp p, unsigned v) { *(LAS unsigned*)p = v; }
DI unsigned umax4(u32x4 v) { return max(max(v.x, v.y), max(v.z, v.w)); }
DI bf16x8 b8(u32x4 v) { return __builtin_bit_cast(bf16x8, v); }
DI bf16x16 b16(u32x4 a, u32x4 b) { return __builtin_bit_cast(bf16x16, __builtin_shufflevector(a, b, 0, 1, 2, 3, 4, 5, 6, 7)); }
DI u32x4 lo4(bf16x16 v) { u32x8 w = __builtin_bit_cast(u32x8, v); return __builtin_shufflevector(w, w, 0, 1, 2, 3); }
DI u32x4 hi4(bf16x16 v) { u32x8 w = __builtin_bit_cast(u32x8, v); return __builtin_shufflevector(w, w, 4, 5, 6, 7); }
DI bf16x16 lds8(ldsp p, int d) { return b16(lds4(p), lds4(p + d)); }
DI f32x4 smf(u32x4 a, bf16x16 b, f32x4 c, int idx) { return __builtin_amdgcn_smfmac_f32_16x16x64_bf16(b8(a), b, c, idx, 0, 0); }
DI f32x4 mf16(u32x4 a, u32x4 b, f32x4 c) { return __builtin_amdgcn_mfma_f32_16x16x32_bf16(b8(a), b8(b), c, 0, 0, 0); }
DI void mf16v(f32x4& c, u32x4 a, u32x4 b) { asm volatile("s_nop 1\n\tv_mfma_f32_16x16x32_bf16 %0, %1, %2, %0" : "+v"(c) : "v"(a), "v"(b)); }
DI void mfdone(f32x4& a, f32x4& b, f32x4& c) { asm volatile("s_nop 7\n\ts_nop 1" : "+v"(a), "+v"(b), "+v"(c)); }
DI u32x4 sent4() { u32x2 a, b; asm volatile("v_mov_b64 %0, -1\n\tv_mov_b64 %1, -1" : "=v"(a), "=v"(b)); return __builtin_shufflevector(a, b, 0, 1, 2, 3); }
DI f32x16 mf32(u32x4 a, u32x4 b, f32x16 c) { return __builtin_amdgcn_mfma_f32_32x32x16_bf16(b8(a), b8(b), c, 0, 0, 0); }
template <int RB, int R1, int R2> DI bf16x16 rd_sp(ldsp p) {
  u32x2 a = trd(p), b = trd(p + R1 * RB), c = trd(p + R2 * RB), d = trd(p + (R1 + R2) * RB);
  u32x8 r = {a.x, a.y, b.x, b.y, c.x, c.y, d.x, d.y};
  return __builtin_bit_cast(bf16x16, r);
}
template <int D> DI u32x4 rd_dn(ldsp p) { u32x2 a = trd(p), b = trd(p + D); return (u32x4){a.x, a.y, b.x, b.y}; }
template <int C> DI float dppf(float x) { return __uint_as_float(__builtin_amdgcn_update_dpp(0u, __float_as_uint(x), C, 0xF, 0xF, true)); }
template <int C> DI unsigned dppu(unsigned x) { return (unsigned)__builtin_amdgcn_update_dpp(0u, x, C, 0xF, 0xF, true); }
DI unsigned rtl() { unsigned long long t; asm volatile("s_memrealtime %0\n\ts_waitcnt lgkmcnt(0)" : "=s"(t)::"memory"); return (unsigned)t; }
DI unsigned ctm() { unsigned long long t; asm volatile("s_memtime %0\n\ts_waitcnt lgkmcnt(0)" : "=s"(t)::"memory"); return (unsigned)t; }
DI unsigned wait_t(unsigned t) {
  unsigned long long now;
  unsigned d;
  asm volatile("s_memrealtime %0\n\ts_waitcnt lgkmcnt(0)\n\ts_sub_u32 %1, %2, s98\n\ts_cmp_gt_u32 %1, 0xfa0\n\ts_cbranch_scc1 2f\n"
               "1:\n\ts_memrealtime %0\n\ts_waitcnt lgkmcnt(0)\n\ts_sub_u32 %1, %2, s98\n\ts_cmp_gt_i32 %1, 0\n\ts_cbranch_scc1 1b\n2:"
               : "=&{s[98:99]}"(now), "=&s"(d) : "s"(t) : "memory", "scc");
  return (unsigned)now;
}
template <class T> DI void pins(T& v) { asm volatile("" : "+s"(v)); }
DI float rowmax(float v) {
  asm volatile("s_nop 1\n\tv_max_f32_dpp %0, %0, %0 row_ror:8 row_mask:0xf bank_mask:0xf\n\ts_nop 1\n\tv_max_f32_dpp %0, %0, %0 row_ror:4 row_mask:0xf bank_mask:0xf\n\t"
               "s_nop 1\n\tv_max_f32_dpp %0, %0, %0 row_ror:2 row_mask:0xf bank_mask:0xf\n\ts_nop 1\n\tv_max_f32_dpp %0, %0, %0 row_ror:1 row_mask:0xf bank_mask:0xf" : "+v"(v));
  return v;
}
DI unsigned xcc_id() { return (unsigned)__builtin_amdgcn_s_getreg(20 | (3 << 11)) & 7u; }
DI unsigned qb(unsigned x) { return (x & 1) << 14 | (x >> 1) << 12; }
DI unsigned fa(unsigned c, unsigned u) { return u * UN + (((c ^ (u >> 7)) & 1u) << 13); }
DI unsigned na(unsigned c, unsigned u) { return fa(c, u) ^ 8192u; }
DI unsigned t1p(unsigned c, unsigned i, unsigned o) {
  const unsigned k = i >> 5, x = 3 * (i & 31) + (o >> 12);
  return (k == 2 ? fa(c, (x < 54 ? 100 : 201) + (x >> 1)) : ((k == 3 ? 96 : 48 * k) + (x >> 1)) * UN) + (x & 1) * 4096 + (o & 4095);
}
DI unsigned flag1(unsigned c, unsigned i) { return na(c, 255) ^ (i * 128); }
DI unsigned flag2(unsigned c, unsigned i) { return BLK + na(c, 255) + i * 128; }
DI unsigned progo(unsigned c) { return BLK + fa(c, 255); }
DI unsigned uoff(unsigned c, unsigned cg, unsigned i) { return fa(c, (cg / 10) * 28 + cg * 10 + (i >> 2)) + (i & 3) * 2048; }
DI unsigned unoff(unsigned c, unsigned cg, unsigned i) { return NB * BLK + na(c, (cg / 10) * 48 + cg * 8 + (i >> 2)) + (i & 3) * 2048; }
DI unsigned nb(unsigned c, unsigned s) { return NA * BLK + na(c, s * 128); }
DI unsigned pxoff(unsigned hf, unsigned h) { return (108 + 4 * hf + (h >> 3)) * UN + (h & 7) * 1024; }
DI unsigned pmoff(unsigned hf, unsigned h) { return (116 + hf) * UN + h * 256; }
DI unsigned min16(unsigned v) {
  v = min(v, dppu<0x128>(v)); v = min(v, dppu<0x124>(v)); v = min(v, dppu<0x122>(v)); v = min(v, dppu<0x121>(v));
  return rfl(v);
}
DI void wait_flag(rsrc_t rs, unsigned vo, unsigned need, unsigned& seen, bool& dead) {
  if (seen >= need) return;
#pragma nounroll
  for (unsigned sp = 0; !dead; ++sp) {
    memfence();
    seen = min16(ld1(rs, vo, 0));
    if (seen >= need) return;
    if (sp > SPINCAP) dead = true;
    __builtin_amdgcn_s_sleep(4);
  }
}
template <int N, class F> DI void poll4(u32x4 (&v)[N], F&& ld, bool& dead) {
  for (unsigned sp = 0;; ++sp) {
    memfence();
    unsigned m = 0;
#pragma unroll
    for (int i = 0; i < N; ++i) v[i] = ld(i);
#pragma unroll
    for (int i = 0; i < N; ++i) m = max(m, umax4(v[i]));
    if (!__any(m == SENT) || dead) return;
    if (sp > SPINCAP) dead = true;
  }
}
#define HASCAL
#define TS(k)
#define TSF(s, a0, a1)

typedef const __attribute__((address_space(4))) Args KArgs;
DI KArgs& reload(KArgs* ap) {
  asm volatile("" : "+s"(ap));
  return *ap;
}
DI unsigned calltag(rsrc_t rs) { return rfl(ld1(rs, OFF_CALL, 0)) + 0x9e377900u; }
DI unsigned hs_wait(rsrc_t rs, unsigned reg, unsigned exp, int lane) {
#pragma nounroll
  for (unsigned sp = 0;; ++sp) {
    memfence();
    const u32x4 w = ld4(rs, lane * 16, reg) ^ exp;
    const unsigned o = w.x | w.y | w.z | w.w;
    if (!__any(o & ~248u) || sp > SPINCAP) {
      unsigned m = 0;
#pragma unroll
      for (unsigned b = 32; b < 256; b *= 2) {
        unsigned n = 0;
#pragma unroll
        for (int i = 0; i < 4; ++i) n += __builtin_popcountll(__builtin_amdgcn_ballot_w64((w[i] & b) != 0u));
        m |= n >= 20 ? b << 7 : 0u;
      }
      return m ^ qb(QF);
    }
    if (!__any(o > 255u)) return 1;
    __builtin_amdgcn_s_sleep(HSL);
  }
}
DI float sum8(float v) { v += dppf<0xB1>(v); v += dppf<0x4E>(v); return v + dppf<0x141>(v); }
DI f32x2 unpk(unsigned u) { return (f32x2){__uint_as_float(u << 16), __uint_as_float(u & 0xFFFF0000u)}; }
DI void ln8(u32x4 (&y)[10], const u32x4 (&x)[10]) {
  f32x2 s = {0.f, 0.f}, q = {0.f, 0.f};
#pragma unroll
  for (int i = 0; i < 40; ++i) s += unpk(x[i >> 2][i & 3]);
  const float mean = sum8(s.x + s.y) * (1.f / 640.f);
#pragma unroll
  for (int i = 0; i < 40; ++i) { const f32x2 d = unpk(x[i >> 2][i & 3]) - mean; q += d * d; }
  const float mb = rbf(mean), vb = rbf(sum8(q.x + q.y) * (1.f / 640.f));
  const float inv = 1.f / rbf(sqrtf(rbf(vb + 1e-5f)));
#pragma unroll
  for (int i = 0; i < 40; ++i) {
    const f32x2 d = unpk(x[i >> 2][i & 3]) - mb, e = unpk(cvtpk(d.x, d.y)) * inv;
    y[i >> 2][i & 3] = cvtpk(e.x, e.y);
  }
#pragma unroll
  for (int i = 0; i < 10; ++i) pinv(y[i]);
}
template <int AUX> DI void xpre(u32x4 (&x)[10], rsrc_t ro, unsigned b, int lane) {
#pragma unroll
  for (int k = 0; k < 10 * PW; ++k) __builtin_amdgcn_raw_buffer_store_b128(x[k], ro, (b * 8 + (lane >> 3)) * 1280 + (lane & 7) * 16 + k * 128, 0, AUX);
}
DI void xin(u32x4 (&x)[10], rsrc_t rx, unsigned b, int lane) {
  memfence(); SB();
#pragma unroll
  for (int k = 0; k < 10; ++k) x[k] = __builtin_amdgcn_raw_buffer_load_b128(rx, (b * 8 + (lane >> 3)) * 1280 + (lane & 7) * 16 + k * 128, 0, XNT);
  memfence(); SB();
}
template <int AUX> DI void xout(u32x4 (&x)[10], rsrc_t rs, ldsp t, unsigned b, int lane, unsigned side) {
#pragma unroll
  for (int k = 0; k < 10; ++k) sts4(t + k * 1152 + (lane & 7) * 144 + (lane >> 3) * 16, x[k]);
  memfence();
  const unsigned o = (b >> 2) * BLK + 249 * UN + (lane >> 3) * 512 + ((b & 3) * 8 + (lane & 7)) * 16 + side;
#pragma unroll
  for (int k = 0; k < 10; ++k) x[k] = lds4(t + k * 1152 + (lane >> 3) * 144 + (lane & 7) * 16);
  memfence();
#pragma unroll
  for (int k = 0; k < 10; ++k) __builtin_amdgcn_raw_buffer_store_b128(x[k], rs, o + ((k + (k > 4)) >> 1) * UN + ((k + (k > 4)) & 1) * 4096, 0, AUX);
  if (b >= 4 * SWS) return;
#pragma unroll
  for (int k = 0; k < 10; ++k) __builtin_amdgcn_raw_buffer_store_b128(x[k], rs, (o ^ 8192) + ((k + (k > 4)) >> 1) * UN + ((k + (k > 4)) & 1) * 4096, 0, AUX);
}
template <int N> DI void lndone(KArgs& a, rsrc_t rs, unsigned i, unsigned n, int lane, int wv) {
  wait_vm<N>();
  bar();
  st4((u32x4){n, n, n, n}, rs, lane < 8 && wv == 0 ? OFF_LN + i * 128 + lane * 16 : OOB, 0);
}
DI unsigned helper(KArgs& a, const unsigned j, const unsigned hp, ldsp lds, const int lane, const int wv) {
  rsrc_t rs = mkrs(a.ws, WS_RANGE), rx = mkrs(a.x, 2048u * 1280u), ro = mkrs(a.out, 2048u * 1280u);
  const unsigned own = lane || wv ? OOB : OFF_ARR + (hp >> 8);
  unsigned vote = 0;
  u32x4 A[10], B[10], C[10];
  const unsigned t0 = rtl();
  if (j >= 40) xin(A, rx, (j - 40) * 4 + wv, lane);
  else {
    if (wv == 0) {
      unsigned base = lane ? OOB : (NB + j / 5) * BLK + 80 * UN + U_Q + 256 + j % 5 * 256 + (j & 1) * 8192, ad[4];
      st1(0u, rs, base, 0); st1(0u, rs, base ^ 8192u, 0);
      wait_vm<1>();
      const unsigned c0 = ctm();
      wait_vm<0>();
      const unsigned far = ctm() - c0 > CTH, x = xcc_id();
      unsigned long long t[8];
#pragma unroll
      for (int i = 0; i < 4; ++i) ad[i] = base ^ (far ? 128u : 8320u) ^ qb((i ^ (j >> 1)) & 3);
      asm volatile("s_memtime %0\n\tbuffer_store_dword %8, %8, %12, 0 offen sc1\n\ts_sleep 2\n\ts_memtime %1\n\tbuffer_store_dword %9, %9, %12, 0 offen sc1\n\ts_sleep 2\n\t"
                   "s_memtime %2\n\tbuffer_store_dword %10, %10, %12, 0 offen sc1\n\ts_sleep 2\n\ts_memtime %3\n\tbuffer_store_dword %11, %11, %12, 0 offen sc1\n\ts_barrier\n\t"
                   "s_waitcnt vmcnt(3)\n\ts_memtime %4\n\ts_waitcnt vmcnt(2)\n\ts_memtime %5\n\ts_waitcnt vmcnt(1)\n\ts_memtime %6\n\ts_waitcnt vmcnt(0)\n\ts_memtime %7\n\ts_waitcnt lgkmcnt(0)"
                   : "=&s"(t[0]), "=&s"(t[1]), "=&s"(t[2]), "=&s"(t[3]), "=&s"(t[4]), "=&s"(t[5]), "=&s"(t[6]), "=&s"(t[7]) : "v"(ad[0]), "v"(ad[1]), "v"(ad[2]), "v"(ad[3]), "s"(rs) : "memory");
      const int d0 = t[4] - t[0], d1 = t[5] - t[1], d2 = t[6] - t[2], d3 = t[7] - t[3];
      vote = (((d2 + d3 < d0 + d1) ^ (j >> 2) ^ (x >> 1)) & 1u) | ((far ^ j ^ (x >> 2)) & 1u) << 1 | (((d1 + d3 < d0 + d2) ^ (j >> 1) ^ x) & 1u) << 2;
    } else asm volatile("s_barrier" ::: "memory");
  }
  const unsigned exp = (calltag(rs) & ~255u) | (hp & 255u);
  if (j < 40) {
    const unsigned z = j * 128 + (wv & 1) * 64 + lane, o = z * 16;
    if (wv < 2) {
      if (j == 0) st1(rtl(), rs, (own & ~1023u) + 2048, 0);
      const unsigned q = z < 2048 ? 0u : z < 4096 ? SENT : QREC0;
      st4((u32x4){q, q, q, q}, rs, z < 2048 ? (z >> 10) * BLK + 255 * UN + (z & 1023) * 16 : z < 4096 ? (NB * BLK + 94 * UN) + o : z < 4224 ? (NB + ((z & 127) >> 4)) * BLK + 80 * UN + U_Q + (z & 8) * 1024 + (z & 7) * 16 : OOB, 0);
    } else {
      const unsigned d = o < 65536u ? o : 65536u + (o & 14336u) * 2 + (o & 2047u), l = (d >> 15) * 4096 + (d & 4095u), x = (d >> 14 & 1) | (d >> 11 & 2);
      const unsigned mo = ((l >> 5) & 7) * 1280 + (l >> 8) * 32;
      const u32x4 v0 = ld4p(mkrs(a.mem0, 10240u), mo, 0), v1 = ld4p(mkrs(a.mem0, 10240u), mo, 16);
      u32x4 v = l & 16 ? (u32x4){v0.y, v0.w, v1.y, v1.w} : (u32x4){v0.x, v0.z, v1.x, v1.z};
      st4(sent4(), rs, d, (NB + 1) * BLK + 80 * UN); st4(sent4(), rs, d, (NB + 2) * BLK + 80 * UN);
      if (x & 1) v = (u32x4){v.y, v.z, v.w, v.x};
      if (x & 2) v = (u32x4){v.z, v.w, v.x, v.y};
      st4((u32x4){canon(v.x), canon(v.y), canon(v.z), canon(v.w)}, rs, d, NB * BLK + 80 * UN);
    }
  } else {
    st1(exp, rs, own, 0);
    auto done = [&](unsigned n) { lndone<0>(a, rs, j - 40, n | (exp & ~255u), lane, wv); };
    auto past = [&](unsigned n) { lndone<10>(a, rs, j - 40, n | (exp & ~255u), lane, wv); };
    unsigned b = (j - 40) * 4 + wv;
    ldsp t = lds + wv * WREG;
    ln8(C, A); xout<SC1>(C, rs, t, b, lane, 0); done(1); xpre<SC1>(A, ro, b, lane);
    wait_t(t0 + LW);
    xin(A, rx, b + 32, lane);
    unsigned sd = 0;
    xin(B, rx, b + 64, lane);
#pragma nounroll
    for (;; b += 64) {
      ln8(C, A);
      if (b >= 32) past((b >> 5) + 1);
#define SIDE { sd = rfl(hs_wait(rs, OFF_ARR + ((hp >> 8) & 1024u), exp, lane)); if (sd == 1) return exp; sd &= 8192u; }
      if (SWS >= 24 && b >> 6 == 1) SIDE
      xout<SC1>(C, rs, t, b + 32, lane, sd);
      if (b < 32) done(2);
      if (!DX || (DX == 1 ? b < 32 : b >= 128)) xpre<SC1>(A, ro, b + 32, lane);
      if (b >= 192) break;
      xin(A, rx, b + 96, lane); ln8(C, B); past((b >> 5) + 2);
      if (SWS < 24 && b < 32) SIDE
      xout<SC1>(C, rs, t, b + 64, lane, sd);
      if (!DX || (DX == 2 && b >= 64)) xpre<SC1>(B, ro, b + 64, lane);
      xin(B, rx, b + 128, lane);
    }
    done(8);
    return exp;
  }
  wait_vm<0>();
  bar();
  st1(exp | vote * 32, rs, own, 0);
  return exp;
}

DI u32x2 rings(unsigned c) { return (u32x2){NB * BLK + na(c, 80), NB * BLK + na(c, 208)}; }
DI unsigned rot8(unsigned r, unsigned x) { return (r & ~14u) | ((2 * r + 4 * x) & 12u) | (r >> 2 & 2u); }
DI unsigned wsrc(int lane, unsigned b1, unsigned x) { return rot8((lane >> 3 & 1) + 2 * b1 + (lane >> 4) * 4, x) * 12288 + ((lane & 7) ^ (lane >> 4) * 2) * 16; }
DI unsigned wblk(int lane, unsigned t) {
  const unsigned i = (lane >> 3 & 1) * 2 + (lane >> 4 & 1);
  return (lane >> 5) * 6144 + (t >> 2) * 2048 + i * 256 + (lane >> 2 & 1) * 128 + ((t & 3) ^ i) * 32 + (lane & 3) * 8;
}
DI u32x4 rsw(const void* p, unsigned range) {
  const uint64_t x = (uint64_t)p;
  return (u32x4){rfl((unsigned)x), rfl((unsigned)(x >> 32) & 0xFFFFu), range, 0x00020000u};
}
DI unsigned la(ldsp p) { return (unsigned)(uintptr_t)p; }
DI unsigned dbgo(int did, int wv) { return OFF_DBG + MiB + did * 8192 + wv * 32; }
constexpr int IRS = 416, VRS = 400;

DI unsigned role_t1(KArgs& a, const int i1, const unsigned hp, ldsp lds, const int lane, const int wv) {
  const int kind = i1 >> 5, h = i1 & 31, kh = wv >> 1, nh = wv & 1;
  const u16* W = kind == 0 ? a.Wq : kind == 1 ? a.Wk : kind == 2 ? a.Wv : a.w1;
  const unsigned stride = kind < 3 ? 12288u : 3072u, reg = la(lds) + wv * WREG;
  const int gm = lane >> 4, q = (lane >> 2) & 3, p = lane & 3, n = lane & 31, g = 2 * kh + (lane >> 5);
  u32x16 vi = (u32x16)0u, si = (u32x16)0u;
#pragma unroll
  for (int r = 0; r < 3; ++r) { const unsigned L = r * 64 + lane; vi[r] = (kh * 320 + nh * 32 + L / 24) * stride + (L % 24) * 16; }
  vi[3] = la(lds) + 2 * kh * WREG + (8 * (gm >> 1) + q) * 384 + nh * 192 + (4 * (p >> 1) + 2 * ((gm & 1) ^ kh) + (p & 1)) * 8;
  vi[4] = lane * 16;
  const unsigned sd = 0, so = kind == 0 ? (n >> 4) * 6144 + 3 * nh * 1024 + g * 256 + (n & 15) * 16 : kind == 1 ? (n & 1) * 6144 + 3 * nh * 1024 + g * 256 + (n >> 1) * 16
                    : 6 * nh * 1024 + g * 512 + (8 * ((n >> 2) & 3) + 4 * (n >> 4) + (n & 3)) * 16;
  vi[5] = t1p(sd, i1, so); vi[11] = t1p(sd, i1, so + (kind < 2 ? 1024 : 2048)); vi[12] = t1p(sd, i1, so + (kind < 2 ? 2048 : 4096));
  vi[6] = reg + lane * 16;
  vi[7] = la(lds) + (wv ^ 2) * WREG + lane * 16;
  vi[8] = (h * 192 + 96 * nh + 4 * g) * 2;
  vi[14] = OFF_LN + (lane & 7) * 128;
  si[0] = h * 384; si[1] = reg; si[2] = 8 * stride; si[3] = na(xcc_id() >> 2, 249 + 3 * kh);
  si[4] = kind == 2 ? 0 : nb(sd, 0); si[5] = kind == 2 ? BLK : BLK / 2; si[9] = kind == 2 ? 0 : 8192; si[10] = xcc_id() < 4 && (MAP || kind < 3); si[12] = kind == 3 ? MW : kind == 2 ? 0 : xcc_id() < 4 ? MQ : MK;
  si[13] = (xcc_id() >> 2) * 8192; si[11] = opq(hp); si[6] = flag1(sd, i1); si[7] = kind == 3; si[8] = opq(wv); si[15] = dbgo(104 + i1, wv);
#pragma unroll
  for (int i = 0; i < 16; ++i) si[i] = rfl(si[i]);
  const u32x4 rs = rsw(a.ws, WS_RANGE), rw = rsw(W, 640u * stride), rb = rsw(a.b1, 3072u);
  asm volatile(R"ASM(
	v_lshrrev_b32 v15, 2, v4
	v_add_u32 v13, 36864, v3
	s_mov_b32 s19, m0
	s_mov_b32 s18, 12566528
	s_mov_b32 s69, 0x20000
	buffer_load_dword v9, off, s[20:23], s18 offset:3968 sc1
	s_mul_i32 s58, s34, 0
	s_add_u32 s58, s58, s32
	s_add_u32 m0, s33, 0
	s_nop 0
	buffer_load_dwordx4 v0, s[24:27], s58 offen nt lds
	s_add_u32 m0, s33, 1024
	s_nop 0
	buffer_load_dwordx4 v1, s[24:27], s58 offen nt lds
	s_add_u32 m0, s33, 2048
	s_nop 0
	buffer_load_dwordx4 v2, s[24:27], s58 offen nt lds
	s_mul_i32 s58, s34, 1
	s_add_u32 s58, s58, s32
	s_add_u32 m0, s33, 3072
	s_nop 0
	buffer_load_dwordx4 v0, s[24:27], s58 offen nt lds
	s_add_u32 m0, s33, 4096
	s_nop 0
	buffer_load_dwordx4 v1, s[24:27], s58 offen nt lds
	s_add_u32 m0, s33, 5120
	s_nop 0
	buffer_load_dwordx4 v2, s[24:27], s58 offen nt lds
	s_mul_i32 s58, s34, 2
	s_add_u32 s58, s58, s32
	s_add_u32 m0, s33, 6144
	s_nop 0
	buffer_load_dwordx4 v0, s[24:27], s58 offen nt lds
	s_add_u32 m0, s33, 7168
	s_nop 0
	buffer_load_dwordx4 v1, s[24:27], s58 offen nt lds
	s_add_u32 m0, s33, 8192
	s_nop 0
	buffer_load_dwordx4 v2, s[24:27], s58 offen nt lds
	s_mul_i32 s58, s34, 3
	s_add_u32 s58, s58, s32
	s_add_u32 m0, s33, 9216
	s_nop 0
	buffer_load_dwordx4 v0, s[24:27], s58 offen nt lds
	s_add_u32 m0, s33, 10240
	s_nop 0
	buffer_load_dwordx4 v1, s[24:27], s58 offen nt lds
	s_add_u32 m0, s33, 11264
	s_nop 0
	buffer_load_dwordx4 v2, s[24:27], s58 offen nt lds
	s_waitcnt vmcnt(12)
	v_readfirstlane_b32 s70, v9
	s_and_b32 s18, s43, 255
	s_add_u32 s70, s70, 0x9e377900
	s_andn2_b32 s70, s70, 255
	s_or_b32 s70, s70, s18
	s_lshr_b32 s64, s43, 8
	s_and_b32 s71, s64, 1024
	s_add_u32 s71, s71, 12566528
	s_add_u32 s64, s64, 12566528
	v_mov_b32 v9, s70
	s_bitcmp0_b32 s64, 11
	s_cselect_b64 exec, 1, 0
	buffer_store_dword v9, off, s[20:23], s64 sc1
	s_mov_b64 exec, -1
	s_cmp_eq_u32 s44, 0
	s_cbranch_scc1 .Lt1_l2
	s_memrealtime s[98:99]
	s_waitcnt lgkmcnt(0)
	s_add_u32 s76, s98, s44
	s_memrealtime s[98:99]
	s_waitcnt lgkmcnt(0)
	s_sub_u32 s18, s76, s98
	s_cmp_gt_u32 s18, 0xfa0
	s_cbranch_scc1 .Lt1_w4
.Lt1_w3:
	s_memrealtime s[98:99]
	s_waitcnt lgkmcnt(0)
	s_sub_u32 s18, s76, s98
	s_cmp_gt_i32 s18, 0
	s_cbranch_scc1 .Lt1_w3
.Lt1_w4:
.Lt1_l2:
	s_mul_i32 s58, s34, 8
	s_add_u32 s58, s58, s32
	s_add_u32 m0, s33, 12288
	s_nop 0
	buffer_load_dwordx4 v0, s[24:27], s58 offen nt lds
	s_add_u32 m0, s33, 13312
	s_nop 0
	buffer_load_dwordx4 v1, s[24:27], s58 offen nt lds
	s_add_u32 m0, s33, 14336
	s_nop 0
	buffer_load_dwordx4 v2, s[24:27], s58 offen nt lds
	s_mul_i32 s58, s34, 9
	s_add_u32 s58, s58, s32
	s_add_u32 m0, s33, 15360
	s_nop 0
	buffer_load_dwordx4 v0, s[24:27], s58 offen nt lds
	s_add_u32 m0, s33, 16384
	s_nop 0
	buffer_load_dwordx4 v1, s[24:27], s58 offen nt lds
	s_add_u32 m0, s33, 17408
	s_nop 0
	buffer_load_dwordx4 v2, s[24:27], s58 offen nt lds
	s_mul_i32 s58, s34, 10
	s_add_u32 s58, s58, s32
	s_add_u32 m0, s33, 18432
	s_nop 0
	buffer_load_dwordx4 v0, s[24:27], s58 offen nt lds
	s_add_u32 m0, s33, 19456
	s_nop 0
	buffer_load_dwordx4 v1, s[24:27], s58 offen nt lds
	s_add_u32 m0, s33, 20480
	s_nop 0
	buffer_load_dwordx4 v2, s[24:27], s58 offen nt lds
	s_mul_i32 s58, s34, 11
	s_add_u32 s58, s58, s32
	s_add_u32 m0, s33, 21504
	s_nop 0
	buffer_load_dwordx4 v0, s[24:27], s58 offen nt lds
	s_add_u32 m0, s33, 22528
	s_nop 0
	buffer_load_dwordx4 v1, s[24:27], s58 offen nt lds
	s_add_u32 m0, s33, 23552
	s_nop 0
	buffer_load_dwordx4 v2, s[24:27], s58 offen nt lds
	s_mul_i32 s58, s34, 16
	s_add_u32 s58, s58, s32
	s_add_u32 m0, s33, 24576
	s_nop 0
	buffer_load_dwordx4 v0, s[24:27], s58 offen nt lds
	s_add_u32 m0, s33, 25600
	s_nop 0
	buffer_load_dwordx4 v1, s[24:27], s58 offen nt lds
	s_add_u32 m0, s33, 26624
	s_nop 0
	buffer_load_dwordx4 v2, s[24:27], s58 offen nt lds
	s_mul_i32 s58, s34, 17
	s_add_u32 s58, s58, s32
	s_add_u32 m0, s33, 27648
	s_nop 0
	buffer_load_dwordx4 v0, s[24:27], s58 offen nt lds
	s_add_u32 m0, s33, 28672
	s_nop 0
	buffer_load_dwordx4 v1, s[24:27], s58 offen nt lds
	s_add_u32 m0, s33, 29696
	s_nop 0
	buffer_load_dwordx4 v2, s[24:27], s58 offen nt lds
	s_mul_i32 s58, s34, 18
	s_add_u32 s58, s58, s32
	s_add_u32 m0, s33, 30720
	s_nop 0
	buffer_load_dwordx4 v0, s[24:27], s58 offen nt lds
	s_add_u32 m0, s33, 31744
	s_nop 0
	buffer_load_dwordx4 v1, s[24:27], s58 offen nt lds
	s_add_u32 m0, s33, 32768
	s_nop 0
	buffer_load_dwordx4 v2, s[24:27], s58 offen nt lds
	s_mul_i32 s58, s34, 19
	s_add_u32 s58, s58, s32
	s_add_u32 m0, s33, 33792
	s_nop 0
	buffer_load_dwordx4 v0, s[24:27], s58 offen nt lds
	s_add_u32 m0, s33, 34816
	s_nop 0
	buffer_load_dwordx4 v1, s[24:27], s58 offen nt lds
	s_add_u32 m0, s33, 35840
	s_nop 0
	buffer_load_dwordx4 v2, s[24:27], s58 offen nt lds
	buffer_load_dwordx2 v[192:193], v8, s[28:31], 0 offen offset:0
	buffer_load_dwordx2 v[194:195], v8, s[28:31], 0 offen offset:32
	buffer_load_dwordx2 v[196:197], v8, s[28:31], 0 offen offset:64
	buffer_load_dwordx2 v[198:199], v8, s[28:31], 0 offen offset:96
	buffer_load_dwordx2 v[200:201], v8, s[28:31], 0 offen offset:128
	buffer_load_dwordx2 v[202:203], v8, s[28:31], 0 offen offset:160
	s_waitcnt vmcnt(30)
	s_barrier
	ds_read_b64_tr_b16 a[0:1], v3 offset:0
	ds_read_b64_tr_b16 a[2:3], v3 offset:1536
	ds_read_b64_tr_b16 a[4:5], v3 offset:64
	ds_read_b64_tr_b16 a[6:7], v3 offset:1600
	ds_read_b64_tr_b16 a[8:9], v3 offset:128
	ds_read_b64_tr_b16 a[10:11], v3 offset:1664
	ds_read_b64_tr_b16 a[12:13], v3 offset:6144
	ds_read_b64_tr_b16 a[14:15], v3 offset:7680
	ds_read_b64_tr_b16 a[16:17], v3 offset:6208
	ds_read_b64_tr_b16 a[18:19], v3 offset:7744
	ds_read_b64_tr_b16 a[20:21], v3 offset:6272
	ds_read_b64_tr_b16 a[22:23], v3 offset:7808
	ds_read_b64_tr_b16 a[24:25], v13 offset:0
	ds_read_b64_tr_b16 a[26:27], v13 offset:1536
	ds_read_b64_tr_b16 a[28:29], v13 offset:64
	ds_read_b64_tr_b16 a[30:31], v13 offset:1600
	ds_read_b64_tr_b16 a[32:33], v13 offset:128
	ds_read_b64_tr_b16 a[34:35], v13 offset:1664
	ds_read_b64_tr_b16 a[36:37], v13 offset:6144
	ds_read_b64_tr_b16 a[38:39], v13 offset:7680
	ds_read_b64_tr_b16 a[40:41], v13 offset:6208
	ds_read_b64_tr_b16 a[42:43], v13 offset:7744
	ds_read_b64_tr_b16 a[44:45], v13 offset:6272
	ds_read_b64_tr_b16 a[46:47], v13 offset:7808
	s_waitcnt lgkmcnt(0)
	s_barrier
	s_mul_i32 s58, s34, 24
	s_add_u32 s58, s58, s32
	s_add_u32 m0, s33, 0
	s_nop 0
	buffer_load_dwordx4 v0, s[24:27], s58 offen nt lds
	s_add_u32 m0, s33, 1024
	s_nop 0
	buffer_load_dwordx4 v1, s[24:27], s58 offen nt lds
	s_add_u32 m0, s33, 2048
	s_nop 0
	buffer_load_dwordx4 v2, s[24:27], s58 offen nt lds
	s_mul_i32 s58, s34, 25
	s_add_u32 s58, s58, s32
	s_add_u32 m0, s33, 3072
	s_nop 0
	buffer_load_dwordx4 v0, s[24:27], s58 offen nt lds
	s_add_u32 m0, s33, 4096
	s_nop 0
	buffer_load_dwordx4 v1, s[24:27], s58 offen nt lds
	s_add_u32 m0, s33, 5120
	s_nop 0
	buffer_load_dwordx4 v2, s[24:27], s58 offen nt lds
	s_mul_i32 s58, s34, 26
	s_add_u32 s58, s58, s32
	s_add_u32 m0, s33, 6144
	s_nop 0
	buffer_load_dwordx4 v0, s[24:27], s58 offen nt lds
	s_add_u32 m0, s33, 7168
	s_nop 0
	buffer_load_dwordx4 v1, s[24:27], s58 offen nt lds
	s_add_u32 m0, s33, 8192
	s_nop 0
	buffer_load_dwordx4 v2, s[24:27], s58 offen nt lds
	s_mul_i32 s58, s34, 27
	s_add_u32 s58, s58, s32
	s_add_u32 m0, s33, 9216
	s_nop 0
	buffer_load_dwordx4 v0, s[24:27], s58 offen nt lds
	s_add_u32 m0, s33, 10240
	s_nop 0
	buffer_load_dwordx4 v1, s[24:27], s58 offen nt lds
	s_add_u32 m0, s33, 11264
	s_nop 0
	buffer_load_dwordx4 v2, s[24:27], s58 offen nt lds
	buffer_load_dwordx4 v[252:255], v4, s[20:23], s71 offen offset:0 sc1
	buffer_load_dword v251, v14, s[20:23], 0 offen sc1
	s_waitcnt vmcnt(32)
	s_barrier
	ds_read_b64_tr_b16 a[48:49], v3 offset:12288
	ds_read_b64_tr_b16 a[50:51], v3 offset:13824
	ds_read_b64_tr_b16 a[52:53], v3 offset:12352
	ds_read_b64_tr_b16 a[54:55], v3 offset:13888
	ds_read_b64_tr_b16 a[56:57], v3 offset:12416
	ds_read_b64_tr_b16 a[58:59], v3 offset:13952
	ds_read_b64_tr_b16 a[60:61], v3 offset:18432
	ds_read_b64_tr_b16 a[62:63], v3 offset:19968
	ds_read_b64_tr_b16 a[64:65], v3 offset:18496
	ds_read_b64_tr_b16 a[66:67], v3 offset:20032
	ds_read_b64_tr_b16 a[68:69], v3 offset:18560
	ds_read_b64_tr_b16 a[70:71], v3 offset:20096
	ds_read_b64_tr_b16 a[72:73], v13 offset:12288
	ds_read_b64_tr_b16 a[74:75], v13 offset:13824
	ds_read_b64_tr_b16 a[76:77], v13 offset:12352
	ds_read_b64_tr_b16 a[78:79], v13 offset:13888
	ds_read_b64_tr_b16 a[80:81], v13 offset:12416
	ds_read_b64_tr_b16 a[82:83], v13 offset:13952
	ds_read_b64_tr_b16 a[84:85], v13 offset:18432
	ds_read_b64_tr_b16 a[86:87], v13 offset:19968
	ds_read_b64_tr_b16 a[88:89], v13 offset:18496
	ds_read_b64_tr_b16 a[90:91], v13 offset:20032
	ds_read_b64_tr_b16 a[92:93], v13 offset:18560
	ds_read_b64_tr_b16 a[94:95], v13 offset:20096
	s_waitcnt lgkmcnt(0)
	s_barrier
	s_mul_i32 s58, s34, 32
	s_add_u32 s58, s58, s32
	s_add_u32 m0, s33, 12288
	s_nop 0
	buffer_load_dwordx4 v0, s[24:27], s58 offen nt lds
	s_add_u32 m0, s33, 13312
	s_nop 0
	buffer_load_dwordx4 v1, s[24:27], s58 offen nt lds
	s_add_u32 m0, s33, 14336
	s_nop 0
	buffer_load_dwordx4 v2, s[24:27], s58 offen nt lds
	s_mul_i32 s58, s34, 33
	s_add_u32 s58, s58, s32
	s_add_u32 m0, s33, 15360
	s_nop 0
	buffer_load_dwordx4 v0, s[24:27], s58 offen nt lds
	s_add_u32 m0, s33, 16384
	s_nop 0
	buffer_load_dwordx4 v1, s[24:27], s58 offen nt lds
	s_add_u32 m0, s33, 17408
	s_nop 0
	buffer_load_dwordx4 v2, s[24:27], s58 offen nt lds
	s_mul_i32 s58, s34, 34
	s_add_u32 s58, s58, s32
	s_add_u32 m0, s33, 18432
	s_nop 0
	buffer_load_dwordx4 v0, s[24:27], s58 offen nt lds
	s_add_u32 m0, s33, 19456
	s_nop 0
	buffer_load_dwordx4 v1, s[24:27], s58 offen nt lds
	s_add_u32 m0, s33, 20480
	s_nop 0
	buffer_load_dwordx4 v2, s[24:27], s58 offen nt lds
	s_mul_i32 s58, s34, 35
	s_add_u32 s58, s58, s32
	s_add_u32 m0, s33, 21504
	s_nop 0
	buffer_load_dwordx4 v0, s[24:27], s58 offen nt lds
	s_add_u32 m0, s33, 22528
	s_nop 0
	buffer_load_dwordx4 v1, s[24:27], s58 offen nt lds
	s_add_u32 m0, s33, 23552
	s_nop 0
	buffer_load_dwordx4 v2, s[24:27], s58 offen nt lds
	s_waitcnt vmcnt(32)
	s_barrier
	ds_read_b64_tr_b16 a[96:97], v3 offset:24576
	ds_read_b64_tr_b16 a[98:99], v3 offset:26112
	ds_read_b64_tr_b16 a[100:101], v3 offset:24640
	ds_read_b64_tr_b16 a[102:103], v3 offset:26176
	ds_read_b64_tr_b16 a[104:105], v3 offset:24704
	ds_read_b64_tr_b16 a[106:107], v3 offset:26240
	ds_read_b64_tr_b16 a[108:109], v3 offset:30720
	ds_read_b64_tr_b16 a[110:111], v3 offset:32256
	ds_read_b64_tr_b16 a[112:113], v3 offset:30784
	ds_read_b64_tr_b16 a[114:115], v3 offset:32320
	ds_read_b64_tr_b16 a[116:117], v3 offset:30848
	ds_read_b64_tr_b16 a[118:119], v3 offset:32384
	ds_read_b64_tr_b16 a[120:121], v13 offset:24576
	ds_read_b64_tr_b16 a[122:123], v13 offset:26112
	ds_read_b64_tr_b16 a[124:125], v13 offset:24640
	ds_read_b64_tr_b16 a[126:127], v13 offset:26176
	ds_read_b64_tr_b16 a[128:129], v13 offset:24704
	ds_read_b64_tr_b16 a[130:131], v13 offset:26240
	ds_read_b64_tr_b16 a[132:133], v13 offset:30720
	ds_read_b64_tr_b16 a[134:135], v13 offset:32256
	ds_read_b64_tr_b16 a[136:137], v13 offset:30784
	ds_read_b64_tr_b16 a[138:139], v13 offset:32320
	ds_read_b64_tr_b16 a[140:141], v13 offset:30848
	ds_read_b64_tr_b16 a[142:143], v13 offset:32384
	s_waitcnt lgkmcnt(0)
	s_waitcnt vmcnt(14)
	s_barrier
	ds_read_b64_tr_b16 a[144:145], v3 offset:0
	ds_read_b64_tr_b16 a[146:147], v3 offset:1536
	ds_read_b64_tr_b16 a[148:149], v3 offset:64
	ds_read_b64_tr_b16 a[150:151], v3 offset:1600
	ds_read_b64_tr_b16 a[152:153], v3 offset:128
	ds_read_b64_tr_b16 a[154:155], v3 offset:1664
	ds_read_b64_tr_b16 a[156:157], v3 offset:6144
	ds_read_b64_tr_b16 a[158:159], v3 offset:7680
	ds_read_b64_tr_b16 a[160:161], v3 offset:6208
	ds_read_b64_tr_b16 a[162:163], v3 offset:7744
	ds_read_b64_tr_b16 a[164:165], v3 offset:6272
	ds_read_b64_tr_b16 a[166:167], v3 offset:7808
	ds_read_b64_tr_b16 a[168:169], v13 offset:0
	ds_read_b64_tr_b16 a[170:171], v13 offset:1536
	ds_read_b64_tr_b16 a[172:173], v13 offset:64
	ds_read_b64_tr_b16 a[174:175], v13 offset:1600
	ds_read_b64_tr_b16 a[176:177], v13 offset:128
	ds_read_b64_tr_b16 a[178:179], v13 offset:1664
	ds_read_b64_tr_b16 a[180:181], v13 offset:6144
	ds_read_b64_tr_b16 a[182:183], v13 offset:7680
	ds_read_b64_tr_b16 a[184:185], v13 offset:6208
	ds_read_b64_tr_b16 a[186:187], v13 offset:7744
	ds_read_b64_tr_b16 a[188:189], v13 offset:6272
	ds_read_b64_tr_b16 a[190:191], v13 offset:7808
	s_waitcnt lgkmcnt(0)
.Lt1_h5:
	s_waitcnt vmcnt(13)
	v_xor_b32 v252, s70, v252
	v_xor_b32 v253, s70, v253
	v_xor_b32 v254, s70, v254
	v_xor_b32 v255, s70, v255
	v_or3_b32 v9, v252, v253, v254
	v_or_b32 v9, v9, v255
	v_and_b32 v10, 0xffffff07, v9
	v_cmp_ne_u32 vcc, 0, v10
	s_cbranch_vccz .Lt1_h6
	v_cmp_lt_u32 vcc, 0xff, v9
	s_cbranch_vccz .Lt1_x1
	s_cmp_eq_u32 s69, 0
	s_cbranch_scc1 .Lt1_h6
	s_sub_u32 s69, s69, 1
	s_sleep 2
	buffer_load_dwordx4 v[252:255], v4, s[20:23], s71 offen offset:0 sc1
	s_waitcnt vmcnt(0)
	s_branch .Lt1_h5
.Lt1_h6:
	v_and_b32 v10, 64, v252
	v_cmp_ne_u32 vcc, 0, v10
	s_bcnt1_i32_b64 s72, vcc
	v_and_b32 v10, 64, v253
	v_cmp_ne_u32 vcc, 0, v10
	s_bcnt1_i32_b64 s18, vcc
	s_add_u32 s72, s72, s18
	v_and_b32 v10, 64, v254
	v_cmp_ne_u32 vcc, 0, v10
	s_bcnt1_i32_b64 s18, vcc
	s_add_u32 s72, s72, s18
	v_and_b32 v10, 64, v255
	v_cmp_ne_u32 vcc, 0, v10
	s_bcnt1_i32_b64 s18, vcc
	s_add_u32 s72, s72, s18
	s_cmp_ge_u32 s72, 20
	s_cselect_b32 s72, 8192, 0
	s_and_b32 s18, s72, s41
	s_andn2_b32 s58, s72, s41
	s_mov_b32 s43, s72
	s_xor_b32 s35, s72, s35
	s_xor_b32 s38, s72, s38
	s_xor_b32 s36, s18, s36
	v_xor_b32 v5, s58, v5
	v_xor_b32 v11, s58, v11
	v_xor_b32 v12, s58, v12
	s_add_u32 s50, s35, 0
	s_add_u32 s51, s35, 4096
	s_add_u32 s52, s35, 16384
	s_add_u32 s53, s35, 20480
	s_add_u32 s54, s35, 32768
	s_add_u32 s35, s35, 4194304
	s_and_b32 s35, s35, 268435455
	s_andn2_b32 s58, s70, 255
	s_add_u32 s58, s58, 1
.Lt1_n8:
	s_waitcnt vmcnt(12)
	v_subrev_u32 v251, s58, v251
	v_cmp_lt_u32 vcc, 15, v251
	s_cbranch_vccz .Lt1_n9
	s_cmp_eq_u32 s69, 0
	s_cbranch_scc1 .Lt1_n9
	s_sub_u32 s69, s69, 1
	s_sleep 2
	buffer_load_dword v251, v14, s[20:23], 0 offen sc1
	s_waitcnt vmcnt(0)
	s_branch .Lt1_n8
.Lt1_n9:
	buffer_load_dwordx4 v[16:19], v4, s[20:23], s50 offen offset:0 sc1
	buffer_load_dwordx4 v[20:23], v4, s[20:23], s50 offen offset:1024 sc1
	buffer_load_dwordx4 v[24:27], v4, s[20:23], s50 offen offset:2048 sc1
	buffer_load_dwordx4 v[28:31], v4, s[20:23], s50 offen offset:3072 sc1
	buffer_load_dwordx4 v[32:35], v4, s[20:23], s51 offen offset:0 sc1
	buffer_load_dwordx4 v[36:39], v4, s[20:23], s51 offen offset:1024 sc1
	buffer_load_dwordx4 v[40:43], v4, s[20:23], s51 offen offset:2048 sc1
	buffer_load_dwordx4 v[44:47], v4, s[20:23], s51 offen offset:3072 sc1
	buffer_load_dwordx4 v[48:51], v4, s[20:23], s52 offen offset:0 sc1
	buffer_load_dwordx4 v[52:55], v4, s[20:23], s52 offen offset:1024 sc1
	buffer_load_dwordx4 v[56:59], v4, s[20:23], s52 offen offset:2048 sc1
	buffer_load_dwordx4 v[60:63], v4, s[20:23], s52 offen offset:3072 sc1
	buffer_load_dwordx4 v[64:67], v4, s[20:23], s53 offen offset:0 sc1
	buffer_load_dwordx4 v[68:71], v4, s[20:23], s53 offen offset:1024 sc1
	buffer_load_dwordx4 v[72:75], v4, s[20:23], s53 offen offset:2048 sc1
	buffer_load_dwordx4 v[76:79], v4, s[20:23], s53 offen offset:3072 sc1
	buffer_load_dwordx4 v[80:83], v4, s[20:23], s54 offen offset:0 sc1
	buffer_load_dwordx4 v[84:87], v4, s[20:23], s54 offen offset:1024 sc1
	buffer_load_dwordx4 v[88:91], v4, s[20:23], s54 offen offset:2048 sc1
	buffer_load_dwordx4 v[92:95], v4, s[20:23], s54 offen offset:3072 sc1
	s_add_u32 s50, s35, 0
	s_add_u32 s51, s35, 4096
	s_add_u32 s52, s35, 16384
	s_add_u32 s53, s35, 20480
	s_add_u32 s54, s35, 32768
	s_add_u32 s35, s35, 4194304
	s_and_b32 s35, s35, 268435455
	s_waitcnt vmcnt(20)
	s_barrier
	ds_read_b64_tr_b16 a[192:193], v3 offset:12288
	ds_read_b64_tr_b16 a[194:195], v3 offset:13824
	ds_read_b64_tr_b16 a[196:197], v3 offset:12352
	ds_read_b64_tr_b16 a[198:199], v3 offset:13888
	ds_read_b64_tr_b16 a[200:201], v3 offset:12416
	ds_read_b64_tr_b16 a[202:203], v3 offset:13952
	ds_read_b64_tr_b16 a[204:205], v3 offset:18432
	ds_read_b64_tr_b16 a[206:207], v3 offset:19968
	ds_read_b64_tr_b16 a[208:209], v3 offset:18496
	ds_read_b64_tr_b16 a[210:211], v3 offset:20032
	ds_read_b64_tr_b16 a[212:213], v3 offset:18560
	ds_read_b64_tr_b16 a[214:215], v3 offset:20096
	ds_read_b64_tr_b16 a[216:217], v13 offset:12288
	ds_read_b64_tr_b16 a[218:219], v13 offset:13824
	ds_read_b64_tr_b16 a[220:221], v13 offset:12352
	ds_read_b64_tr_b16 a[222:223], v13 offset:13888
	ds_read_b64_tr_b16 a[224:225], v13 offset:12416
	ds_read_b64_tr_b16 a[226:227], v13 offset:13952
	ds_read_b64_tr_b16 a[228:229], v13 offset:18432
	ds_read_b64_tr_b16 a[230:231], v13 offset:19968
	ds_read_b64_tr_b16 a[232:233], v13 offset:18496
	ds_read_b64_tr_b16 a[234:235], v13 offset:20032
	ds_read_b64_tr_b16 a[236:237], v13 offset:18560
	ds_read_b64_tr_b16 a[238:239], v13 offset:20096
	s_waitcnt lgkmcnt(0)
	s_waitcnt vmcnt(19)
	v_mfma_f32_32x32x16_bf16 v[96:111], a[0:3], v[16:19], 0
	v_mfma_f32_32x32x16_bf16 v[112:127], a[4:7], v[16:19], 0
	v_mfma_f32_32x32x16_bf16 v[128:143], a[8:11], v[16:19], 0
	buffer_load_dwordx4 v[16:19], v4, s[20:23], s50 offen offset:0 sc1
	s_waitcnt vmcnt(19)
	v_mfma_f32_32x32x16_bf16 v[96:111], a[12:15], v[20:23], v[96:111]
	v_mfma_f32_32x32x16_bf16 v[112:127], a[16:19], v[20:23], v[112:127]
	v_mfma_f32_32x32x16_bf16 v[128:143], a[20:23], v[20:23], v[128:143]
	buffer_load_dwordx4 v[20:23], v4, s[20:23], s50 offen offset:1024 sc1
	s_waitcnt vmcnt(19)
	v_mfma_f32_32x32x16_bf16 v[96:111], a[24:27], v[24:27], v[96:111]
	v_mfma_f32_32x32x16_bf16 v[112:127], a[28:31], v[24:27], v[112:127]
	v_mfma_f32_32x32x16_bf16 v[128:143], a[32:35], v[24:27], v[128:143]
	buffer_load_dwordx4 v[24:27], v4, s[20:23], s50 offen offset:2048 sc1
	s_waitcnt vmcnt(19)
	v_mfma_f32_32x32x16_bf16 v[96:111], a[36:39], v[28:31], v[96:111]
	v_mfma_f32_32x32x16_bf16 v[112:127], a[40:43], v[28:31], v[112:127]
	v_mfma_f32_32x32x16_bf16 v[128:143], a[44:47], v[28:31], v[128:143]
	buffer_load_dwordx4 v[28:31], v4, s[20:23], s50 offen offset:3072 sc1
	s_waitcnt vmcnt(19)
	v_mfma_f32_32x32x16_bf16 v[96:111], a[48:51], v[32:35], v[96:111]
	v_mfma_f32_32x32x16_bf16 v[112:127], a[52:55], v[32:35], v[112:127]
	v_mfma_f32_32x32x16_bf16 v[128:143], a[56:59], v[32:35], v[128:143]
	buffer_load_dwordx4 v[32:35], v4, s[20:23], s51 offen offset:0 sc1
	s_waitcnt vmcnt(19)
	v_mfma_f32_32x32x16_bf16 v[96:111], a[60:63], v[36:39], v[96:111]
	v_mfma_f32_32x32x16_bf16 v[112:127], a[64:67], v[36:39], v[112:127]
	v_mfma_f32_32x32x16_bf16 v[128:143], a[68:71], v[36:39], v[128:143]
	buffer_load_dwordx4 v[36:39], v4, s[20:23], s51 offen offset:1024 sc1
	s_waitcnt vmcnt(19)
	v_mfma_f32_32x32x16_bf16 v[96:111], a[72:75], v[40:43], v[96:111]
	v_mfma_f32_32x32x16_bf16 v[112:127], a[76:79], v[40:43], v[112:127]
	v_mfma_f32_32x32x16_bf16 v[128:143], a[80:83], v[40:43], v[128:143]
	buffer_load_dwordx4 v[40:43], v4, s[20:23], s51 offen offset:2048 sc1
	s_waitcnt vmcnt(19)
	v_mfma_f32_32x32x16_bf16 v[96:111], a[84:87], v[44:47], v[96:111]
	v_mfma_f32_32x32x16_bf16 v[112:127], a[88:91], v[44:47], v[112:127]
	v_mfma_f32_32x32x16_bf16 v[128:143], a[92:95], v[44:47], v[128:143]
	buffer_load_dwordx4 v[44:47], v4, s[20:23], s51 offen offset:3072 sc1
	s_waitcnt vmcnt(19)
	v_mfma_f32_32x32x16_bf16 v[96:111], a[96:99], v[48:51], v[96:111]
	v_mfma_f32_32x32x16_bf16 v[112:127], a[100:103], v[48:51], v[112:127]
	v_mfma_f32_32x32x16_bf16 v[128:143], a[104:107], v[48:51], v[128:143]
	buffer_load_dwordx4 v[48:51], v4, s[20:23], s52 offen offset:0 sc1
	s_waitcnt vmcnt(19)
	v_mfma_f32_32x32x16_bf16 v[96:111], a[108:111], v[52:55], v[96:111]
	v_mfma_f32_32x32x16_bf16 v[112:127], a[112:115], v[52:55], v[112:127]
	v_mfma_f32_32x32x16_bf16 v[128:143], a[116:119], v[52:55], v[128:143]
	buffer_load_dwordx4 v[52:55], v4, s[20:23], s52 offen offset:1024 sc1
	s_waitcnt vmcnt(19)
	v_mfma_f32_32x32x16_bf16 v[96:111], a[120:123], v[56:59], v[96:111]
	v_mfma_f32_32x32x16_bf16 v[112:127], a[124:127], v[56:59], v[112:127]
	v_mfma_f32_32x32x16_bf16 v[128:143], a[128:131], v[56:59], v[128:143]
	buffer_load_dwordx4 v[56:59], v4, s[20:23], s52 offen offset:2048 sc1
	s_waitcnt vmcnt(19)
	v_mfma_f32_32x32x16_bf16 v[96:111], a[132:135], v[60:63], v[96:111]
	v_mfma_f32_32x32x16_bf16 v[112:127], a[136:139], v[60:63], v[112:127]
	v_mfma_f32_32x32x16_bf16 v[128:143], a[140:143], v[60:63], v[128:143]
	buffer_load_dwordx4 v[60:63], v4, s[20:23], s52 offen offset:3072 sc1
	s_waitcnt vmcnt(19)
	v_mfma_f32_32x32x16_bf16 v[96:111], a[144:147], v[64:67], v[96:111]
	v_mfma_f32_32x32x16_bf16 v[112:127], a[148:151], v[64:67], v[112:127]
	v_mfma_f32_32x32x16_bf16 v[128:143], a[152:155], v[64:67], v[128:143]
	buffer_load_dwordx4 v[64:67], v4, s[20:23], s53 offen offset:0 sc1
	s_waitcnt vmcnt(19)
	v_mfma_f32_32x32x16_bf16 v[96:111], a[156:159], v[68:71], v[96:111]
	v_mfma_f32_32x32x16_bf16 v[112:127], a[160:163], v[68:71], v[112:127]
	v_mfma_f32_32x32x16_bf16 v[128:143], a[164:167], v[68:71], v[128:143]
	buffer_load_dwordx4 v[68:71], v4, s[20:23], s53 offen offset:1024 sc1
	s_waitcnt vmcnt(19)
	v_mfma_f32_32x32x16_bf16 v[96:111], a[168:171], v[72:75], v[96:111]
	v_mfma_f32_32x32x16_bf16 v[112:127], a[172:175], v[72:75], v[112:127]
	v_mfma_f32_32x32x16_bf16 v[128:143], a[176:179], v[72:75], v[128:143]
	buffer_load_dwordx4 v[72:75], v4, s[20:23], s53 offen offset:2048 sc1
	s_waitcnt vmcnt(19)
	v_mfma_f32_32x32x16_bf16 v[96:111], a[180:183], v[76:79], v[96:111]
	v_mfma_f32_32x32x16_bf16 v[112:127], a[184:187], v[76:79], v[112:127]
	v_mfma_f32_32x32x16_bf16 v[128:143], a[188:191], v[76:79], v[128:143]
	buffer_load_dwordx4 v[76:79], v4, s[20:23], s53 offen offset:3072 sc1
	s_waitcnt vmcnt(19)
	v_mfma_f32_32x32x16_bf16 v[96:111], a[192:195], v[80:83], v[96:111]
	v_mfma_f32_32x32x16_bf16 v[112:127], a[196:199], v[80:83], v[112:127]
	v_mfma_f32_32x32x16_bf16 v[128:143], a[200:203], v[80:83], v[128:143]
	buffer_load_dwordx4 v[80:83], v4, s[20:23], s54 offen offset:0 sc1
	s_waitcnt vmcnt(19)
	v_mfma_f32_32x32x16_bf16 v[96:111], a[204:207], v[84:87], v[96:111]
	v_mfma_f32_32x32x16_bf16 v[112:127], a[208:211], v[84:87], v[112:127]
	v_mfma_f32_32x32x16_bf16 v[128:143], a[212:215], v[84:87], v[128:143]
	buffer_load_dwordx4 v[84:87], v4, s[20:23], s54 offen offset:1024 sc1
	s_waitcnt vmcnt(19)
	v_mfma_f32_32x32x16_bf16 v[96:111], a[216:219], v[88:91], v[96:111]
	v_mfma_f32_32x32x16_bf16 v[112:127], a[220:223], v[88:91], v[112:127]
	v_mfma_f32_32x32x16_bf16 v[128:143], a[224:227], v[88:91], v[128:143]
	buffer_load_dwordx4 v[88:91], v4, s[20:23], s54 offen offset:2048 sc1
	s_waitcnt vmcnt(19)
	v_mfma_f32_32x32x16_bf16 v[96:111], a[228:231], v[92:95], v[96:111]
	v_mfma_f32_32x32x16_bf16 v[112:127], a[232:235], v[92:95], v[112:127]
	v_mfma_f32_32x32x16_bf16 v[128:143], a[236:239], v[92:95], v[128:143]
	buffer_load_dwordx4 v[92:95], v4, s[20:23], s54 offen offset:3072 sc1
	v_lshlrev_b32 v216, 16, v192
	v_and_b32 v217, 0xffff0000, v192
	v_lshlrev_b32 v218, 16, v193
	v_and_b32 v219, 0xffff0000, v193
	v_lshlrev_b32 v220, 16, v194
	v_and_b32 v221, 0xffff0000, v194
	v_lshlrev_b32 v222, 16, v195
	v_and_b32 v223, 0xffff0000, v195
	v_lshlrev_b32 v224, 16, v196
	v_and_b32 v225, 0xffff0000, v196
	v_lshlrev_b32 v226, 16, v197
	v_and_b32 v227, 0xffff0000, v197
	v_lshlrev_b32 v228, 16, v198
	v_and_b32 v229, 0xffff0000, v198
	v_lshlrev_b32 v230, 16, v199
	v_and_b32 v231, 0xffff0000, v199
	v_lshlrev_b32 v232, 16, v200
	v_and_b32 v233, 0xffff0000, v200
	v_lshlrev_b32 v234, 16, v201
	v_and_b32 v235, 0xffff0000, v201
	v_lshlrev_b32 v236, 16, v202
	v_and_b32 v237, 0xffff0000, v202
	v_lshlrev_b32 v238, 16, v203
	v_and_b32 v239, 0xffff0000, v203
	s_mov_b32 m0, s19
	s_mov_b32 s48, 1
	s_mov_b32 s59, 0
	s_cmp_eq_u32 s40, 0
	s_cselect_b64 s[60:61], 1, 0
	s_cmp_lg_u32 s39, 0
	s_cbranch_scc1 .Lt1_k10
	s_add_u32 s55, s36, s37
	s_xor_b32 s55, s55, s41
	s_lshl_b32 s65, s42, 7
	s_or_b32 s65, s65, 31
.Lt1_seg13:
	s_and_b32 s58, s48, 7
	s_add_u32 s18, s48, 4
	s_cmp_eq_u32 s18, s65
	s_cselect_b32 s58, 5, s58
	s_lshl2_add_u32 s58, s58, s40
	s_mov_b32 s63, 0
	s_cmp_lg_u32 s58, 20
	s_cbranch_scc1 .Lt1_l14
	s_lshr_b32 s63, s48, 3
	s_add_u32 s63, s63, 2
	s_min_u32 s63, s63, 8
	s_cmp_eq_u32 s18, s65
	s_cselect_b32 s63, 24, s63
	buffer_load_dword v252, v14, s[20:23], 0 offen sc1
.Lt1_l14:
	s_waitcnt vmcnt(19)
	v_mfma_f32_32x32x16_bf16 v[144:159], a[0:3], v[16:19], 0
	s_add_u32 s50, s35, 0
	s_add_u32 s51, s35, 4096
	s_add_u32 s52, s35, 16384
	s_add_u32 s53, s35, 20480
	s_add_u32 s54, s35, 32768
	s_add_u32 s35, s35, 4194304
	s_and_b32 s35, s35, 268435455
	v_mfma_f32_32x32x16_bf16 v[160:175], a[4:7], v[16:19], 0
	ds_write_b128 v6, v[104:107] offset:0
	v_mfma_f32_32x32x16_bf16 v[176:191], a[8:11], v[16:19], 0
	buffer_load_dwordx4 v[16:19], v4, s[20:23], s50 offen offset:0 sc1
	ds_write_b128 v6, v[108:111] offset:1024
	s_waitcnt vmcnt(19)
	v_mfma_f32_32x32x16_bf16 v[144:159], a[12:15], v[20:23], v[144:159]
	ds_write_b128 v6, v[120:123] offset:2048
	v_mfma_f32_32x32x16_bf16 v[160:175], a[16:19], v[20:23], v[160:175]
	ds_write_b128 v6, v[124:127] offset:3072
	v_mfma_f32_32x32x16_bf16 v[176:191], a[20:23], v[20:23], v[176:191]
	buffer_load_dwordx4 v[20:23], v4, s[20:23], s50 offen offset:1024 sc1
	ds_write_b128 v6, v[136:139] offset:4096
	s_waitcnt vmcnt(19)
	v_mfma_f32_32x32x16_bf16 v[144:159], a[24:27], v[24:27], v[144:159]
	ds_write_b128 v6, v[140:143] offset:5120
	v_mfma_f32_32x32x16_bf16 v[160:175], a[28:31], v[24:27], v[160:175]
	v_mfma_f32_32x32x16_bf16 v[176:191], a[32:35], v[24:27], v[176:191]
	buffer_load_dwordx4 v[24:27], v4, s[20:23], s50 offen offset:2048 sc1
	s_waitcnt vmcnt(19)
	v_mfma_f32_32x32x16_bf16 v[144:159], a[36:39], v[28:31], v[144:159]
	v_mfma_f32_32x32x16_bf16 v[160:175], a[40:43], v[28:31], v[160:175]
	s_waitcnt lgkmcnt(0)
	s_barrier
	s_sub_u32 s58, s48, 2
	s_max_i32 s58, s58, s59
	v_mov_b32 v9, s58
	s_mov_b64 exec, s[60:61]
	buffer_store_dword v9, off, s[20:23], s38 sc1
	s_mov_b64 exec, -1
	v_mfma_f32_32x32x16_bf16 v[176:191], a[44:47], v[28:31], v[176:191]
	buffer_load_dwordx4 v[28:31], v4, s[20:23], s50 offen offset:3072 sc1
	ds_read_b128 v[192:195], v7 offset:0
	ds_read_b128 v[196:199], v7 offset:1024
	s_waitcnt vmcnt(19)
	v_mfma_f32_32x32x16_bf16 v[144:159], a[48:51], v[32:35], v[144:159]
	ds_read_b128 v[200:203], v7 offset:2048
	ds_read_b128 v[204:207], v7 offset:3072
	v_mfma_f32_32x32x16_bf16 v[160:175], a[52:55], v[32:35], v[160:175]
	ds_read_b128 v[208:211], v7 offset:4096
	ds_read_b128 v[212:215], v7 offset:5120
	v_mfma_f32_32x32x16_bf16 v[176:191], a[56:59], v[32:35], v[176:191]
	buffer_load_dwordx4 v[32:35], v4, s[20:23], s51 offen offset:0 sc1
	s_waitcnt vmcnt(19)
	v_mfma_f32_32x32x16_bf16 v[144:159], a[60:63], v[36:39], v[144:159]
	v_mfma_f32_32x32x16_bf16 v[160:175], a[64:67], v[36:39], v[160:175]
	s_waitcnt lgkmcnt(5)
	v_add_f32 v192, v192, v96
	v_add_f32 v193, v193, v97
	v_add_f32 v194, v194, v98
	v_add_f32 v195, v195, v99
	v_mfma_f32_32x32x16_bf16 v[176:191], a[68:71], v[36:39], v[176:191]
	buffer_load_dwordx4 v[36:39], v4, s[20:23], s51 offen offset:1024 sc1
	s_waitcnt lgkmcnt(4)
	v_add_f32 v196, v196, v100
	v_add_f32 v197, v197, v101
	v_add_f32 v198, v198, v102
	v_add_f32 v199, v199, v103
	s_waitcnt vmcnt(19)
	v_mfma_f32_32x32x16_bf16 v[144:159], a[72:75], v[40:43], v[144:159]
	v_cvt_pk_bf16_f32 v240, v192, v196
	v_cvt_pk_bf16_f32 v241, v193, v197
	v_cvt_pk_bf16_f32 v242, v194, v198
	v_cvt_pk_bf16_f32 v243, v195, v199
	v_mfma_f32_32x32x16_bf16 v[160:175], a[76:79], v[40:43], v[160:175]
	buffer_store_dwordx4 v[240:243], v5, s[20:23], s36 offen sc1
	s_waitcnt lgkmcnt(3)
	v_add_f32 v200, v200, v112
	v_add_f32 v201, v201, v113
	v_add_f32 v202, v202, v114
	v_mfma_f32_32x32x16_bf16 v[176:191], a[80:83], v[40:43], v[176:191]
	buffer_load_dwordx4 v[40:43], v4, s[20:23], s51 offen offset:2048 sc1
	v_add_f32 v203, v203, v115
	s_waitcnt lgkmcnt(2)
	v_add_f32 v204, v204, v116
	v_add_f32 v205, v205, v117
	v_add_f32 v206, v206, v118
	s_waitcnt vmcnt(20)
	v_mfma_f32_32x32x16_bf16 v[144:159], a[84:87], v[44:47], v[144:159]
	v_add_f32 v207, v207, v119
	v_cvt_pk_bf16_f32 v244, v200, v204
	v_cvt_pk_bf16_f32 v245, v201, v205
	v_cvt_pk_bf16_f32 v246, v202, v206
	v_mfma_f32_32x32x16_bf16 v[160:175], a[88:91], v[44:47], v[160:175]
	v_cvt_pk_bf16_f32 v247, v203, v207
	buffer_store_dwordx4 v[244:247], v11, s[20:23], s36 offen sc1
	s_waitcnt lgkmcnt(1)
	v_add_f32 v208, v208, v128
	v_add_f32 v209, v209, v129
	v_mfma_f32_32x32x16_bf16 v[176:191], a[92:95], v[44:47], v[176:191]
	buffer_load_dwordx4 v[44:47], v4, s[20:23], s51 offen offset:3072 sc1
	v_add_f32 v210, v210, v130
	v_add_f32 v211, v211, v131
	s_waitcnt lgkmcnt(0)
	v_add_f32 v212, v212, v132
	v_add_f32 v213, v213, v133
	s_waitcnt vmcnt(21)
	v_mfma_f32_32x32x16_bf16 v[144:159], a[96:99], v[48:51], v[144:159]
	v_add_f32 v214, v214, v134
	v_add_f32 v215, v215, v135
	v_cvt_pk_bf16_f32 v248, v208, v212
	v_cvt_pk_bf16_f32 v249, v209, v213
	v_mfma_f32_32x32x16_bf16 v[160:175], a[100:103], v[48:51], v[160:175]
	v_cvt_pk_bf16_f32 v250, v210, v214
	v_cvt_pk_bf16_f32 v251, v211, v215
	buffer_store_dwordx4 v[248:251], v12, s[20:23], s36 offen sc1
	v_mfma_f32_32x32x16_bf16 v[176:191], a[104:107], v[48:51], v[176:191]
	buffer_load_dwordx4 v[48:51], v4, s[20:23], s52 offen offset:0 sc1
	s_waitcnt vmcnt(22)
	v_mfma_f32_32x32x16_bf16 v[144:159], a[108:111], v[52:55], v[144:159]
	v_mfma_f32_32x32x16_bf16 v[160:175], a[112:115], v[52:55], v[160:175]
	v_mfma_f32_32x32x16_bf16 v[176:191], a[116:119], v[52:55], v[176:191]
	buffer_load_dwordx4 v[52:55], v4, s[20:23], s52 offen offset:1024 sc1
	s_waitcnt vmcnt(22)
	v_mfma_f32_32x32x16_bf16 v[144:159], a[120:123], v[56:59], v[144:159]
	v_mfma_f32_32x32x16_bf16 v[160:175], a[124:127], v[56:59], v[160:175]
	v_mfma_f32_32x32x16_bf16 v[176:191], a[128:131], v[56:59], v[176:191]
	buffer_load_dwordx4 v[56:59], v4, s[20:23], s52 offen offset:2048 sc1
	s_waitcnt vmcnt(22)
	v_mfma_f32_32x32x16_bf16 v[144:159], a[132:135], v[60:63], v[144:159]
	v_mfma_f32_32x32x16_bf16 v[160:175], a[136:139], v[60:63], v[160:175]
	v_mfma_f32_32x32x16_bf16 v[176:191], a[140:143], v[60:63], v[176:191]
	buffer_load_dwordx4 v[60:63], v4, s[20:23], s52 offen offset:3072 sc1
	s_waitcnt vmcnt(22)
	v_mfma_f32_32x32x16_bf16 v[144:159], a[144:147], v[64:67], v[144:159]
	v_mfma_f32_32x32x16_bf16 v[160:175], a[148:151], v[64:67], v[160:175]
	v_mfma_f32_32x32x16_bf16 v[176:191], a[152:155], v[64:67], v[176:191]
	buffer_load_dwordx4 v[64:67], v4, s[20:23], s53 offen offset:0 sc1
	s_waitcnt vmcnt(22)
	v_mfma_f32_32x32x16_bf16 v[144:159], a[156:159], v[68:71], v[144:159]
	v_mfma_f32_32x32x16_bf16 v[160:175], a[160:163], v[68:71], v[160:175]
	v_mfma_f32_32x32x16_bf16 v[176:191], a[164:167], v[68:71], v[176:191]
	buffer_load_dwordx4 v[68:71], v4, s[20:23], s53 offen offset:1024 sc1
	s_waitcnt vmcnt(22)
	v_mfma_f32_32x32x16_bf16 v[144:159], a[168:171], v[72:75], v[144:159]
	v_mfma_f32_32x32x16_bf16 v[160:175], a[172:175], v[72:75], v[160:175]
	v_mfma_f32_32x32x16_bf16 v[176:191], a[176:179], v[72:75], v[176:191]
	buffer_load_dwordx4 v[72:75], v4, s[20:23], s53 offen offset:2048 sc1
	s_waitcnt vmcnt(22)
	v_mfma_f32_32x32x16_bf16 v[144:159], a[180:183], v[76:79], v[144:159]
	v_mfma_f32_32x32x16_bf16 v[160:175], a[184:187], v[76:79], v[160:175]
	s_cmp_ge_u32 s48, 8
	s_cbranch_scc1 .Lt1_l15
	s_waitcnt vmcnt(7)
	s_barrier
	s_mov_b32 s59, s48
	v_mov_b32 v9, s48
	s_mov_b64 exec, s[60:61]
	buffer_store_dword v9, off, s[20:23], s38 sc1
	s_mov_b64 exec, -1
.Lt1_l15:
	v_mfma_f32_32x32x16_bf16 v[176:191], a[188:191], v[76:79], v[176:191]
	buffer_load_dwordx4 v[76:79], v4, s[20:23], s53 offen offset:3072 sc1
	s_waitcnt vmcnt(22)
	v_mfma_f32_32x32x16_bf16 v[144:159], a[192:195], v[80:83], v[144:159]
	v_mfma_f32_32x32x16_bf16 v[160:175], a[196:199], v[80:83], v[160:175]
	v_mfma_f32_32x32x16_bf16 v[176:191], a[200:203], v[80:83], v[176:191]
	buffer_load_dwordx4 v[80:83], v4, s[20:23], s54 offen offset:0 sc1
	s_waitcnt vmcnt(22)
	v_mfma_f32_32x32x16_bf16 v[144:159], a[204:207], v[84:87], v[144:159]
	v_mfma_f32_32x32x16_bf16 v[160:175], a[208:211], v[84:87], v[160:175]
	v_mfma_f32_32x32x16_bf16 v[176:191], a[212:215], v[84:87], v[176:191]
	buffer_load_dwordx4 v[84:87], v4, s[20:23], s54 offen offset:1024 sc1
	s_waitcnt vmcnt(22)
	v_mfma_f32_32x32x16_bf16 v[144:159], a[216:219], v[88:91], v[144:159]
	v_mfma_f32_32x32x16_bf16 v[160:175], a[220:223], v[88:91], v[160:175]
	v_mfma_f32_32x32x16_bf16 v[176:191], a[224:227], v[88:91], v[176:191]
	buffer_load_dwordx4 v[88:91], v4, s[20:23], s54 offen offset:2048 sc1
	s_waitcnt vmcnt(22)
	v_mfma_f32_32x32x16_bf16 v[144:159], a[228:231], v[92:95], v[144:159]
	v_mfma_f32_32x32x16_bf16 v[160:175], a[232:235], v[92:95], v[160:175]
	v_mfma_f32_32x32x16_bf16 v[176:191], a[236:239], v[92:95], v[176:191]
	buffer_load_dwordx4 v[92:95], v4, s[20:23], s54 offen offset:3072 sc1
	s_mov_b32 s36, s55
	s_add_u32 s55, s55, s37
	s_xor_b32 s55, s55, s41
	s_add_u32 s48, s48, 1
	s_cmp_eq_u32 s48, 64
	s_cbranch_scc1 .Lt1_out12
	s_and_b32 s18, s63, 15
	s_cbranch_scc0 .Lt1_l14m
	s_andn2_b32 s58, s70, 255
	s_add_u32 s58, s58, s18
.Lt1_n16:
	v_subrev_u32 v252, s58, v252
	v_cmp_lt_u32 vcc, 15, v252
	s_cbranch_vccz .Lt1_n17
	s_cmp_eq_u32 s69, 0
	s_cbranch_scc1 .Lt1_n17
	s_sub_u32 s69, s69, 1
	s_sleep 2
	buffer_load_dword v252, v14, s[20:23], 0 offen sc1
	s_waitcnt vmcnt(0)
	s_branch .Lt1_n16
.Lt1_n17:
	s_cmp_lt_u32 s63, 16
	s_cbranch_scc1 .Lt1_l14m
	buffer_inv sc1
.Lt1_l14m:
	s_waitcnt vmcnt(22)
	v_mfma_f32_32x32x16_bf16 v[96:111], a[0:3], v[16:19], 0
	s_add_u32 s50, s35, 0
	s_add_u32 s51, s35, 4096
	s_add_u32 s52, s35, 16384
	s_add_u32 s53, s35, 20480
	s_add_u32 s54, s35, 32768
	s_add_u32 s35, s35, 4194304
	s_and_b32 s35, s35, 268435455
	v_mfma_f32_32x32x16_bf16 v[112:127], a[4:7], v[16:19], 0
	ds_write_b128 v6, v[152:155] offset:6144
	v_mfma_f32_32x32x16_bf16 v[128:143], a[8:11], v[16:19], 0
	buffer_load_dwordx4 v[16:19], v4, s[20:23], s50 offen offset:0 sc1
	ds_write_b128 v6, v[156:159] offset:7168
	s_waitcnt vmcnt(22)
	v_mfma_f32_32x32x16_bf16 v[96:111], a[12:15], v[20:23], v[96:111]
	ds_write_b128 v6, v[168:171] offset:8192
	v_mfma_f32_32x32x16_bf16 v[112:127], a[16:19], v[20:23], v[112:127]
	ds_write_b128 v6, v[172:175] offset:9216
	v_mfma_f32_32x32x16_bf16 v[128:143], a[20:23], v[20:23], v[128:143]
	buffer_load_dwordx4 v[20:23], v4, s[20:23], s50 offen offset:1024 sc1
	ds_write_b128 v6, v[184:187] offset:10240
	s_waitcnt vmcnt(22)
	v_mfma_f32_32x32x16_bf16 v[96:111], a[24:27], v[24:27], v[96:111]
	ds_write_b128 v6, v[188:191] offset:11264
	v_mfma_f32_32x32x16_bf16 v[112:127], a[28:31], v[24:27], v[112:127]
	v_mfma_f32_32x32x16_bf16 v[128:143], a[32:35], v[24:27], v[128:143]
	buffer_load_dwordx4 v[24:27], v4, s[20:23], s50 offen offset:2048 sc1
	s_waitcnt vmcnt(22)
	v_mfma_f32_32x32x16_bf16 v[96:111], a[36:39], v[28:31], v[96:111]
	v_mfma_f32_32x32x16_bf16 v[112:127], a[40:43], v[28:31], v[112:127]
	s_waitcnt lgkmcnt(0)
	s_barrier
	s_sub_u32 s58, s48, 2
	s_max_i32 s58, s58, s59
	v_mov_b32 v9, s58
	s_mov_b64 exec, s[60:61]
	buffer_store_dword v9, off, s[20:23], s38 sc1
	s_mov_b64 exec, -1
	v_mfma_f32_32x32x16_bf16 v[128:143], a[44:47], v[28:31], v[128:143]
	buffer_load_dwordx4 v[28:31], v4, s[20:23], s50 offen offset:3072 sc1
	ds_read_b128 v[192:195], v7 offset:6144
	ds_read_b128 v[196:199], v7 offset:7168
	s_waitcnt vmcnt(22)
	v_mfma_f32_32x32x16_bf16 v[96:111], a[48:51], v[32:35], v[96:111]
	ds_read_b128 v[200:203], v7 offset:8192
	ds_read_b128 v[204:207], v7 offset:9216
	v_mfma_f32_32x32x16_bf16 v[112:127], a[52:55], v[32:35], v[112:127]
	ds_read_b128 v[208:211], v7 offset:10240
	ds_read_b128 v[212:215], v7 offset:11264
	v_mfma_f32_32x32x16_bf16 v[128:143], a[56:59], v[32:35], v[128:143]
	buffer_load_dwordx4 v[32:35], v4, s[20:23], s51 offen offset:0 sc1
	s_waitcnt vmcnt(22)
	v_mfma_f32_32x32x16_bf16 v[96:111], a[60:63], v[36:39], v[96:111]
	v_mfma_f32_32x32x16_bf16 v[112:127], a[64:67], v[36:39], v[112:127]
	s_waitcnt lgkmcnt(5)
	v_add_f32 v192, v192, v144
	v_add_f32 v193, v193, v145
	v_add_f32 v194, v194, v146
	v_add_f32 v195, v195, v147
	v_mfma_f32_32x32x16_bf16 v[128:143], a[68:71], v[36:39], v[128:143]
	buffer_load_dwordx4 v[36:39], v4, s[20:23], s51 offen offset:1024 sc1
	s_waitcnt lgkmcnt(4)
	v_add_f32 v196, v196, v148
	v_add_f32 v197, v197, v149
	v_add_f32 v198, v198, v150
	v_add_f32 v199, v199, v151
	s_waitcnt vmcnt(21)
	v_mfma_f32_32x32x16_bf16 v[96:111], a[72:75], v[40:43], v[96:111]
	v_cvt_pk_bf16_f32 v240, v192, v196
	v_cvt_pk_bf16_f32 v241, v193, v197
	v_cvt_pk_bf16_f32 v242, v194, v198
	v_cvt_pk_bf16_f32 v243, v195, v199
	v_mfma_f32_32x32x16_bf16 v[112:127], a[76:79], v[40:43], v[112:127]
	buffer_store_dwordx4 v[240:243], v5, s[20:23], s36 offen sc1
	s_waitcnt lgkmcnt(3)
	v_add_f32 v200, v200, v160
	v_add_f32 v201, v201, v161
	v_add_f32 v202, v202, v162
	v_mfma_f32_32x32x16_bf16 v[128:143], a[80:83], v[40:43], v[128:143]
	buffer_load_dwordx4 v[40:43], v4, s[20:23], s51 offen offset:2048 sc1
	v_add_f32 v203, v203, v163
	s_waitcnt lgkmcnt(2)
	v_add_f32 v204, v204, v164
	v_add_f32 v205, v205, v165
	v_add_f32 v206, v206, v166
	s_waitcnt vmcnt(21)
	v_mfma_f32_32x32x16_bf16 v[96:111], a[84:87], v[44:47], v[96:111]
	v_add_f32 v207, v207, v167
	v_cvt_pk_bf16_f32 v244, v200, v204
	v_cvt_pk_bf16_f32 v245, v201, v205
	v_cvt_pk_bf16_f32 v246, v202, v206
	v_mfma_f32_32x32x16_bf16 v[112:127], a[88:91], v[44:47], v[112:127]
	v_cvt_pk_bf16_f32 v247, v203, v207
	buffer_store_dwordx4 v[244:247], v11, s[20:23], s36 offen sc1
	s_waitcnt lgkmcnt(1)
	v_add_f32 v208, v208, v176
	v_add_f32 v209, v209, v177
	v_mfma_f32_32x32x16_bf16 v[128:143], a[92:95], v[44:47], v[128:143]
	buffer_load_dwordx4 v[44:47], v4, s[20:23], s51 offen offset:3072 sc1
	v_add_f32 v210, v210, v178
	v_add_f32 v211, v211, v179
	s_waitcnt lgkmcnt(0)
	v_add_f32 v212, v212, v180
	v_add_f32 v213, v213, v181
	s_waitcnt vmcnt(21)
	v_mfma_f32_32x32x16_bf16 v[96:111], a[96:99], v[48:51], v[96:111]
	v_add_f32 v214, v214, v182
	v_add_f32 v215, v215, v183
	v_cvt_pk_bf16_f32 v248, v208, v212
	v_cvt_pk_bf16_f32 v249, v209, v213
	v_mfma_f32_32x32x16_bf16 v[112:127], a[100:103], v[48:51], v[112:127]
	v_cvt_pk_bf16_f32 v250, v210, v214
	v_cvt_pk_bf16_f32 v251, v211, v215
	buffer_store_dwordx4 v[248:251], v12, s[20:23], s36 offen sc1
	v_mfma_f32_32x32x16_bf16 v[128:143], a[104:107], v[48:51], v[128:143]
	buffer_load_dwordx4 v[48:51], v4, s[20:23], s52 offen offset:0 sc1
	s_waitcnt vmcnt(22)
	v_mfma_f32_32x32x16_bf16 v[96:111], a[108:111], v[52:55], v[96:111]
	v_mfma_f32_32x32x16_bf16 v[112:127], a[112:115], v[52:55], v[112:127]
	v_mfma_f32_32x32x16_bf16 v[128:143], a[116:119], v[52:55], v[128:143]
	buffer_load_dwordx4 v[52:55], v4, s[20:23], s52 offen offset:1024 sc1
	s_waitcnt vmcnt(22)
	v_mfma_f32_32x32x16_bf16 v[96:111], a[120:123], v[56:59], v[96:111]
	v_mfma_f32_32x32x16_bf16 v[112:127], a[124:127], v[56:59], v[112:127]
	v_mfma_f32_32x32x16_bf16 v[128:143], a[128:131], v[56:59], v[128:143]
	buffer_load_dwordx4 v[56:59], v4, s[20:23], s52 offen offset:2048 sc1
	s_waitcnt vmcnt(22)
	v_mfma_f32_32x32x16_bf16 v[96:111], a[132:135], v[60:63], v[96:111]
	v_mfma_f32_32x32x16_bf16 v[112:127], a[136:139], v[60:63], v[112:127]
	v_mfma_f32_32x32x16_bf16 v[128:143], a[140:143], v[60:63], v[128:143]
	buffer_load_dwordx4 v[60:63], v4, s[20:23], s52 offen offset:3072 sc1
	s_waitcnt vmcnt(22)
	v_mfma_f32_32x32x16_bf16 v[96:111], a[144:147], v[64:67], v[96:111]
	v_mfma_f32_32x32x16_bf16 v[112:127], a[148:151], v[64:67], v[112:127]
	v_mfma_f32_32x32x16_bf16 v[128:143], a[152:155], v[64:67], v[128:143]
	buffer_load_dwordx4 v[64:67], v4, s[20:23], s53 offen offset:0 sc1
	s_waitcnt vmcnt(22)
	v_mfma_f32_32x32x16_bf16 v[96:111], a[156:159], v[68:71], v[96:111]
	v_mfma_f32_32x32x16_bf16 v[112:127], a[160:163], v[68:71], v[112:127]
	v_mfma_f32_32x32x16_bf16 v[128:143], a[164:167], v[68:71], v[128:143]
	buffer_load_dwordx4 v[68:71], v4, s[20:23], s53 offen offset:1024 sc1
	s_waitcnt vmcnt(22)
	v_mfma_f32_32x32x16_bf16 v[96:111], a[168:171], v[72:75], v[96:111]
	v_mfma_f32_32x32x16_bf16 v[112:127], a[172:175], v[72:75], v[112:127]
	v_mfma_f32_32x32x16_bf16 v[128:143], a[176:179], v[72:75], v[128:143]
	buffer_load_dwordx4 v[72:75], v4, s[20:23], s53 offen offset:2048 sc1
	s_waitcnt vmcnt(22)
	v_mfma_f32_32x32x16_bf16 v[96:111], a[180:183], v[76:79], v[96:111]
	v_mfma_f32_32x32x16_bf16 v[112:127], a[184:187], v[76:79], v[112:127]
	s_cmp_ge_u32 s48, 8
	s_cbranch_scc1 .Lt1_l18
	s_waitcnt vmcnt(7)
	s_barrier
	s_mov_b32 s59, s48
	v_mov_b32 v9, s48
	s_mov_b64 exec, s[60:61]
	buffer_store_dword v9, off, s[20:23], s38 sc1
	s_mov_b64 exec, -1
.Lt1_l18:
	v_mfma_f32_32x32x16_bf16 v[128:143], a[188:191], v[76:79], v[128:143]
	buffer_load_dwordx4 v[76:79], v4, s[20:23], s53 offen offset:3072 sc1
	s_waitcnt vmcnt(22)
	v_mfma_f32_32x32x16_bf16 v[96:111], a[192:195], v[80:83], v[96:111]
	v_mfma_f32_32x32x16_bf16 v[112:127], a[196:199], v[80:83], v[112:127]
	v_mfma_f32_32x32x16_bf16 v[128:143], a[200:203], v[80:83], v[128:143]
	buffer_load_dwordx4 v[80:83], v4, s[20:23], s54 offen offset:0 sc1
	s_waitcnt vmcnt(22)
	v_mfma_f32_32x32x16_bf16 v[96:111], a[204:207], v[84:87], v[96:111]
	v_mfma_f32_32x32x16_bf16 v[112:127], a[208:211], v[84:87], v[112:127]
	v_mfma_f32_32x32x16_bf16 v[128:143], a[212:215], v[84:87], v[128:143]
	buffer_load_dwordx4 v[84:87], v4, s[20:23], s54 offen offset:1024 sc1
	s_waitcnt vmcnt(22)
	v_mfma_f32_32x32x16_bf16 v[96:111], a[216:219], v[88:91], v[96:111]
	v_mfma_f32_32x32x16_bf16 v[112:127], a[220:223], v[88:91], v[112:127]
	v_mfma_f32_32x32x16_bf16 v[128:143], a[224:227], v[88:91], v[128:143]
	buffer_load_dwordx4 v[88:91], v4, s[20:23], s54 offen offset:2048 sc1
	s_waitcnt vmcnt(22)
	v_mfma_f32_32x32x16_bf16 v[96:111], a[228:231], v[92:95], v[96:111]
	v_mfma_f32_32x32x16_bf16 v[112:127], a[232:235], v[92:95], v[112:127]
	v_mfma_f32_32x32x16_bf16 v[128:143], a[236:239], v[92:95], v[128:143]
	buffer_load_dwordx4 v[92:95], v4, s[20:23], s54 offen offset:3072 sc1
	s_mov_b32 s36, s55
	s_add_u32 s55, s55, s37
	s_xor_b32 s55, s55, s41
	s_add_u32 s48, s48, 1
	s_cmp_lt_u32 s48, s65
	s_cbranch_scc1 .Lt1_seg13
	s_xor_b32 s35, s35, s45
.Lt1_seg19:
	s_waitcnt vmcnt(22)
	v_mfma_f32_32x32x16_bf16 v[144:159], a[0:3], v[16:19], 0
	s_add_u32 s50, s35, 0
	s_add_u32 s51, s35, 4096
	s_add_u32 s52, s35, 16384
	s_add_u32 s53, s35, 20480
	s_add_u32 s54, s35, 32768
	s_add_u32 s35, s35, 4194304
	s_and_b32 s35, s35, 268435455
	v_mfma_f32_32x32x16_bf16 v[160:175], a[4:7], v[16:19], 0
	ds_write_b128 v6, v[104:107] offset:0
	v_mfma_f32_32x32x16_bf16 v[176:191], a[8:11], v[16:19], 0
	buffer_load_dwordx4 v[16:19], v4, s[20:23], s50 offen offset:0
	ds_write_b128 v6, v[108:111] offset:1024
	s_waitcnt vmcnt(22)
	v_mfma_f32_32x32x16_bf16 v[144:159], a[12:15], v[20:23], v[144:159]
	ds_write_b128 v6, v[120:123] offset:2048
	v_mfma_f32_32x32x16_bf16 v[160:175], a[16:19], v[20:23], v[160:175]
	ds_write_b128 v6, v[124:127] offset:3072
	v_mfma_f32_32x32x16_bf16 v[176:191], a[20:23], v[20:23], v[176:191]
	buffer_load_dwordx4 v[20:23], v4, s[20:23], s50 offen offset:1024
	ds_write_b128 v6, v[136:139] offset:4096
	s_waitcnt vmcnt(22)
	v_mfma_f32_32x32x16_bf16 v[144:159], a[24:27], v[24:27], v[144:159]
	ds_write_b128 v6, v[140:143] offset:5120
	v_mfma_f32_32x32x16_bf16 v[160:175], a[28:31], v[24:27], v[160:175]
	v_mfma_f32_32x32x16_bf16 v[176:191], a[32:35], v[24:27], v[176:191]
	buffer_load_dwordx4 v[24:27], v4, s[20:23], s50 offen offset:2048
	s_waitcnt vmcnt(22)
	v_mfma_f32_32x32x16_bf16 v[144:159], a[36:39], v[28:31], v[144:159]
	v_mfma_f32_32x32x16_bf16 v[160:175], a[40:43], v[28:31], v[160:175]
	s_waitcnt lgkmcnt(0)
	s_barrier
	s_sub_u32 s58, s48, 2
	s_max_i32 s58, s58, s59
	v_mov_b32 v9, s58
	s_mov_b64 exec, s[60:61]
	buffer_store_dword v9, off, s[20:23], s38 sc1
	s_mov_b64 exec, -1
	v_mfma_f32_32x32x16_bf16 v[176:191], a[44:47], v[28:31], v[176:191]
	buffer_load_dwordx4 v[28:31], v4, s[20:23], s50 offen offset:3072
	ds_read_b128 v[192:195], v7 offset:0
	ds_read_b128 v[196:199], v7 offset:1024
	s_waitcnt vmcnt(22)
	v_mfma_f32_32x32x16_bf16 v[144:159], a[48:51], v[32:35], v[144:159]
	ds_read_b128 v[200:203], v7 offset:2048
	ds_read_b128 v[204:207], v7 offset:3072
	v_mfma_f32_32x32x16_bf16 v[160:175], a[52:55], v[32:35], v[160:175]
	ds_read_b128 v[208:211], v7 offset:4096
	ds_read_b128 v[212:215], v7 offset:5120
	v_mfma_f32_32x32x16_bf16 v[176:191], a[56:59], v[32:35], v[176:191]
	buffer_load_dwordx4 v[32:35], v4, s[20:23], s51 offen offset:0
	s_waitcnt vmcnt(22)
	v_mfma_f32_32x32x16_bf16 v[144:159], a[60:63], v[36:39], v[144:159]
	v_mfma_f32_32x32x16_bf16 v[160:175], a[64:67], v[36:39], v[160:175]
	s_waitcnt lgkmcnt(5)
	v_add_f32 v192, v192, v96
	v_add_f32 v193, v193, v97
	v_add_f32 v194, v194, v98
	v_add_f32 v195, v195, v99
	v_mfma_f32_32x32x16_bf16 v[176:191], a[68:71], v[36:39], v[176:191]
	buffer_load_dwordx4 v[36:39], v4, s[20:23], s51 offen offset:1024
	s_waitcnt lgkmcnt(4)
	v_add_f32 v196, v196, v100
	v_add_f32 v197, v197, v101
	v_add_f32 v198, v198, v102
	v_add_f32 v199, v199, v103
	s_waitcnt vmcnt(21)
	v_mfma_f32_32x32x16_bf16 v[144:159], a[72:75], v[40:43], v[144:159]
	v_cvt_pk_bf16_f32 v240, v192, v196
	v_cvt_pk_bf16_f32 v241, v193, v197
	v_cvt_pk_bf16_f32 v242, v194, v198
	v_cvt_pk_bf16_f32 v243, v195, v199
	v_mfma_f32_32x32x16_bf16 v[160:175], a[76:79], v[40:43], v[160:175]
	buffer_store_dwordx4 v[240:243], v5, s[20:23], s36 offen sc1
	s_waitcnt lgkmcnt(3)
	v_add_f32 v200, v200, v112
	v_add_f32 v201, v201, v113
	v_add_f32 v202, v202, v114
	v_mfma_f32_32x32x16_bf16 v[176:191], a[80:83], v[40:43], v[176:191]
	buffer_load_dwordx4 v[40:43], v4, s[20:23], s51 offen offset:2048
	v_add_f32 v203, v203, v115
	s_waitcnt lgkmcnt(2)
	v_add_f32 v204, v204, v116
	v_add_f32 v205, v205, v117
	v_add_f32 v206, v206, v118
	s_waitcnt vmcnt(21)
	v_mfma_f32_32x32x16_bf16 v[144:159], a[84:87], v[44:47], v[144:159]
	v_add_f32 v207, v207, v119
	v_cvt_pk_bf16_f32 v244, v200, v204
	v_cvt_pk_bf16_f32 v245, v201, v205
	v_cvt_pk_bf16_f32 v246, v202, v206
	v_mfma_f32_32x32x16_bf16 v[160:175], a[88:91], v[44:47], v[160:175]
	v_cvt_pk_bf16_f32 v247, v203, v207
	buffer_store_dwordx4 v[244:247], v11, s[20:23], s36 offen sc1
	s_waitcnt lgkmcnt(1)
	v_add_f32 v208, v208, v128
	v_add_f32 v209, v209, v129
	v_mfma_f32_32x32x16_bf16 v[176:191], a[92:95], v[44:47], v[176:191]
	buffer_load_dwordx4 v[44:47], v4, s[20:23], s51 offen offset:3072
	v_add_f32 v210, v210, v130
	v_add_f32 v211, v211, v131
	s_waitcnt lgkmcnt(0)
	v_add_f32 v212, v212, v132
	v_add_f32 v213, v213, v133
	s_waitcnt vmcnt(21)
	v_mfma_f32_32x32x16_bf16 v[144:159], a[96:99], v[48:51], v[144:159]
	v_add_f32 v214, v214, v134
	v_add_f32 v215, v215, v135
	v_cvt_pk_bf16_f32 v248, v208, v212
	v_cvt_pk_bf16_f32 v249, v209, v213
	v_mfma_f32_32x32x16_bf16 v[160:175], a[100:103], v[48:51], v[160:175]
	v_cvt_pk_bf16_f32 v250, v210, v214
	v_cvt_pk_bf16_f32 v251, v211, v215
	buffer_store_dwordx4 v[248:251], v12, s[20:23], s36 offen sc1
	v_mfma_f32_32x32x16_bf16 v[176:191], a[104:107], v[48:51], v[176:191]
	buffer_load_dwordx4 v[48:51], v4, s[20:23], s52 offen offset:0
	s_waitcnt vmcnt(22)
	v_mfma_f32_32x32x16_bf16 v[144:159], a[108:111], v[52:55], v[144:159]
	v_mfma_f32_32x32x16_bf16 v[160:175], a[112:115], v[52:55], v[160:175]
	v_mfma_f32_32x32x16_bf16 v[176:191], a[116:119], v[52:55], v[176:191]
	buffer_load_dwordx4 v[52:55], v4, s[20:23], s52 offen offset:1024
	s_waitcnt vmcnt(22)
	v_mfma_f32_32x32x16_bf16 v[144:159], a[120:123], v[56:59], v[144:159]
	v_mfma_f32_32x32x16_bf16 v[160:175], a[124:127], v[56:59], v[160:175]
	v_mfma_f32_32x32x16_bf16 v[176:191], a[128:131], v[56:59], v[176:191]
	buffer_load_dwordx4 v[56:59], v4, s[20:23], s52 offen offset:2048
	s_waitcnt vmcnt(22)
	v_mfma_f32_32x32x16_bf16 v[144:159], a[132:135], v[60:63], v[144:159]
	v_mfma_f32_32x32x16_bf16 v[160:175], a[136:139], v[60:63], v[160:175]
	v_mfma_f32_32x32x16_bf16 v[176:191], a[140:143], v[60:63], v[176:191]
	buffer_load_dwordx4 v[60:63], v4, s[20:23], s52 offen offset:3072
	s_waitcnt vmcnt(22)
	v_mfma_f32_32x32x16_bf16 v[144:159], a[144:147], v[64:67], v[144:159]
	v_mfma_f32_32x32x16_bf16 v[160:175], a[148:151], v[64:67], v[160:175]
	v_mfma_f32_32x32x16_bf16 v[176:191], a[152:155], v[64:67], v[176:191]
	buffer_load_dwordx4 v[64:67], v4, s[20:23], s53 offen offset:0
	s_waitcnt vmcnt(22)
	v_mfma_f32_32x32x16_bf16 v[144:159], a[156:159], v[68:71], v[144:159]
	v_mfma_f32_32x32x16_bf16 v[160:175], a[160:163], v[68:71], v[160:175]
	v_mfma_f32_32x32x16_bf16 v[176:191], a[164:167], v[68:71], v[176:191]
	buffer_load_dwordx4 v[68:71], v4, s[20:23], s53 offen offset:1024
	s_waitcnt vmcnt(22)
	v_mfma_f32_32x32x16_bf16 v[144:159], a[168:171], v[72:75], v[144:159]
	v_mfma_f32_32x32x16_bf16 v[160:175], a[172:175], v[72:75], v[160:175]
	v_mfma_f32_32x32x16_bf16 v[176:191], a[176:179], v[72:75], v[176:191]
	buffer_load_dwordx4 v[72:75], v4, s[20:23], s53 offen offset:2048
	s_waitcnt vmcnt(22)
	v_mfma_f32_32x32x16_bf16 v[144:159], a[180:183], v[76:79], v[144:159]
	v_mfma_f32_32x32x16_bf16 v[160:175], a[184:187], v[76:79], v[160:175]
	v_mfma_f32_32x32x16_bf16 v[176:191], a[188:191], v[76:79], v[176:191]
	buffer_load_dwordx4 v[76:79], v4, s[20:23], s53 offen offset:3072
	s_waitcnt vmcnt(22)
	v_mfma_f32_32x32x16_bf16 v[144:159], a[192:195], v[80:83], v[144:159]
	v_mfma_f32_32x32x16_bf16 v[160:175], a[196:199], v[80:83], v[160:175]
	v_mfma_f32_32x32x16_bf16 v[176:191], a[200:203], v[80:83], v[176:191]
	buffer_load_dwordx4 v[80:83], v4, s[20:23], s54 offen offset:0
	s_waitcnt vmcnt(22)
	v_mfma_f32_32x32x16_bf16 v[144:159], a[204:207], v[84:87], v[144:159]
	v_mfma_f32_32x32x16_bf16 v[160:175], a[208:211], v[84:87], v[160:175]
	v_mfma_f32_32x32x16_bf16 v[176:191], a[212:215], v[84:87], v[176:191]
	buffer_load_dwordx4 v[84:87], v4, s[20:23], s54 offen offset:1024
	s_waitcnt vmcnt(22)
	v_mfma_f32_32x32x16_bf16 v[144:159], a[216:219], v[88:91], v[144:159]
	v_mfma_f32_32x32x16_bf16 v[160:175], a[220:223], v[88:91], v[160:175]
	v_mfma_f32_32x32x16_bf16 v[176:191], a[224:227], v[88:91], v[176:191]
	buffer_load_dwordx4 v[88:91], v4, s[20:23], s54 offen offset:2048
	s_waitcnt vmcnt(22)
	v_mfma_f32_32x32x16_bf16 v[144:159], a[228:231], v[92:95], v[144:159]
	v_mfma_f32_32x32x16_bf16 v[160:175], a[232:235], v[92:95], v[160:175]
	v_mfma_f32_32x32x16_bf16 v[176:191], a[236:239], v[92:95], v[176:191]
	buffer_load_dwordx4 v[92:95], v4, s[20:23], s54 offen offset:3072
	s_mov_b32 s36, s55
	s_add_u32 s55, s55, s37
	s_xor_b32 s55, s55, s41
	s_add_u32 s48, s48, 1
	s_cmp_eq_u32 s48, 64
	s_cbranch_scc1 .Lt1_out12
	s_waitcnt vmcnt(22)
	v_mfma_f32_32x32x16_bf16 v[96:111], a[0:3], v[16:19], 0
	s_add_u32 s50, s35, 0
	s_add_u32 s51, s35, 4096
	s_add_u32 s52, s35, 16384
	s_add_u32 s53, s35, 20480
	s_add_u32 s54, s35, 32768
	s_add_u32 s35, s35, 4194304
	s_and_b32 s35, s35, 268435455
	v_mfma_f32_32x32x16_bf16 v[112:127], a[4:7], v[16:19], 0
	ds_write_b128 v6, v[152:155] offset:6144
	v_mfma_f32_32x32x16_bf16 v[128:143], a[8:11], v[16:19], 0
	buffer_load_dwordx4 v[16:19], v4, s[20:23], s50 offen offset:0
	ds_write_b128 v6, v[156:159] offset:7168
	s_waitcnt vmcnt(22)
	v_mfma_f32_32x32x16_bf16 v[96:111], a[12:15], v[20:23], v[96:111]
	ds_write_b128 v6, v[168:171] offset:8192
	v_mfma_f32_32x32x16_bf16 v[112:127], a[16:19], v[20:23], v[112:127]
	ds_write_b128 v6, v[172:175] offset:9216
	v_mfma_f32_32x32x16_bf16 v[128:143], a[20:23], v[20:23], v[128:143]
	buffer_load_dwordx4 v[20:23], v4, s[20:23], s50 offen offset:1024
	ds_write_b128 v6, v[184:187] offset:10240
	s_waitcnt vmcnt(22)
	v_mfma_f32_32x32x16_bf16 v[96:111], a[24:27], v[24:27], v[96:111]
	ds_write_b128 v6, v[188:191] offset:11264
	v_mfma_f32_32x32x16_bf16 v[112:127], a[28:31], v[24:27], v[112:127]
	v_mfma_f32_32x32x16_bf16 v[128:143], a[32:35], v[24:27], v[128:143]
	buffer_load_dwordx4 v[24:27], v4, s[20:23], s50 offen offset:2048
	s_waitcnt vmcnt(22)
	v_mfma_f32_32x32x16_bf16 v[96:111], a[36:39], v[28:31], v[96:111]
	v_mfma_f32_32x32x16_bf16 v[112:127], a[40:43], v[28:31], v[112:127]
	s_waitcnt lgkmcnt(0)
	s_barrier
	s_sub_u32 s58, s48, 2
	s_max_i32 s58, s58, s59
	v_mov_b32 v9, s58
	s_mov_b64 exec, s[60:61]
	buffer_store_dword v9, off, s[20:23], s38 sc1
	s_mov_b64 exec, -1
	v_mfma_f32_32x32x16_bf16 v[128:143], a[44:47], v[28:31], v[128:143]
	buffer_load_dwordx4 v[28:31], v4, s[20:23], s50 offen offset:3072
	ds_read_b128 v[192:195], v7 offset:6144
	ds_read_b128 v[196:199], v7 offset:7168
	s_waitcnt vmcnt(22)
	v_mfma_f32_32x32x16_bf16 v[96:111], a[48:51], v[32:35], v[96:111]
	ds_read_b128 v[200:203], v7 offset:8192
	ds_read_b128 v[204:207], v7 offset:9216
	v_mfma_f32_32x32x16_bf16 v[112:127], a[52:55], v[32:35], v[112:127]
	ds_read_b128 v[208:211], v7 offset:10240
	ds_read_b128 v[212:215], v7 offset:11264
	v_mfma_f32_32x32x16_bf16 v[128:143], a[56:59], v[32:35], v[128:143]
	buffer_load_dwordx4 v[32:35], v4, s[20:23], s51 offen offset:0
	s_waitcnt vmcnt(22)
	v_mfma_f32_32x32x16_bf16 v[96:111], a[60:63], v[36:39], v[96:111]
	v_mfma_f32_32x32x16_bf16 v[112:127], a[64:67], v[36:39], v[112:127]
	s_waitcnt lgkmcnt(5)
	v_add_f32 v192, v192, v144
	v_add_f32 v193, v193, v145
	v_add_f32 v194, v194, v146
	v_add_f32 v195, v195, v147
	v_mfma_f32_32x32x16_bf16 v[128:143], a[68:71], v[36:39], v[128:143]
	buffer_load_dwordx4 v[36:39], v4, s[20:23], s51 offen offset:1024
	s_waitcnt lgkmcnt(4)
	v_add_f32 v196, v196, v148
	v_add_f32 v197, v197, v149
	v_add_f32 v198, v198, v150
	v_add_f32 v199, v199, v151
	s_waitcnt vmcnt(21)
	v_mfma_f32_32x32x16_bf16 v[96:111], a[72:75], v[40:43], v[96:111]
	v_cvt_pk_bf16_f32 v240, v192, v196
	v_cvt_pk_bf16_f32 v241, v193, v197
	v_cvt_pk_bf16_f32 v242, v194, v198
	v_cvt_pk_bf16_f32 v243, v195, v199
	v_mfma_f32_32x32x16_bf16 v[112:127], a[76:79], v[40:43], v[112:127]
	buffer_store_dwordx4 v[240:243], v5, s[20:23], s36 offen sc1
	s_waitcnt lgkmcnt(3)
	v_add_f32 v200, v200, v160
	v_add_f32 v201, v201, v161
	v_add_f32 v202, v202, v162
	v_mfma_f32_32x32x16_bf16 v[128:143], a[80:83], v[40:43], v[128:143]
	buffer_load_dwordx4 v[40:43], v4, s[20:23], s51 offen offset:2048
	v_add_f32 v203, v203, v163
	s_waitcnt lgkmcnt(2)
	v_add_f32 v204, v204, v164
	v_add_f32 v205, v205, v165
	v_add_f32 v206, v206, v166
	s_waitcnt vmcnt(21)
	v_mfma_f32_32x32x16_bf16 v[96:111], a[84:87], v[44:47], v[96:111]
	v_add_f32 v207, v207, v167
	v_cvt_pk_bf16_f32 v244, v200, v204
	v_cvt_pk_bf16_f32 v245, v201, v205
	v_cvt_pk_bf16_f32 v246, v202, v206
	v_mfma_f32_32x32x16_bf16 v[112:127], a[88:91], v[44:47], v[112:127]
	v_cvt_pk_bf16_f32 v247, v203, v207
	buffer_store_dwordx4 v[244:247], v11, s[20:23], s36 offen sc1
	s_waitcnt lgkmcnt(1)
	v_add_f32 v208, v208, v176
	v_add_f32 v209, v209, v177
	v_mfma_f32_32x32x16_bf16 v[128:143], a[92:95], v[44:47], v[128:143]
	buffer_load_dwordx4 v[44:47], v4, s[20:23], s51 offen offset:3072
	v_add_f32 v210, v210, v178
	v_add_f32 v211, v211, v179
	s_waitcnt lgkmcnt(0)
	v_add_f32 v212, v212, v180
	v_add_f32 v213, v213, v181
	s_waitcnt vmcnt(21)
	v_mfma_f32_32x32x16_bf16 v[96:111], a[96:99], v[48:51], v[96:111]
	v_add_f32 v214, v214, v182
	v_add_f32 v215, v215, v183
	v_cvt_pk_bf16_f32 v248, v208, v212
	v_cvt_pk_bf16_f32 v249, v209, v213
	v_mfma_f32_32x32x16_bf16 v[112:127], a[100:103], v[48:51], v[112:127]
	v_cvt_pk_bf16_f32 v250, v210, v214
	v_cvt_pk_bf16_f32 v251, v211, v215
	buffer_store_dwordx4 v[248:251], v12, s[20:23], s36 offen sc1
	v_mfma_f32_32x32x16_bf16 v[128:143], a[104:107], v[48:51], v[128:143]
	buffer_load_dwordx4 v[48:51], v4, s[20:23], s52 offen offset:0
	s_waitcnt vmcnt(22)
	v_mfma_f32_32x32x16_bf16 v[96:111], a[108:111], v[52:55], v[96:111]
	v_mfma_f32_32x32x16_bf16 v[112:127], a[112:115], v[52:55], v[112:127]
	v_mfma_f32_32x32x16_bf16 v[128:143], a[116:119], v[52:55], v[128:143]
	buffer_load_dwordx4 v[52:55], v4, s[20:23], s52 offen offset:1024
	s_waitcnt vmcnt(22)
	v_mfma_f32_32x32x16_bf16 v[96:111], a[120:123], v[56:59], v[96:111]
	v_mfma_f32_32x32x16_bf16 v[112:127], a[124:127], v[56:59], v[112:127]
	v_mfma_f32_32x32x16_bf16 v[128:143], a[128:131], v[56:59], v[128:143]
	buffer_load_dwordx4 v[56:59], v4, s[20:23], s52 offen offset:2048
	s_waitcnt vmcnt(22)
	v_mfma_f32_32x32x16_bf16 v[96:111], a[132:135], v[60:63], v[96:111]
	v_mfma_f32_32x32x16_bf16 v[112:127], a[136:139], v[60:63], v[112:127]
	v_mfma_f32_32x32x16_bf16 v[128:143], a[140:143], v[60:63], v[128:143]
	buffer_load_dwordx4 v[60:63], v4, s[20:23], s52 offen offset:3072
	s_waitcnt vmcnt(22)
	v_mfma_f32_32x32x16_bf16 v[96:111], a[144:147], v[64:67], v[96:111]
	v_mfma_f32_32x32x16_bf16 v[112:127], a[148:151], v[64:67], v[112:127]
	v_mfma_f32_32x32x16_bf16 v[128:143], a[152:155], v[64:67], v[128:143]
	buffer_load_dwordx4 v[64:67], v4, s[20:23], s53 offen offset:0
	s_waitcnt vmcnt(22)
	v_mfma_f32_32x32x16_bf16 v[96:111], a[156:159], v[68:71], v[96:111]
	v_mfma_f32_32x32x16_bf16 v[112:127], a[160:163], v[68:71], v[112:127]
	v_mfma_f32_32x32x16_bf16 v[128:143], a[164:167], v[68:71], v[128:143]
	buffer_load_dwordx4 v[68:71], v4, s[20:23], s53 offen offset:1024
	s_waitcnt vmcnt(22)
	v_mfma_f32_32x32x16_bf16 v[96:111], a[168:171], v[72:75], v[96:111]
	v_mfma_f32_32x32x16_bf16 v[112:127], a[172:175], v[72:75], v[112:127]
	v_mfma_f32_32x32x16_bf16 v[128:143], a[176:179], v[72:75], v[128:143]
	buffer_load_dwordx4 v[72:75], v4, s[20:23], s53 offen offset:2048
	s_waitcnt vmcnt(22)
	v_mfma_f32_32x32x16_bf16 v[96:111], a[180:183], v[76:79], v[96:111]
	v_mfma_f32_32x32x16_bf16 v[112:127], a[184:187], v[76:79], v[112:127]
	v_mfma_f32_32x32x16_bf16 v[128:143], a[188:191], v[76:79], v[128:143]
	buffer_load_dwordx4 v[76:79], v4, s[20:23], s53 offen offset:3072
	s_waitcnt vmcnt(22)
	v_mfma_f32_32x32x16_bf16 v[96:111], a[192:195], v[80:83], v[96:111]
	v_mfma_f32_32x32x16_bf16 v[112:127], a[196:199], v[80:83], v[112:127]
	v_mfma_f32_32x32x16_bf16 v[128:143], a[200:203], v[80:83], v[128:143]
	buffer_load_dwordx4 v[80:83], v4, s[20:23], s54 offen offset:0
	s_waitcnt vmcnt(22)
	v_mfma_f32_32x32x16_bf16 v[96:111], a[204:207], v[84:87], v[96:111]
	v_mfma_f32_32x32x16_bf16 v[112:127], a[208:211], v[84:87], v[112:127]
	v_mfma_f32_32x32x16_bf16 v[128:143], a[212:215], v[84:87], v[128:143]
	buffer_load_dwordx4 v[84:87], v4, s[20:23], s54 offen offset:1024
	s_waitcnt vmcnt(22)
	v_mfma_f32_32x32x16_bf16 v[96:111], a[216:219], v[88:91], v[96:111]
	v_mfma_f32_32x32x16_bf16 v[112:127], a[220:223], v[88:91], v[112:127]
	v_mfma_f32_32x32x16_bf16 v[128:143], a[224:227], v[88:91], v[128:143]
	buffer_load_dwordx4 v[88:91], v4, s[20:23], s54 offen offset:2048
	s_waitcnt vmcnt(22)
	v_mfma_f32_32x32x16_bf16 v[96:111], a[228:231], v[92:95], v[96:111]
	v_mfma_f32_32x32x16_bf16 v[112:127], a[232:235], v[92:95], v[112:127]
	v_mfma_f32_32x32x16_bf16 v[128:143], a[236:239], v[92:95], v[128:143]
	buffer_load_dwordx4 v[92:95], v4, s[20:23], s54 offen offset:3072
	s_mov_b32 s36, s55
	s_add_u32 s55, s55, s37
	s_xor_b32 s55, s55, s41
	s_add_u32 s48, s48, 1
	s_branch .Lt1_seg19
.Lt1_out12:
	ds_write_b128 v6, v[152:155] offset:6144
	ds_write_b128 v6, v[156:159] offset:7168
	ds_write_b128 v6, v[168:171] offset:8192
	ds_write_b128 v6, v[172:175] offset:9216
	s_nop 0
	ds_write_b128 v6, v[184:187] offset:10240
	ds_write_b128 v6, v[188:191] offset:11264
	s_waitcnt lgkmcnt(0)
	s_barrier
	ds_read_b128 v[192:195], v7 offset:6144
	ds_read_b128 v[196:199], v7 offset:7168
	ds_read_b128 v[200:203], v7 offset:8192
	ds_read_b128 v[204:207], v7 offset:9216
	ds_read_b128 v[208:211], v7 offset:10240
	ds_read_b128 v[212:215], v7 offset:11264
	s_waitcnt lgkmcnt(5)
	v_add_f32 v192, v192, v144
	v_add_f32 v193, v193, v145
	v_add_f32 v194, v194, v146
	v_add_f32 v195, v195, v147
	s_waitcnt lgkmcnt(4)
	v_add_f32 v196, v196, v148
	v_add_f32 v197, v197, v149
	v_add_f32 v198, v198, v150
	v_add_f32 v199, v199, v151
	v_cvt_pk_bf16_f32 v240, v192, v196
	v_cvt_pk_bf16_f32 v241, v193, v197
	v_cvt_pk_bf16_f32 v242, v194, v198
	v_cvt_pk_bf16_f32 v243, v195, v199
	buffer_store_dwordx4 v[240:243], v5, s[20:23], s36 offen sc1
	s_waitcnt lgkmcnt(3)
	v_add_f32 v200, v200, v160
	v_add_f32 v201, v201, v161
	v_add_f32 v202, v202, v162
	v_add_f32 v203, v203, v163
	s_waitcnt lgkmcnt(2)
	v_add_f32 v204, v204, v164
	v_add_f32 v205, v205, v165
	v_add_f32 v206, v206, v166
	v_add_f32 v207, v207, v167
	v_cvt_pk_bf16_f32 v244, v200, v204
	v_cvt_pk_bf16_f32 v245, v201, v205
	v_cvt_pk_bf16_f32 v246, v202, v206
	v_cvt_pk_bf16_f32 v247, v203, v207
	buffer_store_dwordx4 v[244:247], v11, s[20:23], s36 offen sc1
	s_waitcnt lgkmcnt(1)
	v_add_f32 v208, v208, v176
	v_add_f32 v209, v209, v177
	v_add_f32 v210, v210, v178
	v_add_f32 v211, v211, v179
	s_waitcnt lgkmcnt(0)
	v_add_f32 v212, v212, v180
	v_add_f32 v213, v213, v181
	v_add_f32 v214, v214, v182
	v_add_f32 v215, v215, v183
	v_cvt_pk_bf16_f32 v248, v208, v212
	v_cvt_pk_bf16_f32 v249, v209, v213
	v_cvt_pk_bf16_f32 v250, v210, v214
	v_cvt_pk_bf16_f32 v251, v211, v215
	buffer_store_dwordx4 v[248:251], v12, s[20:23], s36 offen sc1
	s_waitcnt vmcnt(0)
	s_barrier
	v_mov_b32 v9, 64
	s_mov_b64 exec, s[60:61]
	buffer_store_dword v9, off, s[20:23], s38 sc1
	s_mov_b64 exec, -1
	s_branch .Lt1_end11
.Lt1_k10:
	s_add_u32 s55, s36, s37
	s_xor_b32 s55, s55, s41
	s_lshl_b32 s65, s42, 7
	s_or_b32 s65, s65, 31
.Lt1_seg22:
	s_and_b32 s58, s48, 7
	s_add_u32 s18, s48, 4
	s_cmp_eq_u32 s18, s65
	s_cselect_b32 s58, 5, s58
	s_lshl2_add_u32 s58, s58, s40
	s_mov_b32 s63, 0
	s_cmp_lg_u32 s58, 20
	s_cbranch_scc1 .Lt1_l23
	s_lshr_b32 s63, s48, 3
	s_add_u32 s63, s63, 2
	s_min_u32 s63, s63, 8
	s_cmp_eq_u32 s18, s65
	s_cselect_b32 s63, 24, s63
	buffer_load_dword v252, v14, s[20:23], 0 offen sc1
.Lt1_l23:
	s_waitcnt vmcnt(19)
	v_mfma_f32_32x32x16_bf16 v[144:159], a[0:3], v[16:19], 0
	s_add_u32 s50, s35, 0
	s_add_u32 s51, s35, 4096
	s_add_u32 s52, s35, 16384
	s_add_u32 s53, s35, 20480
	s_add_u32 s54, s35, 32768
	s_add_u32 s35, s35, 4194304
	s_and_b32 s35, s35, 268435455
	v_mfma_f32_32x32x16_bf16 v[160:175], a[4:7], v[16:19], 0
	ds_write_b128 v6, v[104:107] offset:0
	v_mfma_f32_32x32x16_bf16 v[176:191], a[8:11], v[16:19], 0
	buffer_load_dwordx4 v[16:19], v4, s[20:23], s50 offen offset:0 sc1
	ds_write_b128 v6, v[108:111] offset:1024
	s_waitcnt vmcnt(19)
	v_mfma_f32_32x32x16_bf16 v[144:159], a[12:15], v[20:23], v[144:159]
	ds_write_b128 v6, v[120:123] offset:2048
	v_mfma_f32_32x32x16_bf16 v[160:175], a[16:19], v[20:23], v[160:175]
	ds_write_b128 v6, v[124:127] offset:3072
	v_mfma_f32_32x32x16_bf16 v[176:191], a[20:23], v[20:23], v[176:191]
	buffer_load_dwordx4 v[20:23], v4, s[20:23], s50 offen offset:1024 sc1
	ds_write_b128 v6, v[136:139] offset:4096
	s_waitcnt vmcnt(19)
	v_mfma_f32_32x32x16_bf16 v[144:159], a[24:27], v[24:27], v[144:159]
	ds_write_b128 v6, v[140:143] offset:5120
	v_mfma_f32_32x32x16_bf16 v[160:175], a[28:31], v[24:27], v[160:175]
	v_mfma_f32_32x32x16_bf16 v[176:191], a[32:35], v[24:27], v[176:191]
	buffer_load_dwordx4 v[24:27], v4, s[20:23], s50 offen offset:2048 sc1
	s_waitcnt vmcnt(19)
	v_mfma_f32_32x32x16_bf16 v[144:159], a[36:39], v[28:31], v[144:159]
	v_mfma_f32_32x32x16_bf16 v[160:175], a[40:43], v[28:31], v[160:175]
	s_waitcnt lgkmcnt(0)
	s_barrier
	s_sub_u32 s58, s48, 2
	s_max_i32 s58, s58, 0
	v_mov_b32 v9, s58
	s_mov_b64 exec, s[60:61]
	buffer_store_dword v9, off, s[20:23], s38 sc1
	s_mov_b64 exec, -1
	v_mfma_f32_32x32x16_bf16 v[176:191], a[44:47], v[28:31], v[176:191]
	buffer_load_dwordx4 v[28:31], v4, s[20:23], s50 offen offset:3072 sc1
	ds_read_b128 v[192:195], v7 offset:0
	ds_read_b128 v[196:199], v7 offset:1024
	s_waitcnt vmcnt(19)
	v_mfma_f32_32x32x16_bf16 v[144:159], a[48:51], v[32:35], v[144:159]
	ds_read_b128 v[200:203], v7 offset:2048
	ds_read_b128 v[204:207], v7 offset:3072
	v_mfma_f32_32x32x16_bf16 v[160:175], a[52:55], v[32:35], v[160:175]
	ds_read_b128 v[208:211], v7 offset:4096
	ds_read_b128 v[212:215], v7 offset:5120
	v_mfma_f32_32x32x16_bf16 v[176:191], a[56:59], v[32:35], v[176:191]
	buffer_load_dwordx4 v[32:35], v4, s[20:23], s51 offen offset:0 sc1
	s_waitcnt vmcnt(19)
	v_mfma_f32_32x32x16_bf16 v[144:159], a[60:63], v[36:39], v[144:159]
	v_mfma_f32_32x32x16_bf16 v[160:175], a[64:67], v[36:39], v[160:175]
	s_waitcnt lgkmcnt(5)
	v_add_f32 v192, v192, v96
	v_add_f32 v193, v193, v97
	v_add_f32 v194, v194, v98
	v_add_f32 v195, v195, v99
	s_waitcnt lgkmcnt(4)
	v_add_f32 v196, v196, v100
	v_mfma_f32_32x32x16_bf16 v[176:191], a[68:71], v[36:39], v[176:191]
	buffer_load_dwordx4 v[36:39], v4, s[20:23], s51 offen offset:1024 sc1
	v_add_f32 v197, v197, v101
	v_add_f32 v198, v198, v102
	v_add_f32 v199, v199, v103
	v_cvt_pk_bf16_f32 v240, v192, v196
	v_cvt_pk_bf16_f32 v241, v193, v197
	s_waitcnt vmcnt(19)
	v_mfma_f32_32x32x16_bf16 v[144:159], a[72:75], v[40:43], v[144:159]
	v_cvt_pk_bf16_f32 v242, v194, v198
	v_cvt_pk_bf16_f32 v243, v195, v199
	v_lshlrev_b32 v192, 16, v240
	v_and_b32 v196, 0xffff0000, v240
	v_lshlrev_b32 v193, 16, v241
	v_mfma_f32_32x32x16_bf16 v[160:175], a[76:79], v[40:43], v[160:175]
	v_and_b32 v197, 0xffff0000, v241
	v_lshlrev_b32 v194, 16, v242
	v_and_b32 v198, 0xffff0000, v242
	v_lshlrev_b32 v195, 16, v243
	v_and_b32 v199, 0xffff0000, v243
	v_mfma_f32_32x32x16_bf16 v[176:191], a[80:83], v[40:43], v[176:191]
	buffer_load_dwordx4 v[40:43], v4, s[20:23], s51 offen offset:2048 sc1
	v_add_f32 v192, v192, v216
	v_add_f32 v193, v193, v217
	v_add_f32 v194, v194, v218
	v_add_f32 v195, v195, v219
	v_add_f32 v196, v196, v220
	s_waitcnt vmcnt(19)
	v_mfma_f32_32x32x16_bf16 v[144:159], a[84:87], v[44:47], v[144:159]
	v_add_f32 v197, v197, v221
	v_add_f32 v198, v198, v222
	v_add_f32 v199, v199, v223
	v_max_f32 v192, 0, v192
	v_max_f32 v193, 0, v193
	v_mfma_f32_32x32x16_bf16 v[160:175], a[88:91], v[44:47], v[160:175]
	v_max_f32 v194, 0, v194
	v_max_f32 v195, 0, v195
	v_max_f32 v196, 0, v196
	v_max_f32 v197, 0, v197
	v_max_f32 v198, 0, v198
	v_mfma_f32_32x32x16_bf16 v[176:191], a[92:95], v[44:47], v[176:191]
	buffer_load_dwordx4 v[44:47], v4, s[20:23], s51 offen offset:3072 sc1
	v_max_f32 v199, 0, v199
	v_cvt_pk_bf16_f32 v240, v192, v196
	v_cvt_pk_bf16_f32 v241, v193, v197
	v_cvt_pk_bf16_f32 v242, v194, v198
	v_cvt_pk_bf16_f32 v243, v195, v199
	s_waitcnt vmcnt(19)
	v_mfma_f32_32x32x16_bf16 v[144:159], a[96:99], v[48:51], v[144:159]
	v_lshlrev_b32 v192, 16, v240
	v_and_b32 v196, 0xffff0000, v240
	v_lshlrev_b32 v193, 16, v241
	v_and_b32 v197, 0xffff0000, v241
	v_lshlrev_b32 v194, 16, v242
	v_mfma_f32_32x32x16_bf16 v[160:175], a[100:103], v[48:51], v[160:175]
	v_and_b32 v198, 0xffff0000, v242
	v_lshlrev_b32 v195, 16, v243
	v_and_b32 v199, 0xffff0000, v243
	v_mul_f32 v192, v192, v192
	v_mul_f32 v193, v193, v193
	v_mfma_f32_32x32x16_bf16 v[176:191], a[104:107], v[48:51], v[176:191]
	buffer_load_dwordx4 v[48:51], v4, s[20:23], s52 offen offset:0 sc1
	v_mul_f32 v194, v194, v194
	v_mul_f32 v195, v195, v195
	v_mul_f32 v196, v196, v196
	v_mul_f32 v197, v197, v197
	v_mul_f32 v198, v198, v198
	s_waitcnt vmcnt(19)
	v_mfma_f32_32x32x16_bf16 v[144:159], a[108:111], v[52:55], v[144:159]
	v_mul_f32 v199, v199, v199
	v_cvt_pk_bf16_f32 v240, v192, v196
	v_cvt_pk_bf16_f32 v241, v193, v197
	v_cvt_pk_bf16_f32 v242, v194, v198
	v_cvt_pk_bf16_f32 v243, v195, v199
	v_mfma_f32_32x32x16_bf16 v[160:175], a[112:115], v[52:55], v[160:175]
	buffer_store_dwordx4 v[240:243], v5, s[20:23], s36 offen sc1
	s_waitcnt lgkmcnt(3)
	v_add_f32 v200, v200, v112
	v_add_f32 v201, v201, v113
	v_add_f32 v202, v202, v114
	v_add_f32 v203, v203, v115
	v_mfma_f32_32x32x16_bf16 v[176:191], a[116:119], v[52:55], v[176:191]
	buffer_load_dwordx4 v[52:55], v4, s[20:23], s52 offen offset:1024 sc1
	s_waitcnt lgkmcnt(2)
	v_add_f32 v204, v204, v116
	v_add_f32 v205, v205, v117
	v_add_f32 v206, v206, v118
	v_add_f32 v207, v207, v119
	v_cvt_pk_bf16_f32 v244, v200, v204
	s_waitcnt vmcnt(20)
	v_mfma_f32_32x32x16_bf16 v[144:159], a[120:123], v[56:59], v[144:159]
	v_cvt_pk_bf16_f32 v245, v201, v205
	v_cvt_pk_bf16_f32 v246, v202, v206
	v_cvt_pk_bf16_f32 v247, v203, v207
	v_lshlrev_b32 v200, 16, v244
	v_and_b32 v204, 0xffff0000, v244
	v_mfma_f32_32x32x16_bf16 v[160:175], a[124:127], v[56:59], v[160:175]
	v_lshlrev_b32 v201, 16, v245
	v_and_b32 v205, 0xffff0000, v245
	v_lshlrev_b32 v202, 16, v246
	v_and_b32 v206, 0xffff0000, v246
	v_lshlrev_b32 v203, 16, v247
	v_mfma_f32_32x32x16_bf16 v[176:191], a[128:131], v[56:59], v[176:191]
	buffer_load_dwordx4 v[56:59], v4, s[20:23], s52 offen offset:2048 sc1
	v_and_b32 v207, 0xffff0000, v247
	v_add_f32 v200, v200, v224
	v_add_f32 v201, v201, v225
	v_add_f32 v202, v202, v226
	v_add_f32 v203, v203, v227
	s_waitcnt vmcnt(20)
	v_mfma_f32_32x32x16_bf16 v[144:159], a[132:135], v[60:63], v[144:159]
	v_add_f32 v204, v204, v228
	v_add_f32 v205, v205, v229
	v_add_f32 v206, v206, v230
	v_add_f32 v207, v207, v231
	v_max_f32 v200, 0, v200
	v_mfma_f32_32x32x16_bf16 v[160:175], a[136:139], v[60:63], v[160:175]
	v_max_f32 v201, 0, v201
	v_max_f32 v202, 0, v202
	v_max_f32 v203, 0, v203
	v_max_f32 v204, 0, v204
	v_max_f32 v205, 0, v205
	v_mfma_f32_32x32x16_bf16 v[176:191], a[140:143], v[60:63], v[176:191]
	buffer_load_dwordx4 v[60:63], v4, s[20:23], s52 offen offset:3072 sc1
	v_max_f32 v206, 0, v206
	v_max_f32 v207, 0, v207
	v_cvt_pk_bf16_f32 v244, v200, v204
	v_cvt_pk_bf16_f32 v245, v201, v205
	v_cvt_pk_bf16_f32 v246, v202, v206
	s_waitcnt vmcnt(20)
	v_mfma_f32_32x32x16_bf16 v[144:159], a[144:147], v[64:67], v[144:159]
	v_cvt_pk_bf16_f32 v247, v203, v207
	v_lshlrev_b32 v200, 16, v244
	v_and_b32 v204, 0xffff0000, v244
	v_lshlrev_b32 v201, 16, v245
	v_and_b32 v205, 0xffff0000, v245
	v_mfma_f32_32x32x16_bf16 v[160:175], a[148:151], v[64:67], v[160:175]
	v_lshlrev_b32 v202, 16, v246
	v_and_b32 v206, 0xffff0000, v246
	v_lshlrev_b32 v203, 16, v247
	v_and_b32 v207, 0xffff0000, v247
	v_mul_f32 v200, v200, v200
	v_mfma_f32_32x32x16_bf16 v[176:191], a[152:155], v[64:67], v[176:191]
	buffer_load_dwordx4 v[64:67], v4, s[20:23], s53 offen offset:0 sc1
	v_mul_f32 v201, v201, v201
	v_mul_f32 v202, v202, v202
	v_mul_f32 v203, v203, v203
	v_mul_f32 v204, v204, v204
	v_mul_f32 v205, v205, v205
	s_waitcnt vmcnt(20)
	v_mfma_f32_32x32x16_bf16 v[144:159], a[156:159], v[68:71], v[144:159]
	v_mul_f32 v206, v206, v206
	v_mul_f32 v207, v207, v207
	v_cvt_pk_bf16_f32 v244, v200, v204
	v_cvt_pk_bf16_f32 v245, v201, v205
	v_cvt_pk_bf16_f32 v246, v202, v206
	v_mfma_f32_32x32x16_bf16 v[160:175], a[160:163], v[68:71], v[160:175]
	v_cvt_pk_bf16_f32 v247, v203, v207
	buffer_store_dwordx4 v[244:247], v11, s[20:23], s36 offen sc1
	s_waitcnt lgkmcnt(1)
	v_add_f32 v208, v208, v128
	v_add_f32 v209, v209, v129
	v_add_f32 v210, v210, v130
	v_mfma_f32_32x32x16_bf16 v[176:191], a[164:167], v[68:71], v[176:191]
	buffer_load_dwordx4 v[68:71], v4, s[20:23], s53 offen offset:1024 sc1
	v_add_f32 v211, v211, v131
	s_waitcnt lgkmcnt(0)
	v_add_f32 v212, v212, v132
	v_add_f32 v213, v213, v133
	v_add_f32 v214, v214, v134
	v_add_f32 v215, v215, v135
	s_waitcnt vmcnt(21)
	v_mfma_f32_32x32x16_bf16 v[144:159], a[168:171], v[72:75], v[144:159]
	v_cvt_pk_bf16_f32 v248, v208, v212
	v_cvt_pk_bf16_f32 v249, v209, v213
	v_cvt_pk_bf16_f32 v250, v210, v214
	v_cvt_pk_bf16_f32 v251, v211, v215
	v_lshlrev_b32 v208, 16, v248
	v_mfma_f32_32x32x16_bf16 v[160:175], a[172:175], v[72:75], v[160:175]
	v_and_b32 v212, 0xffff0000, v248
	v_lshlrev_b32 v209, 16, v249
	v_and_b32 v213, 0xffff0000, v249
	v_lshlrev_b32 v210, 16, v250
	v_and_b32 v214, 0xffff0000, v250
	v_mfma_f32_32x32x16_bf16 v[176:191], a[176:179], v[72:75], v[176:191]
	buffer_load_dwordx4 v[72:75], v4, s[20:23], s53 offen offset:2048 sc1
	v_lshlrev_b32 v211, 16, v251
	v_and_b32 v215, 0xffff0000, v251
	v_add_f32 v208, v208, v232
	v_add_f32 v209, v209, v233
	v_add_f32 v210, v210, v234
	s_waitcnt vmcnt(21)
	v_mfma_f32_32x32x16_bf16 v[144:159], a[180:183], v[76:79], v[144:159]
	v_add_f32 v211, v211, v235
	v_add_f32 v212, v212, v236
	v_add_f32 v213, v213, v237
	v_add_f32 v214, v214, v238
	v_add_f32 v215, v215, v239
	v_mfma_f32_32x32x16_bf16 v[160:175], a[184:187], v[76:79], v[160:175]
	v_max_f32 v208, 0, v208
	v_max_f32 v209, 0, v209
	v_max_f32 v210, 0, v210
	v_max_f32 v211, 0, v211
	v_max_f32 v212, 0, v212
	v_mfma_f32_32x32x16_bf16 v[176:191], a[188:191], v[76:79], v[176:191]
	buffer_load_dwordx4 v[76:79], v4, s[20:23], s53 offen offset:3072 sc1
	v_max_f32 v213, 0, v213
	v_max_f32 v214, 0, v214
	v_max_f32 v215, 0, v215
	v_cvt_pk_bf16_f32 v248, v208, v212
	v_cvt_pk_bf16_f32 v249, v209, v213
	s_waitcnt vmcnt(21)
	v_mfma_f32_32x32x16_bf16 v[144:159], a[192:195], v[80:83], v[144:159]
	v_cvt_pk_bf16_f32 v250, v210, v214
	v_cvt_pk_bf16_f32 v251, v211, v215
	v_lshlrev_b32 v208, 16, v248
	v_and_b32 v212, 0xffff0000, v248
	v_lshlrev_b32 v209, 16, v249
	v_mfma_f32_32x32x16_bf16 v[160:175], a[196:199], v[80:83], v[160:175]
	v_and_b32 v213, 0xffff0000, v249
	v_lshlrev_b32 v210, 16, v250
	v_and_b32 v214, 0xffff0000, v250
	v_lshlrev_b32 v211, 16, v251
	v_and_b32 v215, 0xffff0000, v251
	v_mfma_f32_32x32x16_bf16 v[176:191], a[200:203], v[80:83], v[176:191]
	buffer_load_dwordx4 v[80:83], v4, s[20:23], s54 offen offset:0 sc1
	v_mul_f32 v208, v208, v208
	v_mul_f32 v209, v209, v209
	v_mul_f32 v210, v210, v210
	v_mul_f32 v211, v211, v211
	v_mul_f32 v212, v212, v212
	s_waitcnt vmcnt(21)
	v_mfma_f32_32x32x16_bf16 v[144:159], a[204:207], v[84:87], v[144:159]
	v_mul_f32 v213, v213, v213
	v_mul_f32 v214, v214, v214
	v_mul_f32 v215, v215, v215
	v_cvt_pk_bf16_f32 v248, v208, v212
	v_cvt_pk_bf16_f32 v249, v209, v213
	v_mfma_f32_32x32x16_bf16 v[160:175], a[208:211], v[84:87], v[160:175]
	v_cvt_pk_bf16_f32 v250, v210, v214
	v_cvt_pk_bf16_f32 v251, v211, v215
	buffer_store_dwordx4 v[248:251], v12, s[20:23], s36 offen sc1
	v_mfma_f32_32x32x16_bf16 v[176:191], a[212:215], v[84:87], v[176:191]
	buffer_load_dwordx4 v[84:87], v4, s[20:23], s54 offen offset:1024 sc1
	s_waitcnt vmcnt(22)
	v_mfma_f32_32x32x16_bf16 v[144:159], a[216:219], v[88:91], v[144:159]
	v_mfma_f32_32x32x16_bf16 v[160:175], a[220:223], v[88:91], v[160:175]
	v_mfma_f32_32x32x16_bf16 v[176:191], a[224:227], v[88:91], v[176:191]
	buffer_load_dwordx4 v[88:91], v4, s[20:23], s54 offen offset:2048 sc1
	s_waitcnt vmcnt(22)
	v_mfma_f32_32x32x16_bf16 v[144:159], a[228:231], v[92:95], v[144:159]
	v_mfma_f32_32x32x16_bf16 v[160:175], a[232:235], v[92:95], v[160:175]
	v_mfma_f32_32x32x16_bf16 v[176:191], a[236:239], v[92:95], v[176:191]
	buffer_load_dwordx4 v[92:95], v4, s[20:23], s54 offen offset:3072 sc1
	s_mov_b32 s36, s55
	s_add_u32 s55, s55, s37
	s_xor_b32 s55, s55, s41
	s_add_u32 s48, s48, 1
	s_cmp_eq_u32 s48, 64
	s_cbranch_scc1 .Lt1_out21
	s_and_b32 s18, s63, 15
	s_cbranch_scc0 .Lt1_l23m
	s_andn2_b32 s58, s70, 255
	s_add_u32 s58, s58, s18
.Lt1_n24:
	v_subrev_u32 v252, s58, v252
	v_cmp_lt_u32 vcc, 15, v252
	s_cbranch_vccz .Lt1_n25
	s_cmp_eq_u32 s69, 0
	s_cbranch_scc1 .Lt1_n25
	s_sub_u32 s69, s69, 1
	s_sleep 2
	buffer_load_dword v252, v14, s[20:23], 0 offen sc1
	s_waitcnt vmcnt(0)
	s_branch .Lt1_n24
.Lt1_n25:
	s_cmp_lt_u32 s63, 16
	s_cbranch_scc1 .Lt1_l23m
	buffer_inv sc1
.Lt1_l23m:
	s_waitcnt vmcnt(22)
	v_mfma_f32_32x32x16_bf16 v[96:111], a[0:3], v[16:19], 0
	s_add_u32 s50, s35, 0
	s_add_u32 s51, s35, 4096
	s_add_u32 s52, s35, 16384
	s_add_u32 s53, s35, 20480
	s_add_u32 s54, s35, 32768
	s_add_u32 s35, s35, 4194304
	s_and_b32 s35, s35, 268435455
	v_mfma_f32_32x32x16_bf16 v[112:127], a[4:7], v[16:19], 0
	ds_write_b128 v6, v[152:155] offset:6144
	v_mfma_f32_32x32x16_bf16 v[128:143], a[8:11], v[16:19], 0
	buffer_load_dwordx4 v[16:19], v4, s[20:23], s50 offen offset:0 sc1
	ds_write_b128 v6, v[156:159] offset:7168
	s_waitcnt vmcnt(22)
	v_mfma_f32_32x32x16_bf16 v[96:111], a[12:15], v[20:23], v[96:111]
	ds_write_b128 v6, v[168:171] offset:8192
	v_mfma_f32_32x32x16_bf16 v[112:127], a[16:19], v[20:23], v[112:127]
	ds_write_b128 v6, v[172:175] offset:9216
	v_mfma_f32_32x32x16_bf16 v[128:143], a[20:23], v[20:23], v[128:143]
	buffer_load_dwordx4 v[20:23], v4, s[20:23], s50 offen offset:1024 sc1
	ds_write_b128 v6, v[184:187] offset:10240
	s_waitcnt vmcnt(22)
	v_mfma_f32_32x32x16_bf16 v[96:111], a[24:27], v[24:27], v[96:111]
	ds_write_b128 v6, v[188:191] offset:11264
	v_mfma_f32_32x32x16_bf16 v[112:127], a[28:31], v[24:27], v[112:127]
	v_mfma_f32_32x32x16_bf16 v[128:143], a[32:35], v[24:27], v[128:143]
	buffer_load_dwordx4 v[24:27], v4, s[20:23], s50 offen offset:2048 sc1
	s_waitcnt vmcnt(22)
	v_mfma_f32_32x32x16_bf16 v[96:111], a[36:39], v[28:31], v[96:111]
	v_mfma_f32_32x32x16_bf16 v[112:127], a[40:43], v[28:31], v[112:127]
	s_waitcnt lgkmcnt(0)
	s_barrier
	s_sub_u32 s58, s48, 2
	s_max_i32 s58, s58, 0
	v_mov_b32 v9, s58
	s_mov_b64 exec, s[60:61]
	buffer_store_dword v9, off, s[20:23], s38 sc1
	s_mov_b64 exec, -1
	v_mfma_f32_32x32x16_bf16 v[128:143], a[44:47], v[28:31], v[128:143]
	buffer_load_dwordx4 v[28:31], v4, s[20:23], s50 offen offset:3072 sc1
	ds_read_b128 v[192:195], v7 offset:6144
	ds_read_b128 v[196:199], v7 offset:7168
	s_waitcnt vmcnt(22)
	v_mfma_f32_32x32x16_bf16 v[96:111], a[48:51], v[32:35], v[96:111]
	ds_read_b128 v[200:203], v7 offset:8192
	ds_read_b128 v[204:207], v7 offset:9216
	v_mfma_f32_32x32x16_bf16 v[112:127], a[52:55], v[32:35], v[112:127]
	ds_read_b128 v[208:211], v7 offset:10240
	ds_read_b128 v[212:215], v7 offset:11264
	v_mfma_f32_32x32x16_bf16 v[128:143], a[56:59], v[32:35], v[128:143]
	buffer_load_dwordx4 v[32:35], v4, s[20:23], s51 offen offset:0 sc1
	s_waitcnt vmcnt(22)
	v_mfma_f32_32x32x16_bf16 v[96:111], a[60:63], v[36:39], v[96:111]
	v_mfma_f32_32x32x16_bf16 v[112:127], a[64:67], v[36:39], v[112:127]
	s_waitcnt lgkmcnt(5)
	v_add_f32 v192, v192, v144
	v_add_f32 v193, v193, v145
	v_add_f32 v194, v194, v146
	v_add_f32 v195, v195, v147
	s_waitcnt lgkmcnt(4)
	v_add_f32 v196, v196, v148
	v_mfma_f32_32x32x16_bf16 v[128:143], a[68:71], v[36:39], v[128:143]
	buffer_load_dwordx4 v[36:39], v4, s[20:23], s51 offen offset:1024 sc1
	v_add_f32 v197, v197, v149
	v_add_f32 v198, v198, v150
	v_add_f32 v199, v199, v151
	v_cvt_pk_bf16_f32 v240, v192, v196
	v_cvt_pk_bf16_f32 v241, v193, v197
	s_waitcnt vmcnt(22)
	v_mfma_f32_32x32x16_bf16 v[96:111], a[72:75], v[40:43], v[96:111]
	v_cvt_pk_bf16_f32 v242, v194, v198
	v_cvt_pk_bf16_f32 v243, v195, v199
	v_lshlrev_b32 v192, 16, v240
	v_and_b32 v196, 0xffff0000, v240
	v_lshlrev_b32 v193, 16, v241
	v_mfma_f32_32x32x16_bf16 v[112:127], a[76:79], v[40:43], v[112:127]
	v_and_b32 v197, 0xffff0000, v241
	v_lshlrev_b32 v194, 16, v242
	v_and_b32 v198, 0xffff0000, v242
	v_lshlrev_b32 v195, 16, v243
	v_and_b32 v199, 0xffff0000, v243
	v_mfma_f32_32x32x16_bf16 v[128:143], a[80:83], v[40:43], v[128:143]
	buffer_load_dwordx4 v[40:43], v4, s[20:23], s51 offen offset:2048 sc1
	v_add_f32 v192, v192, v216
	v_add_f32 v193, v193, v217
	v_add_f32 v194, v194, v218
	v_add_f32 v195, v195, v219
	v_add_f32 v196, v196, v220
	s_waitcnt vmcnt(22)
	v_mfma_f32_32x32x16_bf16 v[96:111], a[84:87], v[44:47], v[96:111]
	v_add_f32 v197, v197, v221
	v_add_f32 v198, v198, v222
	v_add_f32 v199, v199, v223
	v_max_f32 v192, 0, v192
	v_max_f32 v193, 0, v193
	v_mfma_f32_32x32x16_bf16 v[112:127], a[88:91], v[44:47], v[112:127]
	v_max_f32 v194, 0, v194
	v_max_f32 v195, 0, v195
	v_max_f32 v196, 0, v196
	v_max_f32 v197, 0, v197
	v_max_f32 v198, 0, v198
	v_mfma_f32_32x32x16_bf16 v[128:143], a[92:95], v[44:47], v[128:143]
	buffer_load_dwordx4 v[44:47], v4, s[20:23], s51 offen offset:3072 sc1
	v_max_f32 v199, 0, v199
	v_cvt_pk_bf16_f32 v240, v192, v196
	v_cvt_pk_bf16_f32 v241, v193, v197
	v_cvt_pk_bf16_f32 v242, v194, v198
	v_cvt_pk_bf16_f32 v243, v195, v199
	s_waitcnt vmcnt(22)
	v_mfma_f32_32x32x16_bf16 v[96:111], a[96:99], v[48:51], v[96:111]
	v_lshlrev_b32 v192, 16, v240
	v_and_b32 v196, 0xffff0000, v240
	v_lshlrev_b32 v193, 16, v241
	v_and_b32 v197, 0xffff0000, v241
	v_lshlrev_b32 v194, 16, v242
	v_mfma_f32_32x32x16_bf16 v[112:127], a[100:103], v[48:51], v[112:127]
	v_and_b32 v198, 0xffff0000, v242
	v_lshlrev_b32 v195, 16, v243
	v_and_b32 v199, 0xffff0000, v243
	v_mul_f32 v192, v192, v192
	v_mul_f32 v193, v193, v193
	v_mfma_f32_32x32x16_bf16 v[128:143], a[104:107], v[48:51], v[128:143]
	buffer_load_dwordx4 v[48:51], v4, s[20:23], s52 offen offset:0 sc1
	v_mul_f32 v194, v194, v194
	v_mul_f32 v195, v195, v195
	v_mul_f32 v196, v196, v196
	v_mul_f32 v197, v197, v197
	v_mul_f32 v198, v198, v198
	s_waitcnt vmcnt(21)
	v_mfma_f32_32x32x16_bf16 v[96:111], a[108:111], v[52:55], v[96:111]
	v_mul_f32 v199, v199, v199
	v_cvt_pk_bf16_f32 v240, v192, v196
	v_cvt_pk_bf16_f32 v241, v193, v197
	v_cvt_pk_bf16_f32 v242, v194, v198
	v_cvt_pk_bf16_f32 v243, v195, v199
	v_mfma_f32_32x32x16_bf16 v[112:127], a[112:115], v[52:55], v[112:127]
	buffer_store_dwordx4 v[240:243], v5, s[20:23], s36 offen sc1
	s_waitcnt lgkmcnt(3)
	v_add_f32 v200, v200, v160
	v_add_f32 v201, v201, v161
	v_add_f32 v202, v202, v162
	v_add_f32 v203, v203, v163
	v_mfma_f32_32x32x16_bf16 v[128:143], a[116:119], v[52:55], v[128:143]
	buffer_load_dwordx4 v[52:55], v4, s[20:23], s52 offen offset:1024 sc1
	s_waitcnt lgkmcnt(2)
	v_add_f32 v204, v204, v164
	v_add_f32 v205, v205, v165
	v_add_f32 v206, v206, v166
	v_add_f32 v207, v207, v167
	v_cvt_pk_bf16_f32 v244, v200, v204
	s_waitcnt vmcnt(22)
	v_mfma_f32_32x32x16_bf16 v[96:111], a[120:123], v[56:59], v[96:111]
	v_cvt_pk_bf16_f32 v245, v201, v205
	v_cvt_pk_bf16_f32 v246, v202, v206
	v_cvt_pk_bf16_f32 v247, v203, v207
	v_lshlrev_b32 v200, 16, v244
	v_and_b32 v204, 0xffff0000, v244
	v_mfma_f32_32x32x16_bf16 v[112:127], a[124:127], v[56:59], v[112:127]
	v_lshlrev_b32 v201, 16, v245
	v_and_b32 v205, 0xffff0000, v245
	v_lshlrev_b32 v202, 16, v246
	v_and_b32 v206, 0xffff0000, v246
	v_lshlrev_b32 v203, 16, v247
	v_mfma_f32_32x32x16_bf16 v[128:143], a[128:131], v[56:59], v[128:143]
	buffer_load_dwordx4 v[56:59], v4, s[20:23], s52 offen offset:2048 sc1
	v_and_b32 v207, 0xffff0000, v247
	v_add_f32 v200, v200, v224
	v_add_f32 v201, v201, v225
	v_add_f32 v202, v202, v226
	v_add_f32 v203, v203, v227
	s_waitcnt vmcnt(22)
	v_mfma_f32_32x32x16_bf16 v[96:111], a[132:135], v[60:63], v[96:111]
	v_add_f32 v204, v204, v228
	v_add_f32 v205, v205, v229
	v_add_f32 v206, v206, v230
	v_add_f32 v207, v207, v231
	v_max_f32 v200, 0, v200
	v_mfma_f32_32x32x16_bf16 v[112:127], a[136:139], v[60:63], v[112:127]
	v_max_f32 v201, 0, v201
	v_max_f32 v202, 0, v202
	v_max_f32 v203, 0, v203
	v_max_f32 v204, 0, v204
	v_max_f32 v205, 0, v205
	v_mfma_f32_32x32x16_bf16 v[128:143], a[140:143], v[60:63], v[128:143]
	buffer_load_dwordx4 v[60:63], v4, s[20:23], s52 offen offset:3072 sc1
	v_max_f32 v206, 0, v206
	v_max_f32 v207, 0, v207
	v_cvt_pk_bf16_f32 v244, v200, v204
	v_cvt_pk_bf16_f32 v245, v201, v205
	v_cvt_pk_bf16_f32 v246, v202, v206
	s_waitcnt vmcnt(22)
	v_mfma_f32_32x32x16_bf16 v[96:111], a[144:147], v[64:67], v[96:111]
	v_cvt_pk_bf16_f32 v247, v203, v207
	v_lshlrev_b32 v200, 16, v244
	v_and_b32 v204, 0xffff0000, v244
	v_lshlrev_b32 v201, 16, v245
	v_and_b32 v205, 0xffff0000, v245
	v_mfma_f32_32x32x16_bf16 v[112:127], a[148:151], v[64:67], v[112:127]
	v_lshlrev_b32 v202, 16, v246
	v_and_b32 v206, 0xffff0000, v246
	v_lshlrev_b32 v203, 16, v247
	v_and_b32 v207, 0xffff0000, v247
	v_mul_f32 v200, v200, v200
	v_mfma_f32_32x32x16_bf16 v[128:143], a[152:155], v[64:67], v[128:143]
	buffer_load_dwordx4 v[64:67], v4, s[20:23], s53 offen offset:0 sc1
	v_mul_f32 v201, v201, v201
	v_mul_f32 v202, v202, v202
	v_mul_f32 v203, v203, v203
	v_mul_f32 v204, v204, v204
	v_mul_f32 v205, v205, v205
	s_waitcnt vmcnt(21)
	v_mfma_f32_32x32x16_bf16 v[96:111], a[156:159], v[68:71], v[96:111]
	v_mul_f32 v206, v206, v206
	v_mul_f32 v207, v207, v207
	v_cvt_pk_bf16_f32 v244, v200, v204
	v_cvt_pk_bf16_f32 v245, v201, v205
	v_cvt_pk_bf16_f32 v246, v202, v206
	v_mfma_f32_32x32x16_bf16 v[112:127], a[160:163], v[68:71], v[112:127]
	v_cvt_pk_bf16_f32 v247, v203, v207
	buffer_store_dwordx4 v[244:247], v11, s[20:23], s36 offen sc1
	s_waitcnt lgkmcnt(1)
	v_add_f32 v208, v208, v176
	v_add_f32 v209, v209, v177
	v_add_f32 v210, v210, v178
	v_mfma_f32_32x32x16_bf16 v[128:143], a[164:167], v[68:71], v[128:143]
	buffer_load_dwordx4 v[68:71], v4, s[20:23], s53 offen offset:1024 sc1
	v_add_f32 v211, v211, v179
	s_waitcnt lgkmcnt(0)
	v_add_f32 v212, v212, v180
	v_add_f32 v213, v213, v181
	v_add_f32 v214, v214, v182
	v_add_f32 v215, v215, v183
	s_waitcnt vmcnt(22)
	v_mfma_f32_32x32x16_bf16 v[96:111], a[168:171], v[72:75], v[96:111]
	v_cvt_pk_bf16_f32 v248, v208, v212
	v_cvt_pk_bf16_f32 v249, v209, v213
	v_cvt_pk_bf16_f32 v250, v210, v214
	v_cvt_pk_bf16_f32 v251, v211, v215
	v_lshlrev_b32 v208, 16, v248
	v_mfma_f32_32x32x16_bf16 v[112:127], a[172:175], v[72:75], v[112:127]
	v_and_b32 v212, 0xffff0000, v248
	v_lshlrev_b32 v209, 16, v249
	v_and_b32 v213, 0xffff0000, v249
	v_lshlrev_b32 v210, 16, v250
	v_and_b32 v214, 0xffff0000, v250
	v_mfma_f32_32x32x16_bf16 v[128:143], a[176:179], v[72:75], v[128:143]
	buffer_load_dwordx4 v[72:75], v4, s[20:23], s53 offen offset:2048 sc1
	v_lshlrev_b32 v211, 16, v251
	v_and_b32 v215, 0xffff0000, v251
	v_add_f32 v208, v208, v232
	v_add_f32 v209, v209, v233
	v_add_f32 v210, v210, v234
	s_waitcnt vmcnt(22)
	v_mfma_f32_32x32x16_bf16 v[96:111], a[180:183], v[76:79], v[96:111]
	v_add_f32 v211, v211, v235
	v_add_f32 v212, v212, v236
	v_add_f32 v213, v213, v237
	v_add_f32 v214, v214, v238
	v_add_f32 v215, v215, v239
	v_mfma_f32_32x32x16_bf16 v[112:127], a[184:187], v[76:79], v[112:127]
	v_max_f32 v208, 0, v208
	v_max_f32 v209, 0, v209
	v_max_f32 v210, 0, v210
	v_max_f32 v211, 0, v211
	v_max_f32 v212, 0, v212
	v_mfma_f32_32x32x16_bf16 v[128:143], a[188:191], v[76:79], v[128:143]
	buffer_load_dwordx4 v[76:79], v4, s[20:23], s53 offen offset:3072 sc1
	v_max_f32 v213, 0, v213
	v_max_f32 v214, 0, v214
	v_max_f32 v215, 0, v215
	v_cvt_pk_bf16_f32 v248, v208, v212
	v_cvt_pk_bf16_f32 v249, v209, v213
	s_waitcnt vmcnt(22)
	v_mfma_f32_32x32x16_bf16 v[96:111], a[192:195], v[80:83], v[96:111]
	v_cvt_pk_bf16_f32 v250, v210, v214
	v_cvt_pk_bf16_f32 v251, v211, v215
	v_lshlrev_b32 v208, 16, v248
	v_and_b32 v212, 0xffff0000, v248
	v_lshlrev_b32 v209, 16, v249
	v_mfma_f32_32x32x16_bf16 v[112:127], a[196:199], v[80:83], v[112:127]
	v_and_b32 v213, 0xffff0000, v249
	v_lshlrev_b32 v210, 16, v250
	v_and_b32 v214, 0xffff0000, v250
	v_lshlrev_b32 v211, 16, v251
	v_and_b32 v215, 0xffff0000, v251
	v_mfma_f32_32x32x16_bf16 v[128:143], a[200:203], v[80:83], v[128:143]
	buffer_load_dwordx4 v[80:83], v4, s[20:23], s54 offen offset:0 sc1
	v_mul_f32 v208, v208, v208
	v_mul_f32 v209, v209, v209
	v_mul_f32 v210, v210, v210
	v_mul_f32 v211, v211, v211
	v_mul_f32 v212, v212, v212
	s_waitcnt vmcnt(21)
	v_mfma_f32_32x32x16_bf16 v[96:111], a[204:207], v[84:87], v[96:111]
	v_mul_f32 v213, v213, v213
	v_mul_f32 v214, v214, v214
	v_mul_f32 v215, v215, v215
	v_cvt_pk_bf16_f32 v248, v208, v212
	v_cvt_pk_bf16_f32 v249, v209, v213
	v_mfma_f32_32x32x16_bf16 v[112:127], a[208:211], v[84:87], v[112:127]
	v_cvt_pk_bf16_f32 v250, v210, v214
	v_cvt_pk_bf16_f32 v251, v211, v215
	buffer_store_dwordx4 v[248:251], v12, s[20:23], s36 offen sc1
	v_mfma_f32_32x32x16_bf16 v[128:143], a[212:215], v[84:87], v[128:143]
	buffer_load_dwordx4 v[84:87], v4, s[20:23], s54 offen offset:1024 sc1
	s_waitcnt vmcnt(22)
	v_mfma_f32_32x32x16_bf16 v[96:111], a[216:219], v[88:91], v[96:111]
	v_mfma_f32_32x32x16_bf16 v[112:127], a[220:223], v[88:91], v[112:127]
	v_mfma_f32_32x32x16_bf16 v[128:143], a[224:227], v[88:91], v[128:143]
	buffer_load_dwordx4 v[88:91], v4, s[20:23], s54 offen offset:2048 sc1
	s_waitcnt vmcnt(22)
	v_mfma_f32_32x32x16_bf16 v[96:111], a[228:231], v[92:95], v[96:111]
	v_mfma_f32_32x32x16_bf16 v[112:127], a[232:235], v[92:95], v[112:127]
	v_mfma_f32_32x32x16_bf16 v[128:143], a[236:239], v[92:95], v[128:143]
	buffer_load_dwordx4 v[92:95], v4, s[20:23], s54 offen offset:3072 sc1
	s_mov_b32 s36, s55
	s_add_u32 s55, s55, s37
	s_xor_b32 s55, s55, s41
	s_add_u32 s48, s48, 1
	s_cmp_lt_u32 s48, s65
	s_cbranch_scc1 .Lt1_seg22
	s_xor_b32 s35, s35, s45
.Lt1_seg26:
	s_waitcnt vmcnt(22)
	v_mfma_f32_32x32x16_bf16 v[144:159], a[0:3], v[16:19], 0
	s_add_u32 s50, s35, 0
	s_add_u32 s51, s35, 4096
	s_add_u32 s52, s35, 16384
	s_add_u32 s53, s35, 20480
	s_add_u32 s54, s35, 32768
	s_add_u32 s35, s35, 4194304
	s_and_b32 s35, s35, 268435455
	v_mfma_f32_32x32x16_bf16 v[160:175], a[4:7], v[16:19], 0
	ds_write_b128 v6, v[104:107] offset:0
	v_mfma_f32_32x32x16_bf16 v[176:191], a[8:11], v[16:19], 0
	buffer_load_dwordx4 v[16:19], v4, s[20:23], s50 offen offset:0
	ds_write_b128 v6, v[108:111] offset:1024
	s_waitcnt vmcnt(22)
	v_mfma_f32_32x32x16_bf16 v[144:159], a[12:15], v[20:23], v[144:159]
	ds_write_b128 v6, v[120:123] offset:2048
	v_mfma_f32_32x32x16_bf16 v[160:175], a[16:19], v[20:23], v[160:175]
	ds_write_b128 v6, v[124:127] offset:3072
	v_mfma_f32_32x32x16_bf16 v[176:191], a[20:23], v[20:23], v[176:191]
	buffer_load_dwordx4 v[20:23], v4, s[20:23], s50 offen offset:1024
	ds_write_b128 v6, v[136:139] offset:4096
	s_waitcnt vmcnt(22)
	v_mfma_f32_32x32x16_bf16 v[144:159], a[24:27], v[24:27], v[144:159]
	ds_write_b128 v6, v[140:143] offset:5120
	v_mfma_f32_32x32x16_bf16 v[160:175], a[28:31], v[24:27], v[160:175]
	v_mfma_f32_32x32x16_bf16 v[176:191], a[32:35], v[24:27], v[176:191]
	buffer_load_dwordx4 v[24:27], v4, s[20:23], s50 offen offset:2048
	s_waitcnt vmcnt(22)
	v_mfma_f32_32x32x16_bf16 v[144:159], a[36:39], v[28:31], v[144:159]
	v_mfma_f32_32x32x16_bf16 v[160:175], a[40:43], v[28:31], v[160:175]
	s_waitcnt lgkmcnt(0)
	s_barrier
	s_sub_u32 s58, s48, 2
	s_max_i32 s58, s58, 0
	v_mov_b32 v9, s58
	s_mov_b64 exec, s[60:61]
	buffer_store_dword v9, off, s[20:23], s38 sc1
	s_mov_b64 exec, -1
	v_mfma_f32_32x32x16_bf16 v[176:191], a[44:47], v[28:31], v[176:191]
	buffer_load_dwordx4 v[28:31], v4, s[20:23], s50 offen offset:3072
	ds_read_b128 v[192:195], v7 offset:0
	ds_read_b128 v[196:199], v7 offset:1024
	s_waitcnt vmcnt(22)
	v_mfma_f32_32x32x16_bf16 v[144:159], a[48:51], v[32:35], v[144:159]
	ds_read_b128 v[200:203], v7 offset:2048
	ds_read_b128 v[204:207], v7 offset:3072
	v_mfma_f32_32x32x16_bf16 v[160:175], a[52:55], v[32:35], v[160:175]
	ds_read_b128 v[208:211], v7 offset:4096
	ds_read_b128 v[212:215], v7 offset:5120
	v_mfma_f32_32x32x16_bf16 v[176:191], a[56:59], v[32:35], v[176:191]
	buffer_load_dwordx4 v[32:35], v4, s[20:23], s51 offen offset:0
	s_waitcnt vmcnt(22)
	v_mfma_f32_32x32x16_bf16 v[144:159], a[60:63], v[36:39], v[144:159]
	v_mfma_f32_32x32x16_bf16 v[160:175], a[64:67], v[36:39], v[160:175]
	s_waitcnt lgkmcnt(5)
	v_add_f32 v192, v192, v96
	v_add_f32 v193, v193, v97
	v_add_f32 v194, v194, v98
	v_add_f32 v195, v195, v99
	s_waitcnt lgkmcnt(4)
	v_add_f32 v196, v196, v100
	v_mfma_f32_32x32x16_bf16 v[176:191], a[68:71], v[36:39], v[176:191]
	buffer_load_dwordx4 v[36:39], v4, s[20:23], s51 offen offset:1024
	v_add_f32 v197, v197, v101
	v_add_f32 v198, v198, v102
	v_add_f32 v199, v199, v103
	v_cvt_pk_bf16_f32 v240, v192, v196
	v_cvt_pk_bf16_f32 v241, v193, v197
	s_waitcnt vmcnt(22)
	v_mfma_f32_32x32x16_bf16 v[144:159], a[72:75], v[40:43], v[144:159]
	v_cvt_pk_bf16_f32 v242, v194, v198
	v_cvt_pk_bf16_f32 v243, v195, v199
	v_lshlrev_b32 v192, 16, v240
	v_and_b32 v196, 0xffff0000, v240
	v_lshlrev_b32 v193, 16, v241
	v_mfma_f32_32x32x16_bf16 v[160:175], a[76:79], v[40:43], v[160:175]
	v_and_b32 v197, 0xffff0000, v241
	v_lshlrev_b32 v194, 16, v242
	v_and_b32 v198, 0xffff0000, v242
	v_lshlrev_b32 v195, 16, v243
	v_and_b32 v199, 0xffff0000, v243
	v_mfma_f32_32x32x16_bf16 v[176:191], a[80:83], v[40:43], v[176:191]
	buffer_load_dwordx4 v[40:43], v4, s[20:23], s51 offen offset:2048
	v_add_f32 v192, v192, v216
	v_add_f32 v193, v193, v217
	v_add_f32 v194, v194, v218
	v_add_f32 v195, v195, v219
	v_add_f32 v196, v196, v220
	s_waitcnt vmcnt(22)
	v_mfma_f32_32x32x16_bf16 v[144:159], a[84:87], v[44:47], v[144:159]
	v_add_f32 v197, v197, v221
	v_add_f32 v198, v198, v222
	v_add_f32 v199, v199, v223
	v_max_f32 v192, 0, v192
	v_max_f32 v193, 0, v193
	v_mfma_f32_32x32x16_bf16 v[160:175], a[88:91], v[44:47], v[160:175]
	v_max_f32 v194, 0, v194
	v_max_f32 v195, 0, v195
	v_max_f32 v196, 0, v196
	v_max_f32 v197, 0, v197
	v_max_f32 v198, 0, v198
	v_mfma_f32_32x32x16_bf16 v[176:191], a[92:95], v[44:47], v[176:191]
	buffer_load_dwordx4 v[44:47], v4, s[20:23], s51 offen offset:3072
	v_max_f32 v199, 0, v199
	v_cvt_pk_bf16_f32 v240, v192, v196
	v_cvt_pk_bf16_f32 v241, v193, v197
	v_cvt_pk_bf16_f32 v242, v194, v198
	v_cvt_pk_bf16_f32 v243, v195, v199
	s_waitcnt vmcnt(22)
	v_mfma_f32_32x32x16_bf16 v[144:159], a[96:99], v[48:51], v[144:159]
	v_lshlrev_b32 v192, 16, v240
	v_and_b32 v196, 0xffff0000, v240
	v_lshlrev_b32 v193, 16, v241
	v_and_b32 v197, 0xffff0000, v241
	v_lshlrev_b32 v194, 16, v242
	v_mfma_f32_32x32x16_bf16 v[160:175], a[100:103], v[48:51], v[160:175]
	v_and_b32 v198, 0xffff0000, v242
	v_lshlrev_b32 v195, 16, v243
	v_and_b32 v199, 0xffff0000, v243
	v_mul_f32 v192, v192, v192
	v_mul_f32 v193, v193, v193
	v_mfma_f32_32x32x16_bf16 v[176:191], a[104:107], v[48:51], v[176:191]
	buffer_load_dwordx4 v[48:51], v4, s[20:23], s52 offen offset:0
	v_mul_f32 v194, v194, v194
	v_mul_f32 v195, v195, v195
	v_mul_f32 v196, v196, v196
	v_mul_f32 v197, v197, v197
	v_mul_f32 v198, v198, v198
	s_waitcnt vmcnt(21)
	v_mfma_f32_32x32x16_bf16 v[144:159], a[108:111], v[52:55], v[144:159]
	v_mul_f32 v199, v199, v199
	v_cvt_pk_bf16_f32 v240, v192, v196
	v_cvt_pk_bf16_f32 v241, v193, v197
	v_cvt_pk_bf16_f32 v242, v194, v198
	v_cvt_pk_bf16_f32 v243, v195, v199
	v_mfma_f32_32x32x16_bf16 v[160:175], a[112:115], v[52:55], v[160:175]
	buffer_store_dwordx4 v[240:243], v5, s[20:23], s36 offen sc1
	s_waitcnt lgkmcnt(3)
	v_add_f32 v200, v200, v112
	v_add_f32 v201, v201, v113
	v_add_f32 v202, v202, v114
	v_add_f32 v203, v203, v115
	v_mfma_f32_32x32x16_bf16 v[176:191], a[116:119], v[52:55], v[176:191]
	buffer_load_dwordx4 v[52:55], v4, s[20:23], s52 offen offset:1024
	s_waitcnt lgkmcnt(2)
	v_add_f32 v204, v204, v116
	v_add_f32 v205, v205, v117
	v_add_f32 v206, v206, v118
	v_add_f32 v207, v207, v119
	v_cvt_pk_bf16_f32 v244, v200, v204
	s_waitcnt vmcnt(22)
	v_mfma_f32_32x32x16_bf16 v[144:159], a[120:123], v[56:59], v[144:159]
	v_cvt_pk_bf16_f32 v245, v201, v205
	v_cvt_pk_bf16_f32 v246, v202, v206
	v_cvt_pk_bf16_f32 v247, v203, v207
	v_lshlrev_b32 v200, 16, v244
	v_and_b32 v204, 0xffff0000, v244
	v_mfma_f32_32x32x16_bf16 v[160:175], a[124:127], v[56:59], v[160:175]
	v_lshlrev_b32 v201, 16, v245
	v_and_b32 v205, 0xffff0000, v245
	v_lshlrev_b32 v202, 16, v246
	v_and_b32 v206, 0xffff0000, v246
	v_lshlrev_b32 v203, 16, v247
	v_mfma_f32_32x32x16_bf16 v[176:191], a[128:131], v[56:59], v[176:191]
	buffer_load_dwordx4 v[56:59], v4, s[20:23], s52 offen offset:2048
	v_and_b32 v207, 0xffff0000, v247
	v_add_f32 v200, v200, v224
	v_add_f32 v201, v201, v225
	v_add_f32 v202, v202, v226
	v_add_f32 v203, v203, v227
	s_waitcnt vmcnt(22)
	v_mfma_f32_32x32x16_bf16 v[144:159], a[132:135], v[60:63], v[144:159]
	v_add_f32 v204, v204, v228
	v_add_f32 v205, v205, v229
	v_add_f32 v206, v206, v230
	v_add_f32 v207, v207, v231
	v_max_f32 v200, 0, v200
	v_mfma_f32_32x32x16_bf16 v[160:175], a[136:139], v[60:63], v[160:175]
	v_max_f32 v201, 0, v201
	v_max_f32 v202, 0, v202
	v_max_f32 v203, 0, v203
	v_max_f32 v204, 0, v204
	v_max_f32 v205, 0, v205
	v_mfma_f32_32x32x16_bf16 v[176:191], a[140:143], v[60:63], v[176:191]
	buffer_load_dwordx4 v[60:63], v4, s[20:23], s52 offen offset:3072
	v_max_f32 v206, 0, v206
	v_max_f32 v207, 0, v207
	v_cvt_pk_bf16_f32 v244, v200, v204
	v_cvt_pk_bf16_f32 v245, v201, v205
	v_cvt_pk_bf16_f32 v246, v202, v206
	s_waitcnt vmcnt(22)
	v_mfma_f32_32x32x16_bf16 v[144:159], a[144:147], v[64:67], v[144:159]
	v_cvt_pk_bf16_f32 v247, v203, v207
	v_lshlrev_b32 v200, 16, v244
	v_and_b32 v204, 0xffff0000, v244
	v_lshlrev_b32 v201, 16, v245
	v_and_b32 v205, 0xffff0000, v245
	v_mfma_f32_32x32x16_bf16 v[160:175], a[148:151], v[64:67], v[160:175]
	v_lshlrev_b32 v202, 16, v246
	v_and_b32 v206, 0xffff0000, v246
	v_lshlrev_b32 v203, 16, v247
	v_and_b32 v207, 0xffff0000, v247
	v_mul_f32 v200, v200, v200
	v_mfma_f32_32x32x16_bf16 v[176:191], a[152:155], v[64:67], v[176:191]
	buffer_load_dwordx4 v[64:67], v4, s[20:23], s53 offen offset:0
	v_mul_f32 v201, v201, v201
	v_mul_f32 v202, v202, v202
	v_mul_f32 v203, v203, v203
	v_mul_f32 v204, v204, v204
	v_mul_f32 v205, v205, v205
	s_waitcnt vmcnt(21)
	v_mfma_f32_32x32x16_bf16 v[144:159], a[156:159], v[68:71], v[144:159]
	v_mul_f32 v206, v206, v206
	v_mul_f32 v207, v207, v207
	v_cvt_pk_bf16_f32 v244, v200, v204
	v_cvt_pk_bf16_f32 v245, v201, v205
	v_cvt_pk_bf16_f32 v246, v202, v206
	v_mfma_f32_32x32x16_bf16 v[160:175], a[160:163], v[68:71], v[160:175]
	v_cvt_pk_bf16_f32 v247, v203, v207
	buffer_store_dwordx4 v[244:247], v11, s[20:23], s36 offen sc1
	s_waitcnt lgkmcnt(1)
	v_add_f32 v208, v208, v128
	v_add_f32 v209, v209, v129
	v_add_f32 v210, v210, v130
	v_mfma_f32_32x32x16_bf16 v[176:191], a[164:167], v[68:71], v[176:191]
	buffer_load_dwordx4 v[68:71], v4, s[20:23], s53 offen offset:1024
	v_add_f32 v211, v211, v131
	s_waitcnt lgkmcnt(0)
	v_add_f32 v212, v212, v132
	v_add_f32 v213, v213, v133
	v_add_f32 v214, v214, v134
	v_add_f32 v215, v215, v135
	s_waitcnt vmcnt(22)
	v_mfma_f32_32x32x16_bf16 v[144:159], a[168:171], v[72:75], v[144:159]
	v_cvt_pk_bf16_f32 v248, v208, v212
	v_cvt_pk_bf16_f32 v249, v209, v213
	v_cvt_pk_bf16_f32 v250, v210, v214
	v_cvt_pk_bf16_f32 v251, v211, v215
	v_lshlrev_b32 v208, 16, v248
	v_mfma_f32_32x32x16_bf16 v[160:175], a[172:175], v[72:75], v[160:175]
	v_and_b32 v212, 0xffff0000, v248
	v_lshlrev_b32 v209, 16, v249
	v_and_b32 v213, 0xffff0000, v249
	v_lshlrev_b32 v210, 16, v250
	v_and_b32 v214, 0xffff0000, v250
	v_mfma_f32_32x32x16_bf16 v[176:191], a[176:179], v[72:75], v[176:191]
	buffer_load_dwordx4 v[72:75], v4, s[20:23], s53 offen offset:2048
	v_lshlrev_b32 v211, 16, v251
	v_and_b32 v215, 0xffff0000, v251
	v_add_f32 v208, v208, v232
	v_add_f32 v209, v209, v233
	v_add_f32 v210, v210, v234
	s_waitcnt vmcnt(22)
	v_mfma_f32_32x32x16_bf16 v[144:159], a[180:183], v[76:79], v[144:159]
	v_add_f32 v211, v211, v235
	v_add_f32 v212, v212, v236
	v_add_f32 v213, v213, v237
	v_add_f32 v214, v214, v238
	v_add_f32 v215, v215, v239
	v_mfma_f32_32x32x16_bf16 v[160:175], a[184:187], v[76:79], v[160:175]
	v_max_f32 v208, 0, v208
	v_max_f32 v209, 0, v209
	v_max_f32 v210, 0, v210
	v_max_f32 v211, 0, v211
	v_max_f32 v212, 0, v212
	v_mfma_f32_32x32x16_bf16 v[176:191], a[188:191], v[76:79], v[176:191]
	buffer_load_dwordx4 v[76:79], v4, s[20:23], s53 offen offset:3072
	v_max_f32 v213, 0, v213
	v_max_f32 v214, 0, v214
	v_max_f32 v215, 0, v215
	v_cvt_pk_bf16_f32 v248, v208, v212
	v_cvt_pk_bf16_f32 v249, v209, v213
	s_waitcnt vmcnt(22)
	v_mfma_f32_32x32x16_bf16 v[144:159], a[192:195], v[80:83], v[144:159]
	v_cvt_pk_bf16_f32 v250, v210, v214
	v_cvt_pk_bf16_f32 v251, v211, v215
	v_lshlrev_b32 v208, 16, v248
	v_and_b32 v212, 0xffff0000, v248
	v_lshlrev_b32 v209, 16, v249
	v_mfma_f32_32x32x16_bf16 v[160:175], a[196:199], v[80:83], v[160:175]
	v_and_b32 v213, 0xffff0000, v249
	v_lshlrev_b32 v210, 16, v250
	v_and_b32 v214, 0xffff0000, v250
	v_lshlrev_b32 v211, 16, v251
	v_and_b32 v215, 0xffff0000, v251
	v_mfma_f32_32x32x16_bf16 v[176:191], a[200:203], v[80:83], v[176:191]
	buffer_load_dwordx4 v[80:83], v4, s[20:23], s54 offen offset:0
	v_mul_f32 v208, v208, v208
	v_mul_f32 v209, v209, v209
	v_mul_f32 v210, v210, v210
	v_mul_f32 v211, v211, v211
	v_mul_f32 v212, v212, v212
	s_waitcnt vmcnt(21)
	v_mfma_f32_32x32x16_bf16 v[144:159], a[204:207], v[84:87], v[144:159]
	v_mul_f32 v213, v213, v213
	v_mul_f32 v214, v214, v214
	v_mul_f32 v215, v215, v215
	v_cvt_pk_bf16_f32 v248, v208, v212
	v_cvt_pk_bf16_f32 v249, v209, v213
	v_mfma_f32_32x32x16_bf16 v[160:175], a[208:211], v[84:87], v[160:175]
	v_cvt_pk_bf16_f32 v250, v210, v214
	v_cvt_pk_bf16_f32 v251, v211, v215
	buffer_store_dwordx4 v[248:251], v12, s[20:23], s36 offen sc1
	v_mfma_f32_32x32x16_bf16 v[176:191], a[212:215], v[84:87], v[176:191]
	buffer_load_dwordx4 v[84:87], v4, s[20:23], s54 offen offset:1024
	s_waitcnt vmcnt(22)
	v_mfma_f32_32x32x16_bf16 v[144:159], a[216:219], v[88:91], v[144:159]
	v_mfma_f32_32x32x16_bf16 v[160:175], a[220:223], v[88:91], v[160:175]
	v_mfma_f32_32x32x16_bf16 v[176:191], a[224:227], v[88:91], v[176:191]
	buffer_load_dwordx4 v[88:91], v4, s[20:23], s54 offen offset:2048
	s_waitcnt vmcnt(22)
	v_mfma_f32_32x32x16_bf16 v[144:159], a[228:231], v[92:95], v[144:159]
	v_mfma_f32_32x32x16_bf16 v[160:175], a[232:235], v[92:95], v[160:175]
	v_mfma_f32_32x32x16_bf16 v[176:191], a[236:239], v[92:95], v[176:191]
	buffer_load_dwordx4 v[92:95], v4, s[20:23], s54 offen offset:3072
	s_mov_b32 s36, s55
	s_add_u32 s55, s55, s37
	s_xor_b32 s55, s55, s41
	s_add_u32 s48, s48, 1
	s_cmp_eq_u32 s48, 64
	s_cbranch_scc1 .Lt1_out21
	s_waitcnt vmcnt(22)
	v_mfma_f32_32x32x16_bf16 v[96:111], a[0:3], v[16:19], 0
	s_add_u32 s50, s35, 0
	s_add_u32 s51, s35, 4096
	s_add_u32 s52, s35, 16384
	s_add_u32 s53, s35, 20480
	s_add_u32 s54, s35, 32768
	s_add_u32 s35, s35, 4194304
	s_and_b32 s35, s35, 268435455
	v_mfma_f32_32x32x16_bf16 v[112:127], a[4:7], v[16:19], 0
	ds_write_b128 v6, v[152:155] offset:6144
	v_mfma_f32_32x32x16_bf16 v[128:143], a[8:11], v[16:19], 0
	buffer_load_dwordx4 v[16:19], v4, s[20:23], s50 offen offset:0
	ds_write_b128 v6, v[156:159] offset:7168
	s_waitcnt vmcnt(22)
	v_mfma_f32_32x32x16_bf16 v[96:111], a[12:15], v[20:23], v[96:111]
	ds_write_b128 v6, v[168:171] offset:8192
	v_mfma_f32_32x32x16_bf16 v[112:127], a[16:19], v[20:23], v[112:127]
	ds_write_b128 v6, v[172:175] offset:9216
	v_mfma_f32_32x32x16_bf16 v[128:143], a[20:23], v[20:23], v[128:143]
	buffer_load_dwordx4 v[20:23], v4, s[20:23], s50 offen offset:1024
	ds_write_b128 v6, v[184:187] offset:10240
	s_waitcnt vmcnt(22)
	v_mfma_f32_32x32x16_bf16 v[96:111], a[24:27], v[24:27], v[96:111]
	ds_write_b128 v6, v[188:191] offset:11264
	v_mfma_f32_32x32x16_bf16 v[112:127], a[28:31], v[24:27], v[112:127]
	v_mfma_f32_32x32x16_bf16 v[128:143], a[32:35], v[24:27], v[128:143]
	buffer_load_dwordx4 v[24:27], v4, s[20:23], s50 offen offset:2048
	s_waitcnt vmcnt(22)
	v_mfma_f32_32x32x16_bf16 v[96:111], a[36:39], v[28:31], v[96:111]
	v_mfma_f32_32x32x16_bf16 v[112:127], a[40:43], v[28:31], v[112:127]
	s_waitcnt lgkmcnt(0)
	s_barrier
	s_sub_u32 s58, s48, 2
	s_max_i32 s58, s58, 0
	v_mov_b32 v9, s58
	s_mov_b64 exec, s[60:61]
	buffer_store_dword v9, off, s[20:23], s38 sc1
	s_mov_b64 exec, -1
	v_mfma_f32_32x32x16_bf16 v[128:143], a[44:47], v[28:31], v[128:143]
	buffer_load_dwordx4 v[28:31], v4, s[20:23], s50 offen offset:3072
	ds_read_b128 v[192:195], v7 offset:6144
	ds_read_b128 v[196:199], v7 offset:7168
	s_waitcnt vmcnt(22)
	v_mfma_f32_32x32x16_bf16 v[96:111], a[48:51], v[32:35], v[96:111]
	ds_read_b128 v[200:203], v7 offset:8192
	ds_read_b128 v[204:207], v7 offset:9216
	v_mfma_f32_32x32x16_bf16 v[112:127], a[52:55], v[32:35], v[112:127]
	ds_read_b128 v[208:211], v7 offset:10240
	ds_read_b128 v[212:215], v7 offset:11264
	v_mfma_f32_32x32x16_bf16 v[128:143], a[56:59], v[32:35], v[128:143]
	buffer_load_dwordx4 v[32:35], v4, s[20:23], s51 offen offset:0
	s_waitcnt vmcnt(22)
	v_mfma_f32_32x32x16_bf16 v[96:111], a[60:63], v[36:39], v[96:111]
	v_mfma_f32_32x32x16_bf16 v[112:127], a[64:67], v[36:39], v[112:127]
	s_waitcnt lgkmcnt(5)
	v_add_f32 v192, v192, v144
	v_add_f32 v193, v193, v145
	v_add_f32 v194, v194, v146
	v_add_f32 v195, v195, v147
	s_waitcnt lgkmcnt(4)
	v_add_f32 v196, v196, v148
	v_mfma_f32_32x32x16_bf16 v[128:143], a[68:71], v[36:39], v[128:143]
	buffer_load_dwordx4 v[36:39], v4, s[20:23], s51 offen offset:1024
	v_add_f32 v197, v197, v149
	v_add_f32 v198, v198, v150
	v_add_f32 v199, v199, v151
	v_cvt_pk_bf16_f32 v240, v192, v196
	v_cvt_pk_bf16_f32 v241, v193, v197
	s_waitcnt vmcnt(22)
	v_mfma_f32_32x32x16_bf16 v[96:111], a[72:75], v[40:43], v[96:111]
	v_cvt_pk_bf16_f32 v242, v194, v198
	v_cvt_pk_bf16_f32 v243, v195, v199
	v_lshlrev_b32 v192, 16, v240
	v_and_b32 v196, 0xffff0000, v240
	v_lshlrev_b32 v193, 16, v241
	v_mfma_f32_32x32x16_bf16 v[112:127], a[76:79], v[40:43], v[112:127]
	v_and_b32 v197, 0xffff0000, v241
	v_lshlrev_b32 v194, 16, v242
	v_and_b32 v198, 0xffff0000, v242
	v_lshlrev_b32 v195, 16, v243
	v_and_b32 v199, 0xffff0000, v243
	v_mfma_f32_32x32x16_bf16 v[128:143], a[80:83], v[40:43], v[128:143]
	buffer_load_dwordx4 v[40:43], v4, s[20:23], s51 offen offset:2048
	v_add_f32 v192, v192, v216
	v_add_f32 v193, v193, v217
	v_add_f32 v194, v194, v218
	v_add_f32 v195, v195, v219
	v_add_f32 v196, v196, v220
	s_waitcnt vmcnt(22)
	v_mfma_f32_32x32x16_bf16 v[96:111], a[84:87], v[44:47], v[96:111]
	v_add_f32 v197, v197, v221
	v_add_f32 v198, v198, v222
	v_add_f32 v199, v199, v223
	v_max_f32 v192, 0, v192
	v_max_f32 v193, 0, v193
	v_mfma_f32_32x32x16_bf16 v[112:127], a[88:91], v[44:47], v[112:127]
	v_max_f32 v194, 0, v194
	v_max_f32 v195, 0, v195
	v_max_f32 v196, 0, v196
	v_max_f32 v197, 0, v197
	v_max_f32 v198, 0, v198
	v_mfma_f32_32x32x16_bf16 v[128:143], a[92:95], v[44:47], v[128:143]
	buffer_load_dwordx4 v[44:47], v4, s[20:23], s51 offen offset:3072
	v_max_f32 v199, 0, v199
	v_cvt_pk_bf16_f32 v240, v192, v196
	v_cvt_pk_bf16_f32 v241, v193, v197
	v_cvt_pk_bf16_f32 v242, v194, v198
	v_cvt_pk_bf16_f32 v243, v195, v199
	s_waitcnt vmcnt(22)
	v_mfma_f32_32x32x16_bf16 v[96:111], a[96:99], v[48:51], v[96:111]
	v_lshlrev_b32 v192, 16, v240
	v_and_b32 v196, 0xffff0000, v240
	v_lshlrev_b32 v193, 16, v241
	v_and_b32 v197, 0xffff0000, v241
	v_lshlrev_b32 v194, 16, v242
	v_mfma_f32_32x32x16_bf16 v[112:127], a[100:103], v[48:51], v[112:127]
	v_and_b32 v198, 0xffff0000, v242
	v_lshlrev_b32 v195, 16, v243
	v_and_b32 v199, 0xffff0000, v243
	v_mul_f32 v192, v192, v192
	v_mul_f32 v193, v193, v193
	v_mfma_f32_32x32x16_bf16 v[128:143], a[104:107], v[48:51], v[128:143]
	buffer_load_dwordx4 v[48:51], v4, s[20:23], s52 offen offset:0
	v_mul_f32 v194, v194, v194
	v_mul_f32 v195, v195, v195
	v_mul_f32 v196, v196, v196
	v_mul_f32 v197, v197, v197
	v_mul_f32 v198, v198, v198
	s_waitcnt vmcnt(21)
	v_mfma_f32_32x32x16_bf16 v[96:111], a[108:111], v[52:55], v[96:111]
	v_mul_f32 v199, v199, v199
	v_cvt_pk_bf16_f32 v240, v192, v196
	v_cvt_pk_bf16_f32 v241, v193, v197
	v_cvt_pk_bf16_f32 v242, v194, v198
	v_cvt_pk_bf16_f32 v243, v195, v199
	v_mfma_f32_32x32x16_bf16 v[112:127], a[112:115], v[52:55], v[112:127]
	buffer_store_dwordx4 v[240:243], v5, s[20:23], s36 offen sc1
	s_waitcnt lgkmcnt(3)
	v_add_f32 v200, v200, v160
	v_add_f32 v201, v201, v161
	v_add_f32 v202, v202, v162
	v_add_f32 v203, v203, v163
	v_mfma_f32_32x32x16_bf16 v[128:143], a[116:119], v[52:55], v[128:143]
	buffer_load_dwordx4 v[52:55], v4, s[20:23], s52 offen offset:1024
	s_waitcnt lgkmcnt(2)
	v_add_f32 v204, v204, v164
	v_add_f32 v205, v205, v165
	v_add_f32 v206, v206, v166
	v_add_f32 v207, v207, v167
	v_cvt_pk_bf16_f32 v244, v200, v204
	s_waitcnt vmcnt(22)
	v_mfma_f32_32x32x16_bf16 v[96:111], a[120:123], v[56:59], v[96:111]
	v_cvt_pk_bf16_f32 v245, v201, v205
	v_cvt_pk_bf16_f32 v246, v202, v206
	v_cvt_pk_bf16_f32 v247, v203, v207
	v_lshlrev_b32 v200, 16, v244
	v_and_b32 v204, 0xffff0000, v244
	v_mfma_f32_32x32x16_bf16 v[112:127], a[124:127], v[56:59], v[112:127]
	v_lshlrev_b32 v201, 16, v245
	v_and_b32 v205, 0xffff0000, v245
	v_lshlrev_b32 v202, 16, v246
	v_and_b32 v206, 0xffff0000, v246
	v_lshlrev_b32 v203, 16, v247
	v_mfma_f32_32x32x16_bf16 v[128:143], a[128:131], v[56:59], v[128:143]
	buffer_load_dwordx4 v[56:59], v4, s[20:23], s52 offen offset:2048
	v_and_b32 v207, 0xffff0000, v247
	v_add_f32 v200, v200, v224
	v_add_f32 v201, v201, v225
	v_add_f32 v202, v202, v226
	v_add_f32 v203, v203, v227
	s_waitcnt vmcnt(22)
	v_mfma_f32_32x32x16_bf16 v[96:111], a[132:135], v[60:63], v[96:111]
	v_add_f32 v204, v204, v228
	v_add_f32 v205, v205, v229
	v_add_f32 v206, v206, v230
	v_add_f32 v207, v207, v231
	v_max_f32 v200, 0, v200
	v_mfma_f32_32x32x16_bf16 v[112:127], a[136:139], v[60:63], v[112:127]
	v_max_f32 v201, 0, v201
	v_max_f32 v202, 0, v202
	v_max_f32 v203, 0, v203
	v_max_f32 v204, 0, v204
	v_max_f32 v205, 0, v205
	v_mfma_f32_32x32x16_bf16 v[128:143], a[140:143], v[60:63], v[128:143]
	buffer_load_dwordx4 v[60:63], v4, s[20:23], s52 offen offset:3072
	v_max_f32 v206, 0, v206
	v_max_f32 v207, 0, v207
	v_cvt_pk_bf16_f32 v244, v200, v204
	v_cvt_pk_bf16_f32 v245, v201, v205
	v_cvt_pk_bf16_f32 v246, v202, v206
	s_waitcnt vmcnt(22)
	v_mfma_f32_32x32x16_bf16 v[96:111], a[144:147], v[64:67], v[96:111]
	v_cvt_pk_bf16_f32 v247, v203, v207
	v_lshlrev_b32 v200, 16, v244
	v_and_b32 v204, 0xffff0000, v244
	v_lshlrev_b32 v201, 16, v245
	v_and_b32 v205, 0xffff0000, v245
	v_mfma_f32_32x32x16_bf16 v[112:127], a[148:151], v[64:67], v[112:127]
	v_lshlrev_b32 v202, 16, v246
	v_and_b32 v206, 0xffff0000, v246
	v_lshlrev_b32 v203, 16, v247
	v_and_b32 v207, 0xffff0000, v247
	v_mul_f32 v200, v200, v200
	v_mfma_f32_32x32x16_bf16 v[128:143], a[152:155], v[64:67], v[128:143]
	buffer_load_dwordx4 v[64:67], v4, s[20:23], s53 offen offset:0
	v_mul_f32 v201, v201, v201
	v_mul_f32 v202, v202, v202
	v_mul_f32 v203, v203, v203
	v_mul_f32 v204, v204, v204
	v_mul_f32 v205, v205, v205
	s_waitcnt vmcnt(21)
	v_mfma_f32_32x32x16_bf16 v[96:111], a[156:159], v[68:71], v[96:111]
	v_mul_f32 v206, v206, v206
	v_mul_f32 v207, v207, v207
	v_cvt_pk_bf16_f32 v244, v200, v204
	v_cvt_pk_bf16_f32 v245, v201, v205
	v_cvt_pk_bf16_f32 v246, v202, v206
	v_mfma_f32_32x32x16_bf16 v[112:127], a[160:163], v[68:71], v[112:127]
	v_cvt_pk_bf16_f32 v247, v203, v207
	buffer_store_dwordx4 v[244:247], v11, s[20:23], s36 offen sc1
	s_waitcnt lgkmcnt(1)
	v_add_f32 v208, v208, v176
	v_add_f32 v209, v209, v177
	v_add_f32 v210, v210, v178
	v_mfma_f32_32x32x16_bf16 v[128:143], a[164:167], v[68:71], v[128:143]
	buffer_load_dwordx4 v[68:71], v4, s[20:23], s53 offen offset:1024
	v_add_f32 v211, v211, v179
	s_waitcnt lgkmcnt(0)
	v_add_f32 v212, v212, v180
	v_add_f32 v213, v213, v181
	v_add_f32 v214, v214, v182
	v_add_f32 v215, v215, v183
	s_waitcnt vmcnt(22)
	v_mfma_f32_32x32x16_bf16 v[96:111], a[168:171], v[72:75], v[96:111]
	v_cvt_pk_bf16_f32 v248, v208, v212
	v_cvt_pk_bf16_f32 v249, v209, v213
	v_cvt_pk_bf16_f32 v250, v210, v214
	v_cvt_pk_bf16_f32 v251, v211, v215
	v_lshlrev_b32 v208, 16, v248
	v_mfma_f32_32x32x16_bf16 v[112:127], a[172:175], v[72:75], v[112:127]
	v_and_b32 v212, 0xffff0000, v248
	v_lshlrev_b32 v209, 16, v249
	v_and_b32 v213, 0xffff0000, v249
	v_lshlrev_b32 v210, 16, v250
	v_and_b32 v214, 0xffff0000, v250
	v_mfma_f32_32x32x16_bf16 v[128:143], a[176:179], v[72:75], v[128:143]
	buffer_load_dwordx4 v[72:75], v4, s[20:23], s53 offen offset:2048
	v_lshlrev_b32 v211, 16, v251
	v_and_b32 v215, 0xffff0000, v251
	v_add_f32 v208, v208, v232
	v_add_f32 v209, v209, v233
	v_add_f32 v210, v210, v234
	s_waitcnt vmcnt(22)
	v_mfma_f32_32x32x16_bf16 v[96:111], a[180:183], v[76:79], v[96:111]
	v_add_f32 v211, v211, v235
	v_add_f32 v212, v212, v236
	v_add_f32 v213, v213, v237
	v_add_f32 v214, v214, v238
	v_add_f32 v215, v215, v239
	v_mfma_f32_32x32x16_bf16 v[112:127], a[184:187], v[76:79], v[112:127]
	v_max_f32 v208, 0, v208
	v_max_f32 v209, 0, v209
	v_max_f32 v210, 0, v210
	v_max_f32 v211, 0, v211
	v_max_f32 v212, 0, v212
	v_mfma_f32_32x32x16_bf16 v[128:143], a[188:191], v[76:79], v[128:143]
	buffer_load_dwordx4 v[76:79], v4, s[20:23], s53 offen offset:3072
	v_max_f32 v213, 0, v213
	v_max_f32 v214, 0, v214
	v_max_f32 v215, 0, v215
	v_cvt_pk_bf16_f32 v248, v208, v212
	v_cvt_pk_bf16_f32 v249, v209, v213
	s_waitcnt vmcnt(22)
	v_mfma_f32_32x32x16_bf16 v[96:111], a[192:195], v[80:83], v[96:111]
	v_cvt_pk_bf16_f32 v250, v210, v214
	v_cvt_pk_bf16_f32 v251, v211, v215
	v_lshlrev_b32 v208, 16, v248
	v_and_b32 v212, 0xffff0000, v248
	v_lshlrev_b32 v209, 16, v249
	v_mfma_f32_32x32x16_bf16 v[112:127], a[196:199], v[80:83], v[112:127]
	v_and_b32 v213, 0xffff0000, v249
	v_lshlrev_b32 v210, 16, v250
	v_and_b32 v214, 0xffff0000, v250
	v_lshlrev_b32 v211, 16, v251
	v_and_b32 v215, 0xffff0000, v251
	v_mfma_f32_32x32x16_bf16 v[128:143], a[200:203], v[80:83], v[128:143]
	buffer_load_dwordx4 v[80:83], v4, s[20:23], s54 offen offset:0
	v_mul_f32 v208, v208, v208
	v_mul_f32 v209, v209, v209
	v_mul_f32 v210, v210, v210
	v_mul_f32 v211, v211, v211
	v_mul_f32 v212, v212, v212
	s_waitcnt vmcnt(21)
	v_mfma_f32_32x32x16_bf16 v[96:111], a[204:207], v[84:87], v[96:111]
	v_mul_f32 v213, v213, v213
	v_mul_f32 v214, v214, v214
	v_mul_f32 v215, v215, v215
	v_cvt_pk_bf16_f32 v248, v208, v212
	v_cvt_pk_bf16_f32 v249, v209, v213
	v_mfma_f32_32x32x16_bf16 v[112:127], a[208:211], v[84:87], v[112:127]
	v_cvt_pk_bf16_f32 v250, v210, v214
	v_cvt_pk_bf16_f32 v251, v211, v215
	buffer_store_dwordx4 v[248:251], v12, s[20:23], s36 offen sc1
	v_mfma_f32_32x32x16_bf16 v[128:143], a[212:215], v[84:87], v[128:143]
	buffer_load_dwordx4 v[84:87], v4, s[20:23], s54 offen offset:1024
	s_waitcnt vmcnt(22)
	v_mfma_f32_32x32x16_bf16 v[96:111], a[216:219], v[88:91], v[96:111]
	v_mfma_f32_32x32x16_bf16 v[112:127], a[220:223], v[88:91], v[112:127]
	v_mfma_f32_32x32x16_bf16 v[128:143], a[224:227], v[88:91], v[128:143]
	buffer_load_dwordx4 v[88:91], v4, s[20:23], s54 offen offset:2048
	s_waitcnt vmcnt(22)
	v_mfma_f32_32x32x16_bf16 v[96:111], a[228:231], v[92:95], v[96:111]
	v_mfma_f32_32x32x16_bf16 v[112:127], a[232:235], v[92:95], v[112:127]
	v_mfma_f32_32x32x16_bf16 v[128:143], a[236:239], v[92:95], v[128:143]
	buffer_load_dwordx4 v[92:95], v4, s[20:23], s54 offen offset:3072
	s_mov_b32 s36, s55
	s_add_u32 s55, s55, s37
	s_xor_b32 s55, s55, s41
	s_add_u32 s48, s48, 1
	s_branch .Lt1_seg26
.Lt1_out21:
	ds_write_b128 v6, v[152:155] offset:6144
	ds_write_b128 v6, v[156:159] offset:7168
	ds_write_b128 v6, v[168:171] offset:8192
	ds_write_b128 v6, v[172:175] offset:9216
	s_nop 0
	ds_write_b128 v6, v[184:187] offset:10240
	ds_write_b128 v6, v[188:191] offset:11264
	s_waitcnt lgkmcnt(0)
	s_barrier
	ds_read_b128 v[192:195], v7 offset:6144
	ds_read_b128 v[196:199], v7 offset:7168
	ds_read_b128 v[200:203], v7 offset:8192
	ds_read_b128 v[204:207], v7 offset:9216
	ds_read_b128 v[208:211], v7 offset:10240
	ds_read_b128 v[212:215], v7 offset:11264
	s_waitcnt lgkmcnt(5)
	v_add_f32 v192, v192, v144
	v_add_f32 v193, v193, v145
	v_add_f32 v194, v194, v146
	v_add_f32 v195, v195, v147
	s_waitcnt lgkmcnt(4)
	v_add_f32 v196, v196, v148
	v_add_f32 v197, v197, v149
	v_add_f32 v198, v198, v150
	v_add_f32 v199, v199, v151
	v_cvt_pk_bf16_f32 v240, v192, v196
	v_cvt_pk_bf16_f32 v241, v193, v197
	v_cvt_pk_bf16_f32 v242, v194, v198
	v_cvt_pk_bf16_f32 v243, v195, v199
	v_lshlrev_b32 v192, 16, v240
	v_and_b32 v196, 0xffff0000, v240
	v_lshlrev_b32 v193, 16, v241
	v_and_b32 v197, 0xffff0000, v241
	v_lshlrev_b32 v194, 16, v242
	v_and_b32 v198, 0xffff0000, v242
	v_lshlrev_b32 v195, 16, v243
	v_and_b32 v199, 0xffff0000, v243
	v_add_f32 v192, v192, v216
	v_add_f32 v193, v193, v217
	v_add_f32 v194, v194, v218
	v_add_f32 v195, v195, v219
	v_add_f32 v196, v196, v220
	v_add_f32 v197, v197, v221
	v_add_f32 v198, v198, v222
	v_add_f32 v199, v199, v223
	v_max_f32 v192, 0, v192
	v_max_f32 v193, 0, v193
	v_max_f32 v194, 0, v194
	v_max_f32 v195, 0, v195
	v_max_f32 v196, 0, v196
	v_max_f32 v197, 0, v197
	v_max_f32 v198, 0, v198
	v_max_f32 v199, 0, v199
	v_cvt_pk_bf16_f32 v240, v192, v196
	v_cvt_pk_bf16_f32 v241, v193, v197
	v_cvt_pk_bf16_f32 v242, v194, v198
	v_cvt_pk_bf16_f32 v243, v195, v199
	v_lshlrev_b32 v192, 16, v240
	v_and_b32 v196, 0xffff0000, v240
	v_lshlrev_b32 v193, 16, v241
	v_and_b32 v197, 0xffff0000, v241
	v_lshlrev_b32 v194, 16, v242
	v_and_b32 v198, 0xffff0000, v242
	v_lshlrev_b32 v195, 16, v243
	v_and_b32 v199, 0xffff0000, v243
	v_mul_f32 v192, v192, v192
	v_mul_f32 v193, v193, v193
	v_mul_f32 v194, v194, v194
	v_mul_f32 v195, v195, v195
	v_mul_f32 v196, v196, v196
	v_mul_f32 v197, v197, v197
	v_mul_f32 v198, v198, v198
	v_mul_f32 v199, v199, v199
	v_cvt_pk_bf16_f32 v240, v192, v196
	v_cvt_pk_bf16_f32 v241, v193, v197
	v_cvt_pk_bf16_f32 v242, v194, v198
	v_cvt_pk_bf16_f32 v243, v195, v199
	buffer_store_dwordx4 v[240:243], v5, s[20:23], s36 offen sc1
	s_waitcnt lgkmcnt(3)
	v_add_f32 v200, v200, v160
	v_add_f32 v201, v201, v161
	v_add_f32 v202, v202, v162
	v_add_f32 v203, v203, v163
	s_waitcnt lgkmcnt(2)
	v_add_f32 v204, v204, v164
	v_add_f32 v205, v205, v165
	v_add_f32 v206, v206, v166
	v_add_f32 v207, v207, v167
	v_cvt_pk_bf16_f32 v244, v200, v204
	v_cvt_pk_bf16_f32 v245, v201, v205
	v_cvt_pk_bf16_f32 v246, v202, v206
	v_cvt_pk_bf16_f32 v247, v203, v207
	v_lshlrev_b32 v200, 16, v244
	v_and_b32 v204, 0xffff0000, v244
	v_lshlrev_b32 v201, 16, v245
	v_and_b32 v205, 0xffff0000, v245
	v_lshlrev_b32 v202, 16, v246
	v_and_b32 v206, 0xffff0000, v246
	v_lshlrev_b32 v203, 16, v247
	v_and_b32 v207, 0xffff0000, v247
	v_add_f32 v200, v200, v224
	v_add_f32 v201, v201, v225
	v_add_f32 v202, v202, v226
	v_add_f32 v203, v203, v227
	v_add_f32 v204, v204, v228
	v_add_f32 v205, v205, v229
	v_add_f32 v206, v206, v230
	v_add_f32 v207, v207, v231
	v_max_f32 v200, 0, v200
	v_max_f32 v201, 0, v201
	v_max_f32 v202, 0, v202
	v_max_f32 v203, 0, v203
	v_max_f32 v204, 0, v204
	v_max_f32 v205, 0, v205
	v_max_f32 v206, 0, v206
	v_max_f32 v207, 0, v207
	v_cvt_pk_bf16_f32 v244, v200, v204
	v_cvt_pk_bf16_f32 v245, v201, v205
	v_cvt_pk_bf16_f32 v246, v202, v206
	v_cvt_pk_bf16_f32 v247, v203, v207
	v_lshlrev_b32 v200, 16, v244
	v_and_b32 v204, 0xffff0000, v244
	v_lshlrev_b32 v201, 16, v245
	v_and_b32 v205, 0xffff0000, v245
	v_lshlrev_b32 v202, 16, v246
	v_and_b32 v206, 0xffff0000, v246
	v_lshlrev_b32 v203, 16, v247
	v_and_b32 v207, 0xffff0000, v247
	v_mul_f32 v200, v200, v200
	v_mul_f32 v201, v201, v201
	v_mul_f32 v202, v202, v202
	v_mul_f32 v203, v203, v203
	v_mul_f32 v204, v204, v204
	v_mul_f32 v205, v205, v205
	v_mul_f32 v206, v206, v206
	v_mul_f32 v207, v207, v207
	v_cvt_pk_bf16_f32 v244, v200, v204
	v_cvt_pk_bf16_f32 v245, v201, v205
	v_cvt_pk_bf16_f32 v246, v202, v206
	v_cvt_pk_bf16_f32 v247, v203, v207
	buffer_store_dwordx4 v[244:247], v11, s[20:23], s36 offen sc1
	s_waitcnt lgkmcnt(1)
	v_add_f32 v208, v208, v176
	v_add_f32 v209, v209, v177
	v_add_f32 v210, v210, v178
	v_add_f32 v211, v211, v179
	s_waitcnt lgkmcnt(0)
	v_add_f32 v212, v212, v180
	v_add_f32 v213, v213, v181
	v_add_f32 v214, v214, v182
	v_add_f32 v215, v215, v183
	v_cvt_pk_bf16_f32 v248, v208, v212
	v_cvt_pk_bf16_f32 v249, v209, v213
	v_cvt_pk_bf16_f32 v250, v210, v214
	v_cvt_pk_bf16_f32 v251, v211, v215
	v_lshlrev_b32 v208, 16, v248
	v_and_b32 v212, 0xffff0000, v248
	v_lshlrev_b32 v209, 16, v249
	v_and_b32 v213, 0xffff0000, v249
	v_lshlrev_b32 v210, 16, v250
	v_and_b32 v214, 0xffff0000, v250
	v_lshlrev_b32 v211, 16, v251
	v_and_b32 v215, 0xffff0000, v251
	v_add_f32 v208, v208, v232
	v_add_f32 v209, v209, v233
	v_add_f32 v210, v210, v234
	v_add_f32 v211, v211, v235
	v_add_f32 v212, v212, v236
	v_add_f32 v213, v213, v237
	v_add_f32 v214, v214, v238
	v_add_f32 v215, v215, v239
	v_max_f32 v208, 0, v208
	v_max_f32 v209, 0, v209
	v_max_f32 v210, 0, v210
	v_max_f32 v211, 0, v211
	v_max_f32 v212, 0, v212
	v_max_f32 v213, 0, v213
	v_max_f32 v214, 0, v214
	v_max_f32 v215, 0, v215
	v_cvt_pk_bf16_f32 v248, v208, v212
	v_cvt_pk_bf16_f32 v249, v209, v213
	v_cvt_pk_bf16_f32 v250, v210, v214
	v_cvt_pk_bf16_f32 v251, v211, v215
	v_lshlrev_b32 v208, 16, v248
	v_and_b32 v212, 0xffff0000, v248
	v_lshlrev_b32 v209, 16, v249
	v_and_b32 v213, 0xffff0000, v249
	v_lshlrev_b32 v210, 16, v250
	v_and_b32 v214, 0xffff0000, v250
	v_lshlrev_b32 v211, 16, v251
	v_and_b32 v215, 0xffff0000, v251
	v_mul_f32 v208, v208, v208
	v_mul_f32 v209, v209, v209
	v_mul_f32 v210, v210, v210
	v_mul_f32 v211, v211, v211
	v_mul_f32 v212, v212, v212
	v_mul_f32 v213, v213, v213
	v_mul_f32 v214, v214, v214
	v_mul_f32 v215, v215, v215
	v_cvt_pk_bf16_f32 v248, v208, v212
	v_cvt_pk_bf16_f32 v249, v209, v213
	v_cvt_pk_bf16_f32 v250, v210, v214
	v_cvt_pk_bf16_f32 v251, v211, v215
	buffer_store_dwordx4 v[248:251], v12, s[20:23], s36 offen sc1
	s_waitcnt vmcnt(0)
	s_barrier
	v_mov_b32 v9, 64
	s_mov_b64 exec, s[60:61]
	buffer_store_dword v9, off, s[20:23], s38 sc1
	s_mov_b64 exec, -1
	s_branch .Lt1_end11
.Lt1_x1:
	s_waitcnt vmcnt(0) lgkmcnt(0)
	s_mov_b32 s47, -1
	s_mov_b32 m0, s19
.Lt1_end11:
	s_waitcnt vmcnt(0) lgkmcnt(0)
)ASM" : "+{v[0:15]}"(vi), "+{s[32:47]}"(si) : "{s[20:23]}"(rs), "{s[24:27]}"(rw), "{s[28:31]}"(rb) : "a0", "a1", "a2", "a3", "a4", "a5", "a6", "a7", "a8", "a9", "a10", "a11", "a12", "a13", "a14", "a15", "a16", "a17", "a18", "a19", "a20", "a21", "a22", "a23", "a24", "a25", "a26", "a27", "a28", "a29", "a30", "a31", "a32", "a33", "a34", "a35", "a36", "a37", "a38", "a39", "a40", "a41", "a42", "a43", "a44", "a45", "a46", "a47", "a48", "a49", "a50", "a51", "a52", "a53", "a54", "a55", "a56", "a57", "a58", "a59", "a60", "a61", "a62", "a63", "a64", "a65", "a66", "a67", "a68", "a69", "a70", "a71", "a72", "a73", "a74", "a75", "a76", "a77", "a78", "a79", "a80", "a81", "a82", "a83", "a84", "a85", "a86", "a87", "a88", "a89", "a90", "a91", "a92", "a93", "a94", "a95", "a96", "a97", "a98", "a99", "a100", "a101", "a102", "a103", "a104", "a105", "a106", "a107", "a108", "a109", "a110", "a111", "a112", "a113", "a114", "a115", "a116", "a117", "a118", "a119", "a120", "a121", "a122", "a123", "a124", "a125", "a126", "a127", "a128", "a129", "a130", "a131", "a132", "a133", "a134", "a135", "a136", "a137", "a138", "a139", "a140", "a141", "a142", "a143", "a144", "a145", "a146", "a147", "a148", "a149", "a150", "a151", "a152", "a153", "a154", "a155", "a156", "a157", "a158", "a159", "a160", "a161", "a162", "a163", "a164", "a165", "a166", "a167", "a168", "a169", "a170", "a171", "a172", "a173", "a174", "a175", "a176", "a177", "a178", "a179", "a180", "a181", "a182", "a183", "a184", "a185", "a186", "a187", "a188", "a189", "a190", "a191", "a192", "a193", "a194", "a195", "a196", "a197", "a198", "a199", "a200", "a201", "a202", "a203", "a204", "a205", "a206", "a207", "a208", "a209", "a210", "a211", "a212", "a213", "a214", "a215", "a216", "a217", "a218", "a219", "a220", "a221", "a222", "a223", "a224", "a225", "a226", "a227", "a228", "a229", "a230", "a231", "a232", "a233", "a234", "a235", "a236", "a237", "a238", "a239", "s18", "s19", "s48", "s50", "s51", "s52", "s53", "s54", "s55", "s58", "s59", "s60", "s61", "s63", "s64", "s65", "s69", "s70", "s71", "s72", "s76", "s98", "s99", "v16", "v17", "v18", "v19", "v20", "v21", "v22", "v23", "v24", "v25", "v26", "v27", "v28", "v29", "v30", "v31", "v32", "v33", "v34", "v35", "v36", "v37", "v38", "v39", "v40", "v41", "v42", "v43", "v44", "v45", "v46", "v47", "v48", "v49", "v50", "v51", "v52", "v53", "v54", "v55", "v56", "v57", "v58", "v59", "v60", "v61", "v62", "v63", "v64", "v65", "v66", "v67", "v68", "v69", "v70", "v71", "v72", "v73", "v74", "v75", "v76", "v77", "v78", "v79", "v80", "v81", "v82", "v83", "v84", "v85", "v86", "v87", "v88", "v89", "v90", "v91", "v92", "v93", "v94", "v95", "v96", "v97", "v98", "v99", "v100", "v101", "v102", "v103", "v104", "v105", "v106", "v107", "v108", "v109", "v110", "v111", "v112", "v113", "v114", "v115", "v116", "v117", "v118", "v119", "v120", "v121", "v122", "v123", "v124", "v125", "v126", "v127", "v128", "v129", "v130", "v131", "v132", "v133", "v134", "v135", "v136", "v137", "v138", "v139", "v140", "v141", "v142", "v143", "v144", "v145", "v146", "v147", "v148", "v149", "v150", "v151", "v152", "v153", "v154", "v155", "v156", "v157", "v158", "v159", "v160", "v161", "v162", "v163", "v164", "v165", "v166", "v167", "v168", "v169", "v170", "v171", "v172", "v173", "v174", "v175", "v176", "v177", "v178", "v179", "v180", "v181", "v182", "v183", "v184", "v185", "v186", "v187", "v188", "v189", "v190", "v191", "v192", "v193", "v194", "v195", "v196", "v197", "v198", "v199", "v200", "v201", "v202", "v203", "v204", "v205", "v206", "v207", "v208", "v209", "v210", "v211", "v212", "v213", "v214", "v215", "v216", "v217", "v218", "v219", "v220", "v221", "v222", "v223", "v224", "v225", "v226", "v227", "v228", "v229", "v230", "v231", "v232", "v233", "v234", "v235", "v236", "v237", "v238", "v239", "v240", "v241", "v242", "v243", "v244", "v245", "v246", "v247", "v248", "v249", "v250", "v251", "v252", "v253", "v254", "v255", "vcc", "scc", "memory");
  return si[15] == ~0u ? 2 : si[11] >> 13;
}

DI unsigned role_t2(KArgs& a, const int i2, const unsigned hp, ldsp lds, const int lane, const int wv) {
  const u16* W = i2 < 32 ? a.Wo + (size_t)i2 * 192 * 640 : a.w2 + (size_t)(i2 - 32) * 192 * 640;
  const unsigned reg = la(lds) + wv * WREG, sd = 0;
  const int gm = lane >> 4, q = (lane >> 2) & 3, p = lane & 3, c32 = lane & 31;
  u32x16 vi = (u32x16)0u, si = (u32x16)0u;
#pragma unroll
  for (int r = 0; r < 5; ++r) vi[r] = wv * 10240 + (r * 64 + lane) * 16;
  vi[5] = la(lds) + 2 * (q & 1) * WREG + (4 * (gm >> 1) + (q >> 1)) * 1280 + wv * 320 + (4 * (gm & 1) + p) * 8;
  vi[6] = (i2 < 32 ? lane ^ 8 * wv : lane) * 16;
  const unsigned so = (c32 >> 4) * 1024 + (lane >> 5) * 512 + (c32 & 15) * 16;
  vi[7] = so + uoff(sd, 5 * wv, i2);
  vi[13] = i2 < 32 && ES ? OOB : vi[7];
  vi[11] = i2 < 32 && UR < 64 ? so + unoff(sd, 5 * wv, i2) : OOB;
  vi[12] = lane && i2 < 32 ? progo(sd) : flag1(sd, 64 + i2);
#pragma unroll
  for (int r = 0; r < 3; ++r) vi[8 + r] = t1p(sd, 64 + i2, (3 * wv + r) * 1024 + lane * 16) - t1p(sd, 64 + i2, 0);
  vi[14] = la(lds) + wv * 3072 + lane * 16;
  si[1] = reg; si[2] = t1p(sd, 64 + i2, 0) + (i2 < 32 ? 0 : nb(sd, 0)); si[3] = i2 < 32 ? BLK : BLK / 2; si[8] = i2 < 32 ? 0 : 8192; si[7] = opq(wv); si[12] = i2 >= 32 ? MW2 : 0; si[4] = flag2(sd, i2); si[6] = la(lds);
  si[11] = opq(hp); si[15] = dbgo(208 + i2, wv);
#pragma unroll
  for (int i = 0; i < 16; ++i) si[i] = rfl(si[i]);
  const u32x4 rs = rsw(a.ws, WS_RANGE), rw = rsw(W, 192u * 1280u);
  asm volatile(R"ASM(
	v_mov_b32 v176, v8
	v_mov_b32 v177, v9
	v_mov_b32 v178, v10
	v_mov_b32 v179, v14
	v_mbcnt_lo_u32_b32 v15, -1, 0
	v_mbcnt_hi_u32_b32 v15, -1, v15
	v_lshlrev_b32 v15, 2, v15
	s_mov_b32 s19, m0
	s_mov_b32 s18, 12566528
	s_mov_b32 s69, 0x20000
	buffer_load_dword v9, off, s[20:23], s18 offset:3968 sc1
	s_add_u32 s53, s32, 0
	s_add_u32 m0, s33, 0
	s_nop 0
	buffer_load_dwordx4 v0, s[24:27], s53 offen nt lds
	s_add_u32 m0, s33, 1024
	s_nop 0
	buffer_load_dwordx4 v1, s[24:27], s53 offen nt lds
	s_add_u32 m0, s33, 2048
	s_nop 0
	buffer_load_dwordx4 v2, s[24:27], s53 offen nt lds
	s_add_u32 m0, s33, 3072
	s_nop 0
	buffer_load_dwordx4 v3, s[24:27], s53 offen nt lds
	s_add_u32 m0, s33, 4096
	s_nop 0
	buffer_load_dwordx4 v4, s[24:27], s53 offen nt lds
	s_add_u32 s53, s32, 5120
	s_add_u32 m0, s33, 5120
	s_nop 0
	buffer_load_dwordx4 v0, s[24:27], s53 offen nt lds
	s_add_u32 m0, s33, 6144
	s_nop 0
	buffer_load_dwordx4 v1, s[24:27], s53 offen nt lds
	s_add_u32 m0, s33, 7168
	s_nop 0
	buffer_load_dwordx4 v2, s[24:27], s53 offen nt lds
	s_add_u32 m0, s33, 8192
	s_nop 0
	buffer_load_dwordx4 v3, s[24:27], s53 offen nt lds
	s_add_u32 m0, s33, 9216
	s_nop 0
	buffer_load_dwordx4 v4, s[24:27], s53 offen nt lds
	s_waitcnt vmcnt(10)
	v_readfirstlane_b32 s70, v9
	s_and_b32 s18, s43, 255
	s_add_u32 s70, s70, 0x9e377900
	s_andn2_b32 s70, s70, 255
	s_or_b32 s70, s70, s18
	s_lshr_b32 s64, s43, 8
	s_and_b32 s71, s64, 1024
	s_add_u32 s71, s71, 12566528
	s_add_u32 s64, s64, 12566528
	v_mov_b32 v9, s70
	s_bitcmp0_b32 s64, 11
	s_cselect_b64 exec, 1, 0
	buffer_store_dword v9, off, s[20:23], s64 sc1
	s_mov_b64 exec, -1
	s_cmp_eq_u32 s44, 0
	s_cbranch_scc1 .Lt2_l1
	s_memrealtime s[98:99]
	s_waitcnt lgkmcnt(0)
	s_add_u32 s76, s98, s44
	s_memrealtime s[98:99]
	s_waitcnt lgkmcnt(0)
	s_sub_u32 s18, s76, s98
	s_cmp_gt_u32 s18, 0xfa0
	s_cbranch_scc1 .Lt2_w3
.Lt2_w2:
	s_memrealtime s[98:99]
	s_waitcnt lgkmcnt(0)
	s_sub_u32 s18, s76, s98
	s_cmp_gt_i32 s18, 0
	s_cbranch_scc1 .Lt2_w2
.Lt2_w3:
.Lt2_l1:
	s_add_u32 s53, s32, 40960
	s_add_u32 m0, s33, 10240
	s_nop 0
	buffer_load_dwordx4 v0, s[24:27], s53 offen nt lds
	s_add_u32 m0, s33, 11264
	s_nop 0
	buffer_load_dwordx4 v1, s[24:27], s53 offen nt lds
	s_add_u32 m0, s33, 12288
	s_nop 0
	buffer_load_dwordx4 v2, s[24:27], s53 offen nt lds
	s_add_u32 m0, s33, 13312
	s_nop 0
	buffer_load_dwordx4 v3, s[24:27], s53 offen nt lds
	s_add_u32 m0, s33, 14336
	s_nop 0
	buffer_load_dwordx4 v4, s[24:27], s53 offen nt lds
	s_add_u32 s53, s32, 46080
	s_add_u32 m0, s33, 15360
	s_nop 0
	buffer_load_dwordx4 v0, s[24:27], s53 offen nt lds
	s_add_u32 m0, s33, 16384
	s_nop 0
	buffer_load_dwordx4 v1, s[24:27], s53 offen nt lds
	s_add_u32 m0, s33, 17408
	s_nop 0
	buffer_load_dwordx4 v2, s[24:27], s53 offen nt lds
	s_add_u32 m0, s33, 18432
	s_nop 0
	buffer_load_dwordx4 v3, s[24:27], s53 offen nt lds
	s_add_u32 m0, s33, 19456
	s_nop 0
	buffer_load_dwordx4 v4, s[24:27], s53 offen nt lds
	s_add_u32 s53, s32, 81920
	s_add_u32 m0, s33, 20480
	s_nop 0
	buffer_load_dwordx4 v0, s[24:27], s53 offen nt lds
	s_add_u32 m0, s33, 21504
	s_nop 0
	buffer_load_dwordx4 v1, s[24:27], s53 offen nt lds
	s_add_u32 m0, s33, 22528
	s_nop 0
	buffer_load_dwordx4 v2, s[24:27], s53 offen nt lds
	s_add_u32 m0, s33, 23552
	s_nop 0
	buffer_load_dwordx4 v3, s[24:27], s53 offen nt lds
	s_add_u32 m0, s33, 24576
	s_nop 0
	buffer_load_dwordx4 v4, s[24:27], s53 offen nt lds
	s_add_u32 s53, s32, 87040
	s_add_u32 m0, s33, 25600
	s_nop 0
	buffer_load_dwordx4 v0, s[24:27], s53 offen nt lds
	s_add_u32 m0, s33, 26624
	s_nop 0
	buffer_load_dwordx4 v1, s[24:27], s53 offen nt lds
	s_add_u32 m0, s33, 27648
	s_nop 0
	buffer_load_dwordx4 v2, s[24:27], s53 offen nt lds
	s_add_u32 m0, s33, 28672
	s_nop 0
	buffer_load_dwordx4 v3, s[24:27], s53 offen nt lds
	s_add_u32 m0, s33, 29696
	s_nop 0
	buffer_load_dwordx4 v4, s[24:27], s53 offen nt lds
	v_add_u32 v180, s38, v6
	v_mov_b32 v181, s38
	v_add_u32 v181, 147456, v181
	v_mov_b32 v10, 0
	ds_write2_b32 v181, v10, v10 offset1:1
	s_waitcnt vmcnt(20)
	s_barrier
	ds_read_b64_tr_b16 a[0:1], v5 offset:0
	ds_read_b64_tr_b16 a[2:3], v5 offset:2560
	ds_read_b64_tr_b16 a[4:5], v5 offset:64
	ds_read_b64_tr_b16 a[6:7], v5 offset:2624
	ds_read_b64_tr_b16 a[8:9], v5 offset:128
	ds_read_b64_tr_b16 a[10:11], v5 offset:2688
	ds_read_b64_tr_b16 a[12:13], v5 offset:192
	ds_read_b64_tr_b16 a[14:15], v5 offset:2752
	ds_read_b64_tr_b16 a[16:17], v5 offset:256
	ds_read_b64_tr_b16 a[18:19], v5 offset:2816
	ds_read_b64_tr_b16 a[20:21], v5 offset:36864
	ds_read_b64_tr_b16 a[22:23], v5 offset:39424
	ds_read_b64_tr_b16 a[24:25], v5 offset:36928
	ds_read_b64_tr_b16 a[26:27], v5 offset:39488
	ds_read_b64_tr_b16 a[28:29], v5 offset:36992
	ds_read_b64_tr_b16 a[30:31], v5 offset:39552
	ds_read_b64_tr_b16 a[32:33], v5 offset:37056
	ds_read_b64_tr_b16 a[34:35], v5 offset:39616
	ds_read_b64_tr_b16 a[36:37], v5 offset:37120
	ds_read_b64_tr_b16 a[38:39], v5 offset:39680
	s_waitcnt lgkmcnt(0)
	s_barrier
	s_add_u32 s53, s32, 122880
	s_add_u32 m0, s33, 0
	s_nop 0
	buffer_load_dwordx4 v0, s[24:27], s53 offen nt lds
	s_add_u32 m0, s33, 1024
	s_nop 0
	buffer_load_dwordx4 v1, s[24:27], s53 offen nt lds
	s_add_u32 m0, s33, 2048
	s_nop 0
	buffer_load_dwordx4 v2, s[24:27], s53 offen nt lds
	s_add_u32 m0, s33, 3072
	s_nop 0
	buffer_load_dwordx4 v3, s[24:27], s53 offen nt lds
	s_add_u32 m0, s33, 4096
	s_nop 0
	buffer_load_dwordx4 v4, s[24:27], s53 offen nt lds
	s_add_u32 s53, s32, 128000
	s_add_u32 m0, s33, 5120
	s_nop 0
	buffer_load_dwordx4 v0, s[24:27], s53 offen nt lds
	s_add_u32 m0, s33, 6144
	s_nop 0
	buffer_load_dwordx4 v1, s[24:27], s53 offen nt lds
	s_add_u32 m0, s33, 7168
	s_nop 0
	buffer_load_dwordx4 v2, s[24:27], s53 offen nt lds
	s_add_u32 m0, s33, 8192
	s_nop 0
	buffer_load_dwordx4 v3, s[24:27], s53 offen nt lds
	s_add_u32 m0, s33, 9216
	s_nop 0
	buffer_load_dwordx4 v4, s[24:27], s53 offen nt lds
	s_waitcnt vmcnt(20)
	s_barrier
	ds_read_b64_tr_b16 a[40:41], v5 offset:10240
	ds_read_b64_tr_b16 a[42:43], v5 offset:12800
	ds_read_b64_tr_b16 a[44:45], v5 offset:10304
	ds_read_b64_tr_b16 a[46:47], v5 offset:12864
	ds_read_b64_tr_b16 a[48:49], v5 offset:10368
	ds_read_b64_tr_b16 a[50:51], v5 offset:12928
	ds_read_b64_tr_b16 a[52:53], v5 offset:10432
	ds_read_b64_tr_b16 a[54:55], v5 offset:12992
	ds_read_b64_tr_b16 a[56:57], v5 offset:10496
	ds_read_b64_tr_b16 a[58:59], v5 offset:13056
	ds_read_b64_tr_b16 a[60:61], v5 offset:47104
	ds_read_b64_tr_b16 a[62:63], v5 offset:49664
	ds_read_b64_tr_b16 a[64:65], v5 offset:47168
	ds_read_b64_tr_b16 a[66:67], v5 offset:49728
	ds_read_b64_tr_b16 a[68:69], v5 offset:47232
	ds_read_b64_tr_b16 a[70:71], v5 offset:49792
	ds_read_b64_tr_b16 a[72:73], v5 offset:47296
	ds_read_b64_tr_b16 a[74:75], v5 offset:49856
	ds_read_b64_tr_b16 a[76:77], v5 offset:47360
	ds_read_b64_tr_b16 a[78:79], v5 offset:49920
	s_waitcnt lgkmcnt(0)
	s_barrier
	s_add_u32 s53, s32, 163840
	s_add_u32 m0, s33, 10240
	s_nop 0
	buffer_load_dwordx4 v0, s[24:27], s53 offen nt lds
	s_add_u32 m0, s33, 11264
	s_nop 0
	buffer_load_dwordx4 v1, s[24:27], s53 offen nt lds
	s_add_u32 m0, s33, 12288
	s_nop 0
	buffer_load_dwordx4 v2, s[24:27], s53 offen nt lds
	s_add_u32 m0, s33, 13312
	s_nop 0
	buffer_load_dwordx4 v3, s[24:27], s53 offen nt lds
	s_add_u32 m0, s33, 14336
	s_nop 0
	buffer_load_dwordx4 v4, s[24:27], s53 offen nt lds
	s_add_u32 s53, s32, 168960
	s_add_u32 m0, s33, 15360
	s_nop 0
	buffer_load_dwordx4 v0, s[24:27], s53 offen nt lds
	s_add_u32 m0, s33, 16384
	s_nop 0
	buffer_load_dwordx4 v1, s[24:27], s53 offen nt lds
	s_add_u32 m0, s33, 17408
	s_nop 0
	buffer_load_dwordx4 v2, s[24:27], s53 offen nt lds
	s_add_u32 m0, s33, 18432
	s_nop 0
	buffer_load_dwordx4 v3, s[24:27], s53 offen nt lds
	s_add_u32 m0, s33, 19456
	s_nop 0
	buffer_load_dwordx4 v4, s[24:27], s53 offen nt lds
	s_waitcnt vmcnt(20)
	s_barrier
	ds_read_b64_tr_b16 a[80:81], v5 offset:20480
	ds_read_b64_tr_b16 a[82:83], v5 offset:23040
	ds_read_b64_tr_b16 a[84:85], v5 offset:20544
	ds_read_b64_tr_b16 a[86:87], v5 offset:23104
	ds_read_b64_tr_b16 a[88:89], v5 offset:20608
	ds_read_b64_tr_b16 a[90:91], v5 offset:23168
	ds_read_b64_tr_b16 a[92:93], v5 offset:20672
	ds_read_b64_tr_b16 a[94:95], v5 offset:23232
	ds_read_b64_tr_b16 a[96:97], v5 offset:20736
	ds_read_b64_tr_b16 a[98:99], v5 offset:23296
	ds_read_b64_tr_b16 a[100:101], v5 offset:57344
	ds_read_b64_tr_b16 a[102:103], v5 offset:59904
	ds_read_b64_tr_b16 a[104:105], v5 offset:57408
	ds_read_b64_tr_b16 a[106:107], v5 offset:59968
	ds_read_b64_tr_b16 a[108:109], v5 offset:57472
	ds_read_b64_tr_b16 a[110:111], v5 offset:60032
	ds_read_b64_tr_b16 a[112:113], v5 offset:57536
	ds_read_b64_tr_b16 a[114:115], v5 offset:60096
	ds_read_b64_tr_b16 a[116:117], v5 offset:57600
	ds_read_b64_tr_b16 a[118:119], v5 offset:60160
	s_waitcnt lgkmcnt(0)
	s_barrier
	s_add_u32 s53, s32, 204800
	s_add_u32 m0, s33, 20480
	s_nop 0
	buffer_load_dwordx4 v0, s[24:27], s53 offen nt lds
	s_add_u32 m0, s33, 21504
	s_nop 0
	buffer_load_dwordx4 v1, s[24:27], s53 offen nt lds
	s_add_u32 m0, s33, 22528
	s_nop 0
	buffer_load_dwordx4 v2, s[24:27], s53 offen nt lds
	s_add_u32 m0, s33, 23552
	s_nop 0
	buffer_load_dwordx4 v3, s[24:27], s53 offen nt lds
	s_add_u32 m0, s33, 24576
	s_nop 0
	buffer_load_dwordx4 v4, s[24:27], s53 offen nt lds
	s_add_u32 s53, s32, 209920
	s_add_u32 m0, s33, 25600
	s_nop 0
	buffer_load_dwordx4 v0, s[24:27], s53 offen nt lds
	s_add_u32 m0, s33, 26624
	s_nop 0
	buffer_load_dwordx4 v1, s[24:27], s53 offen nt lds
	s_add_u32 m0, s33, 27648
	s_nop 0
	buffer_load_dwordx4 v2, s[24:27], s53 offen nt lds
	s_add_u32 m0, s33, 28672
	s_nop 0
	buffer_load_dwordx4 v3, s[24:27], s53 offen nt lds
	s_add_u32 m0, s33, 29696
	s_nop 0
	buffer_load_dwordx4 v4, s[24:27], s53 offen nt lds
	buffer_load_dwordx4 v[160:163], v6, s[20:23], s71 offen offset:0 sc1
	s_waitcnt vmcnt(21)
	s_barrier
	ds_read_b64_tr_b16 a[120:121], v5 offset:0
	ds_read_b64_tr_b16 a[122:123], v5 offset:2560
	ds_read_b64_tr_b16 a[124:125], v5 offset:64
	ds_read_b64_tr_b16 a[126:127], v5 offset:2624
	ds_read_b64_tr_b16 a[128:129], v5 offset:128
	ds_read_b64_tr_b16 a[130:131], v5 offset:2688
	ds_read_b64_tr_b16 a[132:133], v5 offset:192
	ds_read_b64_tr_b16 a[134:135], v5 offset:2752
	ds_read_b64_tr_b16 a[136:137], v5 offset:256
	ds_read_b64_tr_b16 a[138:139], v5 offset:2816
	ds_read_b64_tr_b16 a[140:141], v5 offset:36864
	ds_read_b64_tr_b16 a[142:143], v5 offset:39424
	ds_read_b64_tr_b16 a[144:145], v5 offset:36928
	ds_read_b64_tr_b16 a[146:147], v5 offset:39488
	ds_read_b64_tr_b16 a[148:149], v5 offset:36992
	ds_read_b64_tr_b16 a[150:151], v5 offset:39552
	ds_read_b64_tr_b16 a[152:153], v5 offset:37056
	ds_read_b64_tr_b16 a[154:155], v5 offset:39616
	ds_read_b64_tr_b16 a[156:157], v5 offset:37120
	ds_read_b64_tr_b16 a[158:159], v5 offset:39680
	s_waitcnt lgkmcnt(0)
	s_waitcnt vmcnt(11)
	s_barrier
	ds_read_b64_tr_b16 a[160:161], v5 offset:10240
	ds_read_b64_tr_b16 a[162:163], v5 offset:12800
	ds_read_b64_tr_b16 a[164:165], v5 offset:10304
	ds_read_b64_tr_b16 a[166:167], v5 offset:12864
	ds_read_b64_tr_b16 a[168:169], v5 offset:10368
	ds_read_b64_tr_b16 a[170:171], v5 offset:12928
	ds_read_b64_tr_b16 a[172:173], v5 offset:10432
	ds_read_b64_tr_b16 a[174:175], v5 offset:12992
	ds_read_b64_tr_b16 a[176:177], v5 offset:10496
	ds_read_b64_tr_b16 a[178:179], v5 offset:13056
	ds_read_b64_tr_b16 a[180:181], v5 offset:47104
	ds_read_b64_tr_b16 a[182:183], v5 offset:49664
	ds_read_b64_tr_b16 a[184:185], v5 offset:47168
	ds_read_b64_tr_b16 a[186:187], v5 offset:49728
	ds_read_b64_tr_b16 a[188:189], v5 offset:47232
	ds_read_b64_tr_b16 a[190:191], v5 offset:49792
	ds_read_b64_tr_b16 a[192:193], v5 offset:47296
	ds_read_b64_tr_b16 a[194:195], v5 offset:49856
	ds_read_b64_tr_b16 a[196:197], v5 offset:47360
	ds_read_b64_tr_b16 a[198:199], v5 offset:49920
	s_waitcnt lgkmcnt(0)
	s_waitcnt vmcnt(1)
	s_barrier
	ds_read_b64_tr_b16 a[200:201], v5 offset:20480
	ds_read_b64_tr_b16 a[202:203], v5 offset:23040
	ds_read_b64_tr_b16 a[204:205], v5 offset:20544
	ds_read_b64_tr_b16 a[206:207], v5 offset:23104
	ds_read_b64_tr_b16 a[208:209], v5 offset:20608
	ds_read_b64_tr_b16 a[210:211], v5 offset:23168
	ds_read_b64_tr_b16 a[212:213], v5 offset:20672
	ds_read_b64_tr_b16 a[214:215], v5 offset:23232
	ds_read_b64_tr_b16 a[216:217], v5 offset:20736
	ds_read_b64_tr_b16 a[218:219], v5 offset:23296
	ds_read_b64_tr_b16 a[220:221], v5 offset:57344
	ds_read_b64_tr_b16 a[222:223], v5 offset:59904
	ds_read_b64_tr_b16 a[224:225], v5 offset:57408
	ds_read_b64_tr_b16 a[226:227], v5 offset:59968
	ds_read_b64_tr_b16 a[228:229], v5 offset:57472
	ds_read_b64_tr_b16 a[230:231], v5 offset:60032
	ds_read_b64_tr_b16 a[232:233], v5 offset:57536
	ds_read_b64_tr_b16 a[234:235], v5 offset:60096
	ds_read_b64_tr_b16 a[236:237], v5 offset:57600
	ds_read_b64_tr_b16 a[238:239], v5 offset:60160
	s_waitcnt lgkmcnt(0)
.Lt2_h6:
	s_waitcnt vmcnt(0)
	v_xor_b32 v160, s70, v160
	v_xor_b32 v161, s70, v161
	v_xor_b32 v162, s70, v162
	v_xor_b32 v163, s70, v163
	v_or3_b32 v9, v160, v161, v162
	v_or_b32 v9, v9, v163
	v_and_b32 v10, 0xffffff07, v9
	v_cmp_ne_u32 vcc, 0, v10
	s_cbranch_vccz .Lt2_h7
	v_cmp_lt_u32 vcc, 0xff, v9
	s_cbranch_vccz .Lt2_x4
	s_cmp_eq_u32 s69, 0
	s_cbranch_scc1 .Lt2_h7
	s_sub_u32 s69, s69, 1
	s_sleep 2
	buffer_load_dwordx4 v[160:163], v6, s[20:23], s71 offen offset:0 sc1
	s_waitcnt vmcnt(0)
	s_branch .Lt2_h6
.Lt2_h7:
	v_and_b32 v10, 64, v160
	v_cmp_ne_u32 vcc, 0, v10
	s_bcnt1_i32_b64 s72, vcc
	v_and_b32 v10, 64, v161
	v_cmp_ne_u32 vcc, 0, v10
	s_bcnt1_i32_b64 s18, vcc
	s_add_u32 s72, s72, s18
	v_and_b32 v10, 64, v162
	v_cmp_ne_u32 vcc, 0, v10
	s_bcnt1_i32_b64 s18, vcc
	s_add_u32 s72, s72, s18
	v_and_b32 v10, 64, v163
	v_cmp_ne_u32 vcc, 0, v10
	s_bcnt1_i32_b64 s18, vcc
	s_add_u32 s72, s72, s18
	s_cmp_ge_u32 s72, 20
	s_cselect_b32 s72, 8192, 0
	v_xor_b32 v7, s72, v7
	v_xor_b32 v11, s72, v11
	v_xor_b32 v12, s72, v12
	v_xor_b32 v13, s72, v13
	s_xor_b32 s34, s72, s34
	s_xor_b32 s36, s72, s36
	s_mov_b32 s43, s72
	s_mov_b32 m0, s19
	s_mov_b32 s48, 0
	s_mov_b32 s73, 0
	s_mov_b32 s69, 0x20000
	s_mov_b32 s51, 0
	s_mov_b32 s55, 655360
	s_mov_b32 s56, 67633152
	s_cmp_eq_u32 s39, 0
	s_cselect_b64 s[60:61], 1, 0
	v_mov_b64 v[128:129], 0
	v_mov_b64 v[130:131], 0
	v_mov_b64 v[132:133], 0
	v_mov_b64 v[134:135], 0
	v_mov_b64 v[136:137], 0
	v_mov_b64 v[138:139], 0
	v_mov_b64 v[140:141], 0
	v_mov_b64 v[142:143], 0
	v_mov_b32 v182, 0
	s_mov_b32 s62, 0
	buffer_load_dword v8, v12, s[20:23], 0 offen sc1
	s_waitcnt vmcnt(0)
	s_waitcnt lgkmcnt(0)
	s_barrier
	v_readfirstlane_b32 s53, v8
	v_readlane_b32 s18, v8, 1
	s_add_u32 s18, s18, 15
	s_min_u32 s53, s53, s18
	s_max_u32 s73, s73, s53
	s_add_u32 s78, s48, 1
.Lt2_f9:
	s_cmp_ge_u32 s73, s78
	s_cbranch_scc1 .Lt2_f10
	s_cmp_eq_u32 s69, 0
	s_cbranch_scc1 .Lt2_f10
	s_sub_u32 s69, s69, 1
	buffer_load_dword v9, v12, s[20:23], 0 offen sc1
	s_waitcnt vmcnt(0)
	v_readfirstlane_b32 s73, v9
	s_sleep 2
	v_readlane_b32 s18, v9, 1
	s_add_u32 s18, s18, 15
	s_min_u32 s73, s73, s18
	s_branch .Lt2_f9
.Lt2_f10:
	s_max_u32 s73, s73, s78
	s_add_u32 s79, s48, 1
	s_cmp_ge_u32 s51, s79
	s_cbranch_scc1 .Lt2_l11
	s_cmp_ge_u32 s73, s79
	s_cbranch_scc1 .Lt2_l12
	v_readfirstlane_b32 s53, v8
	v_readlane_b32 s18, v8, 1
	s_add_u32 s18, s18, 15
	s_min_u32 s53, s53, s18
	s_max_u32 s73, s73, s53
	s_cmp_lt_u32 s73, s79
	s_cbranch_scc1 .Lt2_l11
.Lt2_l12:
	buffer_load_dwordx4 v[164:167], v176, s[20:23], s34 offen offset:0 sc1
	buffer_load_dwordx4 v[168:171], v177, s[20:23], s34 offen offset:0 sc1
	buffer_load_dwordx4 v[172:175], v178, s[20:23], s34 offen offset:0 sc1
	s_mov_b32 s51, s79
	s_mov_b32 s59, 1
	s_add_u32 s34, s34, s35
	s_xor_b32 s34, s34, s40
.Lt2_l11:
	s_cmp_gt_u32 s51, s78
	s_cbranch_scc1 .Lt2_l13
	s_waitcnt vmcnt(2)
	ds_write_b128 v179, v[164:167] offset:0
	s_waitcnt vmcnt(1)
	ds_write_b128 v179, v[168:171] offset:1024
	s_waitcnt vmcnt(0)
	ds_write_b128 v179, v[172:175] offset:2048
.Lt2_l13:
	s_waitcnt lgkmcnt(0)
	s_barrier
	ds_read_b128 v[16:19], v180 offset:0
	ds_read_b128 v[20:23], v180 offset:1024
	ds_read_b128 v[24:27], v180 offset:2048
	ds_read_b128 v[28:31], v180 offset:3072
	ds_read_b128 v[32:35], v180 offset:4096
	ds_read_b128 v[36:39], v180 offset:5120
	ds_read_b128 v[40:43], v180 offset:6144
	ds_read_b128 v[44:47], v180 offset:7168
	ds_read_b128 v[48:51], v180 offset:8192
	ds_read_b128 v[52:55], v180 offset:9216
	ds_read_b128 v[56:59], v180 offset:10240
	ds_read_b128 v[60:63], v180 offset:11264
	s_or_b32 s63, s39, s44
	s_cselect_b32 s63, -1, 44
	s_cmp_lg_u32 s44, 0
	s_cbranch_scc1 .Lt2_l15
.Lt2_seg16:
	s_waitcnt lgkmcnt(11)
	v_mfma_f32_32x32x16_bf16 v[112:127], v[16:19], a[0:3], 0
	s_add_u32 s79, s48, 2
	s_cmp_ge_u32 s51, s79
	s_cbranch_scc1 .Lt2_l18
	s_cmp_ge_u32 s73, s79
	s_cbranch_scc1 .Lt2_l19
	s_waitcnt vmcnt(9)
	v_readfirstlane_b32 s53, v8
	v_readlane_b32 s18, v8, 1
	s_add_u32 s18, s18, 15
	s_min_u32 s53, s53, s18
	s_max_u32 s73, s73, s53
	s_cmp_lt_u32 s73, s79
	s_cbranch_scc1 .Lt2_l18
.Lt2_l19:
	buffer_load_dwordx4 v[164:167], v176, s[20:23], s34 offen offset:0 sc1
	buffer_load_dwordx4 v[168:171], v177, s[20:23], s34 offen offset:0 sc1
	buffer_load_dwordx4 v[172:175], v178, s[20:23], s34 offen offset:0 sc1
	s_mov_b32 s51, s79
	s_mov_b32 s59, 1
	s_add_u32 s34, s34, s35
	s_xor_b32 s34, s34, s40
.Lt2_l18:
	s_waitcnt lgkmcnt(10)
	v_mfma_f32_32x32x16_bf16 v[112:127], v[20:23], a[20:23], v[112:127]
	s_and_b32 s52, s37, 67108863
	s_cmp_lt_u32 s48, 8
	s_cselect_b32 s53, 67108864, 0
	s_add_u32 s52, s52, s53
	s_add_u32 s53, s48, 2
	s_cmp_ge_u32 s73, s53
	s_cbranch_scc1 .Lt2_l20
	buffer_load_dword v8, v12, s[20:23], 0 offen sc1
.Lt2_l20:
	s_waitcnt lgkmcnt(9)
	v_mfma_f32_32x32x16_bf16 v[112:127], v[24:27], a[40:43], v[112:127]
	v_cvt_pk_bf16_f32 v152, v128, v129
	v_cvt_pk_bf16_f32 v153, v130, v131
	s_waitcnt lgkmcnt(8)
	v_mfma_f32_32x32x16_bf16 v[112:127], v[28:31], a[60:63], v[112:127]
	v_cvt_pk_bf16_f32 v154, v132, v133
	v_cvt_pk_bf16_f32 v155, v134, v135
	s_waitcnt lgkmcnt(7)
	v_mfma_f32_32x32x16_bf16 v[112:127], v[32:35], a[80:83], v[112:127]
	buffer_store_dwordx4 v[152:155], v11, s[20:23], s56 offen offset:0 sc1
	s_waitcnt lgkmcnt(6)
	v_mfma_f32_32x32x16_bf16 v[112:127], v[36:39], a[100:103], v[112:127]
	v_cvt_pk_bf16_f32 v156, v136, v137
	v_cvt_pk_bf16_f32 v157, v138, v139
	s_waitcnt lgkmcnt(5)
	v_mfma_f32_32x32x16_bf16 v[112:127], v[40:43], a[120:123], v[112:127]
	buffer_store_dwordx4 v[152:155], v13, s[20:23], s55 offen offset:0
	s_waitcnt lgkmcnt(4)
	v_mfma_f32_32x32x16_bf16 v[112:127], v[44:47], a[140:143], v[112:127]
	v_cvt_pk_bf16_f32 v158, v140, v141
	v_cvt_pk_bf16_f32 v159, v142, v143
	s_waitcnt lgkmcnt(3)
	v_mfma_f32_32x32x16_bf16 v[112:127], v[48:51], a[160:163], v[112:127]
	s_waitcnt lgkmcnt(2)
	v_mfma_f32_32x32x16_bf16 v[112:127], v[52:55], a[180:183], v[112:127]
	buffer_store_dwordx4 v[156:159], v11, s[20:23], s56 offen offset:256 sc1
	s_waitcnt lgkmcnt(1)
	v_mfma_f32_32x32x16_bf16 v[112:127], v[56:59], a[200:203], v[112:127]
	s_waitcnt lgkmcnt(0)
	v_mfma_f32_32x32x16_bf16 v[112:127], v[60:63], a[220:223], v[112:127]
	buffer_store_dwordx4 v[156:159], v13, s[20:23], s55 offen offset:256
	s_add_u32 s55, s37, 0
	s_add_u32 s56, s52, 0
	v_mfma_f32_32x32x16_bf16 v[128:143], v[16:19], a[4:7], 0
	v_mfma_f32_32x32x16_bf16 v[128:143], v[20:23], a[24:27], v[128:143]
	v_mfma_f32_32x32x16_bf16 v[128:143], v[24:27], a[44:47], v[128:143]
	v_cvt_pk_bf16_f32 v144, v112, v113
	v_cvt_pk_bf16_f32 v145, v114, v115
	v_mfma_f32_32x32x16_bf16 v[128:143], v[28:31], a[64:67], v[128:143]
	v_cvt_pk_bf16_f32 v146, v116, v117
	v_cvt_pk_bf16_f32 v147, v118, v119
	v_mfma_f32_32x32x16_bf16 v[128:143], v[32:35], a[84:87], v[128:143]
	buffer_store_dwordx4 v[144:147], v11, s[20:23], s56 offen offset:0 sc1
	v_mfma_f32_32x32x16_bf16 v[128:143], v[36:39], a[104:107], v[128:143]
	v_cvt_pk_bf16_f32 v148, v120, v121
	v_cvt_pk_bf16_f32 v149, v122, v123
	v_mfma_f32_32x32x16_bf16 v[128:143], v[40:43], a[124:127], v[128:143]
	buffer_store_dwordx4 v[144:147], v13, s[20:23], s55 offen offset:0
	v_mfma_f32_32x32x16_bf16 v[128:143], v[44:47], a[144:147], v[128:143]
	v_cvt_pk_bf16_f32 v150, v124, v125
	v_cvt_pk_bf16_f32 v151, v126, v127
	v_mfma_f32_32x32x16_bf16 v[128:143], v[48:51], a[164:167], v[128:143]
	v_mfma_f32_32x32x16_bf16 v[128:143], v[52:55], a[184:187], v[128:143]
	buffer_store_dwordx4 v[148:151], v11, s[20:23], s56 offen offset:256 sc1
	v_mfma_f32_32x32x16_bf16 v[128:143], v[56:59], a[204:207], v[128:143]
	v_mfma_f32_32x32x16_bf16 v[128:143], v[60:63], a[224:227], v[128:143]
	buffer_store_dwordx4 v[148:151], v13, s[20:23], s55 offen offset:256
	s_add_u32 s55, s37, 163840
	s_add_u32 s56, s52, 131072
	v_mfma_f32_32x32x16_bf16 v[112:127], v[16:19], a[8:11], 0
	v_mfma_f32_32x32x16_bf16 v[112:127], v[20:23], a[28:31], v[112:127]
	s_add_u32 s79, s48, 2
	s_cmp_ge_u32 s51, s79
	s_cbranch_scc1 .Lt2_l21
	s_cmp_ge_u32 s73, s79
	s_cbranch_scc1 .Lt2_l22
	s_waitcnt vmcnt(8)
	v_readfirstlane_b32 s53, v8
	v_readlane_b32 s18, v8, 1
	s_add_u32 s18, s18, 15
	s_min_u32 s53, s53, s18
	s_max_u32 s73, s73, s53
	s_cmp_lt_u32 s73, s79
	s_cbranch_scc1 .Lt2_l21
.Lt2_l22:
	buffer_load_dwordx4 v[164:167], v176, s[20:23], s34 offen offset:0 sc1
	buffer_load_dwordx4 v[168:171], v177, s[20:23], s34 offen offset:0 sc1
	buffer_load_dwordx4 v[172:175], v178, s[20:23], s34 offen offset:0 sc1
	s_mov_b32 s51, s79
	s_mov_b32 s59, 1
	s_add_u32 s34, s34, s35
	s_xor_b32 s34, s34, s40
.Lt2_l21:
	v_mfma_f32_32x32x16_bf16 v[112:127], v[24:27], a[48:51], v[112:127]
	v_cvt_pk_bf16_f32 v152, v128, v129
	v_cvt_pk_bf16_f32 v153, v130, v131
	s_add_u32 s53, s48, 2
	s_cmp_ge_u32 s51, s53
	s_cselect_b64 exec, 0, 1
	v_mov_b32 v10, s53
	ds_write_b32 v181, v10 offset:0
	s_mov_b64 exec, -1
	v_mfma_f32_32x32x16_bf16 v[112:127], v[28:31], a[68:71], v[112:127]
	v_cvt_pk_bf16_f32 v154, v132, v133
	v_cvt_pk_bf16_f32 v155, v134, v135
	v_mfma_f32_32x32x16_bf16 v[112:127], v[32:35], a[88:91], v[112:127]
	buffer_store_dwordx4 v[152:155], v11, s[20:23], s56 offen offset:0 sc1
	v_mfma_f32_32x32x16_bf16 v[112:127], v[36:39], a[108:111], v[112:127]
	v_cvt_pk_bf16_f32 v156, v136, v137
	v_cvt_pk_bf16_f32 v157, v138, v139
	v_mfma_f32_32x32x16_bf16 v[112:127], v[40:43], a[128:131], v[112:127]
	buffer_store_dwordx4 v[152:155], v13, s[20:23], s55 offen offset:0
	v_mfma_f32_32x32x16_bf16 v[112:127], v[44:47], a[148:151], v[112:127]
	v_cvt_pk_bf16_f32 v158, v140, v141
	v_cvt_pk_bf16_f32 v159, v142, v143
	v_mfma_f32_32x32x16_bf16 v[112:127], v[48:51], a[168:171], v[112:127]
	v_mfma_f32_32x32x16_bf16 v[112:127], v[52:55], a[188:191], v[112:127]
	buffer_store_dwordx4 v[156:159], v11, s[20:23], s56 offen offset:256 sc1
	v_mfma_f32_32x32x16_bf16 v[112:127], v[56:59], a[208:211], v[112:127]
	s_add_u32 s53, s48, 3
	s_cmp_ge_u32 s73, s53
	s_cbranch_scc1 .Lt2_l23
	buffer_load_dword v8, v12, s[20:23], 0 offen sc1
.Lt2_l23:
	v_mfma_f32_32x32x16_bf16 v[112:127], v[60:63], a[228:231], v[112:127]
	buffer_store_dwordx4 v[156:159], v13, s[20:23], s55 offen offset:256
	s_add_u32 s55, s37, 327680
	s_add_u32 s56, s52, 262144
	v_mfma_f32_32x32x16_bf16 v[128:143], v[16:19], a[12:15], 0
	v_mfma_f32_32x32x16_bf16 v[128:143], v[20:23], a[32:35], v[128:143]
	v_mfma_f32_32x32x16_bf16 v[128:143], v[24:27], a[52:55], v[128:143]
	v_cvt_pk_bf16_f32 v144, v112, v113
	v_cvt_pk_bf16_f32 v145, v114, v115
	v_mfma_f32_32x32x16_bf16 v[128:143], v[28:31], a[72:75], v[128:143]
	v_cvt_pk_bf16_f32 v146, v116, v117
	v_cvt_pk_bf16_f32 v147, v118, v119
	v_mfma_f32_32x32x16_bf16 v[128:143], v[32:35], a[92:95], v[128:143]
	buffer_store_dwordx4 v[144:147], v11, s[20:23], s56 offen offset:0 sc1
	v_mfma_f32_32x32x16_bf16 v[128:143], v[36:39], a[112:115], v[128:143]
	v_cvt_pk_bf16_f32 v148, v120, v121
	v_cvt_pk_bf16_f32 v149, v122, v123
	v_mfma_f32_32x32x16_bf16 v[128:143], v[40:43], a[132:135], v[128:143]
	buffer_store_dwordx4 v[144:147], v13, s[20:23], s55 offen offset:0
	v_mfma_f32_32x32x16_bf16 v[128:143], v[44:47], a[152:155], v[128:143]
	v_cvt_pk_bf16_f32 v150, v124, v125
	v_cvt_pk_bf16_f32 v151, v126, v127
	v_mfma_f32_32x32x16_bf16 v[128:143], v[48:51], a[172:175], v[128:143]
	v_mfma_f32_32x32x16_bf16 v[128:143], v[52:55], a[192:195], v[128:143]
	buffer_store_dwordx4 v[148:151], v11, s[20:23], s56 offen offset:256 sc1
	v_mfma_f32_32x32x16_bf16 v[128:143], v[56:59], a[212:215], v[128:143]
	v_mfma_f32_32x32x16_bf16 v[128:143], v[60:63], a[232:235], v[128:143]
	buffer_store_dwordx4 v[148:151], v13, s[20:23], s55 offen offset:256
	s_add_u32 s55, s37, 491520
	s_add_u32 s56, s52, 393216
	s_waitcnt vmcnt(10)
	ds_write_b128 v179, v[164:167] offset:12288
	v_mfma_f32_32x32x16_bf16 v[112:127], v[16:19], a[16:19], 0
	s_waitcnt vmcnt(9)
	ds_write_b128 v179, v[168:171] offset:13312
	v_mfma_f32_32x32x16_bf16 v[112:127], v[20:23], a[36:39], v[112:127]
	s_waitcnt vmcnt(8)
	ds_write_b128 v179, v[172:175] offset:14336
	v_mfma_f32_32x32x16_bf16 v[112:127], v[24:27], a[56:59], v[112:127]
	v_cvt_pk_bf16_f32 v152, v128, v129
	v_cvt_pk_bf16_f32 v153, v130, v131
	s_waitcnt lgkmcnt(0)
	s_barrier
	v_mov_b32 v9, s48
	s_mov_b32 s62, s48
	s_mov_b64 exec, s[60:61]
	buffer_store_dword v9, off, s[20:23], s36 sc1
	s_mov_b64 exec, -1
	ds_read_b32 v10, v181 offset:0
	v_mfma_f32_32x32x16_bf16 v[112:127], v[28:31], a[76:79], v[112:127]
	v_cvt_pk_bf16_f32 v154, v132, v133
	v_cvt_pk_bf16_f32 v155, v134, v135
	ds_read_b128 v[64:67], v180 offset:12288
	ds_read_b128 v[68:71], v180 offset:13312
	v_mfma_f32_32x32x16_bf16 v[112:127], v[32:35], a[96:99], v[112:127]
	buffer_store_dwordx4 v[152:155], v11, s[20:23], s56 offen offset:0 sc1
	ds_read_b128 v[72:75], v180 offset:14336
	v_mfma_f32_32x32x16_bf16 v[112:127], v[36:39], a[116:119], v[112:127]
	v_cvt_pk_bf16_f32 v156, v136, v137
	v_cvt_pk_bf16_f32 v157, v138, v139
	ds_read_b128 v[76:79], v180 offset:15360
	v_mfma_f32_32x32x16_bf16 v[112:127], v[40:43], a[136:139], v[112:127]
	buffer_store_dwordx4 v[152:155], v13, s[20:23], s55 offen offset:0
	ds_read_b128 v[80:83], v180 offset:16384
	ds_read_b128 v[84:87], v180 offset:17408
	v_mfma_f32_32x32x16_bf16 v[112:127], v[44:47], a[156:159], v[112:127]
	v_cvt_pk_bf16_f32 v158, v140, v141
	v_cvt_pk_bf16_f32 v159, v142, v143
	ds_read_b128 v[88:91], v180 offset:18432
	v_mfma_f32_32x32x16_bf16 v[112:127], v[48:51], a[176:179], v[112:127]
	ds_read_b128 v[92:95], v180 offset:19456
	v_mfma_f32_32x32x16_bf16 v[112:127], v[52:55], a[196:199], v[112:127]
	buffer_store_dwordx4 v[156:159], v11, s[20:23], s56 offen offset:256 sc1
	ds_read_b128 v[96:99], v180 offset:20480
	ds_read_b128 v[100:103], v180 offset:21504
	v_mfma_f32_32x32x16_bf16 v[112:127], v[56:59], a[216:219], v[112:127]
	ds_read_b128 v[104:107], v180 offset:22528
	v_mfma_f32_32x32x16_bf16 v[112:127], v[60:63], a[236:239], v[112:127]
	buffer_store_dwordx4 v[156:159], v13, s[20:23], s55 offen offset:256
	s_add_u32 s55, s37, 655360
	s_add_u32 s56, s52, 524288
	ds_read_b128 v[108:111], v180 offset:23552
	s_waitcnt lgkmcnt(12)
	v_readfirstlane_b32 s53, v10
	s_add_u32 s37, s37, 4194304
	s_add_u32 s48, s48, 1
	s_cmp_le_u32 s53, s48
	s_cbranch_scc1 .Lt2_l17
	s_add_u32 s79, s48, 1
	s_cmp_ge_u32 s51, s79
	s_cbranch_scc1 .Lt2_l24
	s_cmp_ge_u32 s73, s79
	s_cbranch_scc1 .Lt2_l25
	s_waitcnt vmcnt(9)
	v_readfirstlane_b32 s53, v8
	v_readlane_b32 s18, v8, 1
	s_add_u32 s18, s18, 15
	s_min_u32 s53, s53, s18
	s_max_u32 s73, s73, s53
	s_cmp_lt_u32 s73, s79
	s_cbranch_scc1 .Lt2_l24
.Lt2_l25:
	buffer_load_dwordx4 v[164:167], v176, s[20:23], s34 offen offset:0 sc1
	buffer_load_dwordx4 v[168:171], v177, s[20:23], s34 offen offset:0 sc1
	buffer_load_dwordx4 v[172:175], v178, s[20:23], s34 offen offset:0 sc1
	s_mov_b32 s51, s79
	s_mov_b32 s59, 1
	s_add_u32 s34, s34, s35
	s_xor_b32 s34, s34, s40
.Lt2_l24:
	v_cvt_pk_bf16_f32 v144, v112, v113
	v_cvt_pk_bf16_f32 v145, v114, v115
	v_cvt_pk_bf16_f32 v146, v116, v117
	v_cvt_pk_bf16_f32 v147, v118, v119
	buffer_store_dwordx4 v[144:147], v11, s[20:23], s56 offen offset:0 sc1
	buffer_store_dwordx4 v[144:147], v13, s[20:23], s55 offen offset:0
	v_cvt_pk_bf16_f32 v148, v120, v121
	v_cvt_pk_bf16_f32 v149, v122, v123
	v_cvt_pk_bf16_f32 v150, v124, v125
	v_cvt_pk_bf16_f32 v151, v126, v127
	buffer_store_dwordx4 v[148:151], v11, s[20:23], s56 offen offset:256 sc1
	buffer_store_dwordx4 v[148:151], v13, s[20:23], s55 offen offset:256
	buffer_load_dword v8, v12, s[20:23], 0 offen sc1
	s_waitcnt vmcnt(0)
	s_waitcnt lgkmcnt(0)
	s_barrier
	v_mov_b32 v9, s48
	s_mov_b32 s62, s48
	s_mov_b64 exec, s[60:61]
	buffer_store_dword v9, off, s[20:23], s36 sc1
	s_mov_b64 exec, -1
	v_readfirstlane_b32 s53, v8
	v_readlane_b32 s18, v8, 1
	s_add_u32 s18, s18, 15
	s_min_u32 s53, s53, s18
	s_max_u32 s73, s73, s53
	s_add_u32 s78, s48, 1
.Lt2_f26:
	s_cmp_ge_u32 s73, s78
	s_cbranch_scc1 .Lt2_f27
	s_cmp_eq_u32 s69, 0
	s_cbranch_scc1 .Lt2_f27
	s_sub_u32 s69, s69, 1
	buffer_load_dword v9, v12, s[20:23], 0 offen sc1
	s_waitcnt vmcnt(0)
	v_readfirstlane_b32 s73, v9
	s_sleep 2
	v_readlane_b32 s18, v9, 1
	s_add_u32 s18, s18, 15
	s_min_u32 s73, s73, s18
	s_branch .Lt2_f26
.Lt2_f27:
	s_max_u32 s73, s73, s78
	s_add_u32 s79, s48, 1
	s_cmp_ge_u32 s51, s79
	s_cbranch_scc1 .Lt2_l28
	s_cmp_ge_u32 s73, s79
	s_cbranch_scc1 .Lt2_l29
	v_readfirstlane_b32 s53, v8
	v_readlane_b32 s18, v8, 1
	s_add_u32 s18, s18, 15
	s_min_u32 s53, s53, s18
	s_max_u32 s73, s73, s53
	s_cmp_lt_u32 s73, s79
	s_cbranch_scc1 .Lt2_l28
.Lt2_l29:
	buffer_load_dwordx4 v[164:167], v176, s[20:23], s34 offen offset:0 sc1
	buffer_load_dwordx4 v[168:171], v177, s[20:23], s34 offen offset:0 sc1
	buffer_load_dwordx4 v[172:175], v178, s[20:23], s34 offen offset:0 sc1
	s_mov_b32 s51, s79
	s_mov_b32 s59, 1
	s_add_u32 s34, s34, s35
	s_xor_b32 s34, s34, s40
.Lt2_l28:
	s_cmp_gt_u32 s51, s78
	s_cbranch_scc1 .Lt2_l30
	s_waitcnt vmcnt(2)
	ds_write_b128 v179, v[164:167] offset:12288
	s_waitcnt vmcnt(1)
	ds_write_b128 v179, v[168:171] offset:13312
	s_waitcnt vmcnt(0)
	ds_write_b128 v179, v[172:175] offset:14336
.Lt2_l30:
	s_waitcnt lgkmcnt(0)
	s_barrier
	ds_read_b128 v[64:67], v180 offset:12288
	ds_read_b128 v[68:71], v180 offset:13312
	ds_read_b128 v[72:75], v180 offset:14336
	ds_read_b128 v[76:79], v180 offset:15360
	ds_read_b128 v[80:83], v180 offset:16384
	ds_read_b128 v[84:87], v180 offset:17408
	ds_read_b128 v[88:91], v180 offset:18432
	ds_read_b128 v[92:95], v180 offset:19456
	ds_read_b128 v[96:99], v180 offset:20480
	ds_read_b128 v[100:103], v180 offset:21504
	ds_read_b128 v[104:107], v180 offset:22528
	ds_read_b128 v[108:111], v180 offset:23552
.Lt2_l17:
	s_waitcnt lgkmcnt(11)
	v_mfma_f32_32x32x16_bf16 v[128:143], v[64:67], a[0:3], 0
	s_add_u32 s79, s48, 2
	s_cmp_ge_u32 s51, s79
	s_cbranch_scc1 .Lt2_l32
	s_cmp_ge_u32 s73, s79
	s_cbranch_scc1 .Lt2_l33
	s_waitcnt vmcnt(9)
	v_readfirstlane_b32 s53, v8
	v_readlane_b32 s18, v8, 1
	s_add_u32 s18, s18, 15
	s_min_u32 s53, s53, s18
	s_max_u32 s73, s73, s53
	s_cmp_lt_u32 s73, s79
	s_cbranch_scc1 .Lt2_l32
.Lt2_l33:
	buffer_load_dwordx4 v[164:167], v176, s[20:23], s34 offen offset:0 sc1
	buffer_load_dwordx4 v[168:171], v177, s[20:23], s34 offen offset:0 sc1
	buffer_load_dwordx4 v[172:175], v178, s[20:23], s34 offen offset:0 sc1
	s_mov_b32 s51, s79
	s_mov_b32 s59, 1
	s_add_u32 s34, s34, s35
	s_xor_b32 s34, s34, s40
.Lt2_l32:
	s_waitcnt lgkmcnt(10)
	v_mfma_f32_32x32x16_bf16 v[128:143], v[68:71], a[20:23], v[128:143]
	s_and_b32 s52, s37, 67108863
	s_cmp_lt_u32 s48, 8
	s_cselect_b32 s53, 67108864, 0
	s_add_u32 s52, s52, s53
	s_add_u32 s53, s48, 2
	s_cmp_ge_u32 s73, s53
	s_cbranch_scc1 .Lt2_l34
	buffer_load_dword v8, v12, s[20:23], 0 offen sc1
.Lt2_l34:
	s_waitcnt lgkmcnt(9)
	v_mfma_f32_32x32x16_bf16 v[128:143], v[72:75], a[40:43], v[128:143]
	v_cvt_pk_bf16_f32 v144, v112, v113
	v_cvt_pk_bf16_f32 v145, v114, v115
	s_waitcnt lgkmcnt(8)
	v_mfma_f32_32x32x16_bf16 v[128:143], v[76:79], a[60:63], v[128:143]
	v_cvt_pk_bf16_f32 v146, v116, v117
	v_cvt_pk_bf16_f32 v147, v118, v119
	s_waitcnt lgkmcnt(7)
	v_mfma_f32_32x32x16_bf16 v[128:143], v[80:83], a[80:83], v[128:143]
	buffer_store_dwordx4 v[144:147], v11, s[20:23], s56 offen offset:0 sc1
	s_waitcnt lgkmcnt(6)
	v_mfma_f32_32x32x16_bf16 v[128:143], v[84:87], a[100:103], v[128:143]
	v_cvt_pk_bf16_f32 v148, v120, v121
	v_cvt_pk_bf16_f32 v149, v122, v123
	s_waitcnt lgkmcnt(5)
	v_mfma_f32_32x32x16_bf16 v[128:143], v[88:91], a[120:123], v[128:143]
	buffer_store_dwordx4 v[144:147], v13, s[20:23], s55 offen offset:0
	s_waitcnt lgkmcnt(4)
	v_mfma_f32_32x32x16_bf16 v[128:143], v[92:95], a[140:143], v[128:143]
	v_cvt_pk_bf16_f32 v150, v124, v125
	v_cvt_pk_bf16_f32 v151, v126, v127
	s_waitcnt lgkmcnt(3)
	v_mfma_f32_32x32x16_bf16 v[128:143], v[96:99], a[160:163], v[128:143]
	s_waitcnt lgkmcnt(2)
	v_mfma_f32_32x32x16_bf16 v[128:143], v[100:103], a[180:183], v[128:143]
	buffer_store_dwordx4 v[148:151], v11, s[20:23], s56 offen offset:256 sc1
	s_waitcnt lgkmcnt(1)
	v_mfma_f32_32x32x16_bf16 v[128:143], v[104:107], a[200:203], v[128:143]
	s_waitcnt lgkmcnt(0)
	v_mfma_f32_32x32x16_bf16 v[128:143], v[108:111], a[220:223], v[128:143]
	buffer_store_dwordx4 v[148:151], v13, s[20:23], s55 offen offset:256
	s_add_u32 s55, s37, 0
	s_add_u32 s56, s52, 0
	v_mfma_f32_32x32x16_bf16 v[112:127], v[64:67], a[4:7], 0
	v_mfma_f32_32x32x16_bf16 v[112:127], v[68:71], a[24:27], v[112:127]
	v_mfma_f32_32x32x16_bf16 v[112:127], v[72:75], a[44:47], v[112:127]
	v_cvt_pk_bf16_f32 v152, v128, v129
	v_cvt_pk_bf16_f32 v153, v130, v131
	v_mfma_f32_32x32x16_bf16 v[112:127], v[76:79], a[64:67], v[112:127]
	v_cvt_pk_bf16_f32 v154, v132, v133
	v_cvt_pk_bf16_f32 v155, v134, v135
	v_mfma_f32_32x32x16_bf16 v[112:127], v[80:83], a[84:87], v[112:127]
	buffer_store_dwordx4 v[152:155], v11, s[20:23], s56 offen offset:0 sc1
	v_mfma_f32_32x32x16_bf16 v[112:127], v[84:87], a[104:107], v[112:127]
	v_cvt_pk_bf16_f32 v156, v136, v137
	v_cvt_pk_bf16_f32 v157, v138, v139
	v_mfma_f32_32x32x16_bf16 v[112:127], v[88:91], a[124:127], v[112:127]
	buffer_store_dwordx4 v[152:155], v13, s[20:23], s55 offen offset:0
	v_mfma_f32_32x32x16_bf16 v[112:127], v[92:95], a[144:147], v[112:127]
	v_cvt_pk_bf16_f32 v158, v140, v141
	v_cvt_pk_bf16_f32 v159, v142, v143
	v_mfma_f32_32x32x16_bf16 v[112:127], v[96:99], a[164:167], v[112:127]
	v_mfma_f32_32x32x16_bf16 v[112:127], v[100:103], a[184:187], v[112:127]
	buffer_store_dwordx4 v[156:159], v11, s[20:23], s56 offen offset:256 sc1
	v_mfma_f32_32x32x16_bf16 v[112:127], v[104:107], a[204:207], v[112:127]
	v_mfma_f32_32x32x16_bf16 v[112:127], v[108:111], a[224:227], v[112:127]
	buffer_store_dwordx4 v[156:159], v13, s[20:23], s55 offen offset:256
	s_add_u32 s55, s37, 163840
	s_add_u32 s56, s52, 131072
	v_mfma_f32_32x32x16_bf16 v[128:143], v[64:67], a[8:11], 0
	v_mfma_f32_32x32x16_bf16 v[128:143], v[68:71], a[28:31], v[128:143]
	s_add_u32 s79, s48, 2
	s_cmp_ge_u32 s51, s79
	s_cbranch_scc1 .Lt2_l35
	s_cmp_ge_u32 s73, s79
	s_cbranch_scc1 .Lt2_l36
	s_waitcnt vmcnt(8)
	v_readfirstlane_b32 s53, v8
	v_readlane_b32 s18, v8, 1
	s_add_u32 s18, s18, 15
	s_min_u32 s53, s53, s18
	s_max_u32 s73, s73, s53
	s_cmp_lt_u32 s73, s79
	s_cbranch_scc1 .Lt2_l35
.Lt2_l36:
	buffer_load_dwordx4 v[164:167], v176, s[20:23], s34 offen offset:0 sc1
	buffer_load_dwordx4 v[168:171], v177, s[20:23], s34 offen offset:0 sc1
	buffer_load_dwordx4 v[172:175], v178, s[20:23], s34 offen offset:0 sc1
	s_mov_b32 s51, s79
	s_mov_b32 s59, 1
	s_add_u32 s34, s34, s35
	s_xor_b32 s34, s34, s40
.Lt2_l35:
	v_mfma_f32_32x32x16_bf16 v[128:143], v[72:75], a[48:51], v[128:143]
	v_cvt_pk_bf16_f32 v144, v112, v113
	v_cvt_pk_bf16_f32 v145, v114, v115
	s_add_u32 s53, s48, 2
	s_cmp_ge_u32 s51, s53
	s_cselect_b64 exec, 0, 1
	v_mov_b32 v10, s53
	ds_write_b32 v181, v10 offset:4
	s_mov_b64 exec, -1
	v_mfma_f32_32x32x16_bf16 v[128:143], v[76:79], a[68:71], v[128:143]
	v_cvt_pk_bf16_f32 v146, v116, v117
	v_cvt_pk_bf16_f32 v147, v118, v119
	v_mfma_f32_32x32x16_bf16 v[128:143], v[80:83], a[88:91], v[128:143]
	buffer_store_dwordx4 v[144:147], v11, s[20:23], s56 offen offset:0 sc1
	v_mfma_f32_32x32x16_bf16 v[128:143], v[84:87], a[108:111], v[128:143]
	v_cvt_pk_bf16_f32 v148, v120, v121
	v_cvt_pk_bf16_f32 v149, v122, v123
	v_mfma_f32_32x32x16_bf16 v[128:143], v[88:91], a[128:131], v[128:143]
	buffer_store_dwordx4 v[144:147], v13, s[20:23], s55 offen offset:0
	v_mfma_f32_32x32x16_bf16 v[128:143], v[92:95], a[148:151], v[128:143]
	v_cvt_pk_bf16_f32 v150, v124, v125
	v_cvt_pk_bf16_f32 v151, v126, v127
	v_mfma_f32_32x32x16_bf16 v[128:143], v[96:99], a[168:171], v[128:143]
	v_mfma_f32_32x32x16_bf16 v[128:143], v[100:103], a[188:191], v[128:143]
	buffer_store_dwordx4 v[148:151], v11, s[20:23], s56 offen offset:256 sc1
	v_mfma_f32_32x32x16_bf16 v[128:143], v[104:107], a[208:211], v[128:143]
	s_add_u32 s53, s48, 3
	s_cmp_ge_u32 s73, s53
	s_cbranch_scc1 .Lt2_l37
	buffer_load_dword v8, v12, s[20:23], 0 offen sc1
.Lt2_l37:
	v_mfma_f32_32x32x16_bf16 v[128:143], v[108:111], a[228:231], v[128:143]
	buffer_store_dwordx4 v[148:151], v13, s[20:23], s55 offen offset:256
	s_add_u32 s55, s37, 327680
	s_add_u32 s56, s52, 262144
	v_mfma_f32_32x32x16_bf16 v[112:127], v[64:67], a[12:15], 0
	v_mfma_f32_32x32x16_bf16 v[112:127], v[68:71], a[32:35], v[112:127]
	v_mfma_f32_32x32x16_bf16 v[112:127], v[72:75], a[52:55], v[112:127]
	v_cvt_pk_bf16_f32 v152, v128, v129
	v_cvt_pk_bf16_f32 v153, v130, v131
	v_mfma_f32_32x32x16_bf16 v[112:127], v[76:79], a[72:75], v[112:127]
	v_cvt_pk_bf16_f32 v154, v132, v133
	v_cvt_pk_bf16_f32 v155, v134, v135
	v_mfma_f32_32x32x16_bf16 v[112:127], v[80:83], a[92:95], v[112:127]
	buffer_store_dwordx4 v[152:155], v11, s[20:23], s56 offen offset:0 sc1
	v_mfma_f32_32x32x16_bf16 v[112:127], v[84:87], a[112:115], v[112:127]
	v_cvt_pk_bf16_f32 v156, v136, v137
	v_cvt_pk_bf16_f32 v157, v138, v139
	v_mfma_f32_32x32x16_bf16 v[112:127], v[88:91], a[132:135], v[112:127]
	buffer_store_dwordx4 v[152:155], v13, s[20:23], s55 offen offset:0
	v_mfma_f32_32x32x16_bf16 v[112:127], v[92:95], a[152:155], v[112:127]
	v_cvt_pk_bf16_f32 v158, v140, v141
	v_cvt_pk_bf16_f32 v159, v142, v143
	v_mfma_f32_32x32x16_bf16 v[112:127], v[96:99], a[172:175], v[112:127]
	v_mfma_f32_32x32x16_bf16 v[112:127], v[100:103], a[192:195], v[112:127]
	buffer_store_dwordx4 v[156:159], v11, s[20:23], s56 offen offset:256 sc1
	v_mfma_f32_32x32x16_bf16 v[112:127], v[104:107], a[212:215], v[112:127]
	v_mfma_f32_32x32x16_bf16 v[112:127], v[108:111], a[232:235], v[112:127]
	buffer_store_dwordx4 v[156:159], v13, s[20:23], s55 offen offset:256
	s_add_u32 s55, s37, 491520
	s_add_u32 s56, s52, 393216
	s_waitcnt vmcnt(10)
	ds_write_b128 v179, v[164:167] offset:0
	v_mfma_f32_32x32x16_bf16 v[128:143], v[64:67], a[16:19], 0
	s_waitcnt vmcnt(9)
	ds_write_b128 v179, v[168:171] offset:1024
	v_mfma_f32_32x32x16_bf16 v[128:143], v[68:71], a[36:39], v[128:143]
	s_waitcnt vmcnt(8)
	ds_write_b128 v179, v[172:175] offset:2048
	v_mfma_f32_32x32x16_bf16 v[128:143], v[72:75], a[56:59], v[128:143]
	v_cvt_pk_bf16_f32 v144, v112, v113
	v_cvt_pk_bf16_f32 v145, v114, v115
	s_waitcnt lgkmcnt(0)
	s_barrier
	v_mov_b32 v9, s48
	s_mov_b32 s62, s48
	s_mov_b64 exec, s[60:61]
	buffer_store_dword v9, off, s[20:23], s36 sc1
	s_mov_b64 exec, -1
	ds_read_b32 v10, v181 offset:4
	v_mfma_f32_32x32x16_bf16 v[128:143], v[76:79], a[76:79], v[128:143]
	v_cvt_pk_bf16_f32 v146, v116, v117
	v_cvt_pk_bf16_f32 v147, v118, v119
	ds_read_b128 v[16:19], v180 offset:0
	ds_read_b128 v[20:23], v180 offset:1024
	v_mfma_f32_32x32x16_bf16 v[128:143], v[80:83], a[96:99], v[128:143]
	buffer_store_dwordx4 v[144:147], v11, s[20:23], s56 offen offset:0 sc1
	ds_read_b128 v[24:27], v180 offset:2048
	v_mfma_f32_32x32x16_bf16 v[128:143], v[84:87], a[116:119], v[128:143]
	v_cvt_pk_bf16_f32 v148, v120, v121
	v_cvt_pk_bf16_f32 v149, v122, v123
	ds_read_b128 v[28:31], v180 offset:3072
	v_mfma_f32_32x32x16_bf16 v[128:143], v[88:91], a[136:139], v[128:143]
	buffer_store_dwordx4 v[144:147], v13, s[20:23], s55 offen offset:0
	ds_read_b128 v[32:35], v180 offset:4096
	ds_read_b128 v[36:39], v180 offset:5120
	v_mfma_f32_32x32x16_bf16 v[128:143], v[92:95], a[156:159], v[128:143]
	v_cvt_pk_bf16_f32 v150, v124, v125
	v_cvt_pk_bf16_f32 v151, v126, v127
	ds_read_b128 v[40:43], v180 offset:6144
	v_mfma_f32_32x32x16_bf16 v[128:143], v[96:99], a[176:179], v[128:143]
	ds_read_b128 v[44:47], v180 offset:7168
	v_mfma_f32_32x32x16_bf16 v[128:143], v[100:103], a[196:199], v[128:143]
	buffer_store_dwordx4 v[148:151], v11, s[20:23], s56 offen offset:256 sc1
	ds_read_b128 v[48:51], v180 offset:8192
	ds_read_b128 v[52:55], v180 offset:9216
	v_mfma_f32_32x32x16_bf16 v[128:143], v[104:107], a[216:219], v[128:143]
	ds_read_b128 v[56:59], v180 offset:10240
	v_mfma_f32_32x32x16_bf16 v[128:143], v[108:111], a[236:239], v[128:143]
	buffer_store_dwordx4 v[148:151], v13, s[20:23], s55 offen offset:256
	s_add_u32 s55, s37, 655360
	s_add_u32 s56, s52, 524288
	ds_read_b128 v[60:63], v180 offset:11264
	s_cmp_ge_u32 s48, 7
	s_cselect_b64 exec, -1, 0
	v_mov_b32 v13, v7
	s_mov_b64 exec, -1
	s_waitcnt lgkmcnt(12)
	v_readfirstlane_b32 s53, v10
	s_add_u32 s37, s37, 4194304
	s_add_u32 s48, s48, 1
	s_cmp_le_u32 s53, s48
	s_cbranch_scc1 .Lt2_l31
	s_add_u32 s79, s48, 1
	s_cmp_ge_u32 s51, s79
	s_cbranch_scc1 .Lt2_l38
	s_cmp_ge_u32 s73, s79
	s_cbranch_scc1 .Lt2_l39
	s_waitcnt vmcnt(9)
	v_readfirstlane_b32 s53, v8
	v_readlane_b32 s18, v8, 1
	s_add_u32 s18, s18, 15
	s_min_u32 s53, s53, s18
	s_max_u32 s73, s73, s53
	s_cmp_lt_u32 s73, s79
	s_cbranch_scc1 .Lt2_l38
.Lt2_l39:
	buffer_load_dwordx4 v[164:167], v176, s[20:23], s34 offen offset:0 sc1
	buffer_load_dwordx4 v[168:171], v177, s[20:23], s34 offen offset:0 sc1
	buffer_load_dwordx4 v[172:175], v178, s[20:23], s34 offen offset:0 sc1
	s_mov_b32 s51, s79
	s_mov_b32 s59, 1
	s_add_u32 s34, s34, s35
	s_xor_b32 s34, s34, s40
.Lt2_l38:
	v_cvt_pk_bf16_f32 v152, v128, v129
	v_cvt_pk_bf16_f32 v153, v130, v131
	v_cvt_pk_bf16_f32 v154, v132, v133
	v_cvt_pk_bf16_f32 v155, v134, v135
	buffer_store_dwordx4 v[152:155], v11, s[20:23], s56 offen offset:0 sc1
	buffer_store_dwordx4 v[152:155], v13, s[20:23], s55 offen offset:0
	v_cvt_pk_bf16_f32 v156, v136, v137
	v_cvt_pk_bf16_f32 v157, v138, v139
	v_cvt_pk_bf16_f32 v158, v140, v141
	v_cvt_pk_bf16_f32 v159, v142, v143
	buffer_store_dwordx4 v[156:159], v11, s[20:23], s56 offen offset:256 sc1
	buffer_store_dwordx4 v[156:159], v13, s[20:23], s55 offen offset:256
	buffer_load_dword v8, v12, s[20:23], 0 offen sc1
	s_waitcnt vmcnt(0)
	s_waitcnt lgkmcnt(0)
	s_barrier
	v_mov_b32 v9, s48
	s_mov_b32 s62, s48
	s_mov_b64 exec, s[60:61]
	buffer_store_dword v9, off, s[20:23], s36 sc1
	s_mov_b64 exec, -1
	v_readfirstlane_b32 s53, v8
	v_readlane_b32 s18, v8, 1
	s_add_u32 s18, s18, 15
	s_min_u32 s53, s53, s18
	s_max_u32 s73, s73, s53
	s_add_u32 s78, s48, 1
.Lt2_f40:
	s_cmp_ge_u32 s73, s78
	s_cbranch_scc1 .Lt2_f41
	s_cmp_eq_u32 s69, 0
	s_cbranch_scc1 .Lt2_f41
	s_sub_u32 s69, s69, 1
	buffer_load_dword v9, v12, s[20:23], 0 offen sc1
	s_waitcnt vmcnt(0)
	v_readfirstlane_b32 s73, v9
	s_sleep 2
	v_readlane_b32 s18, v9, 1
	s_add_u32 s18, s18, 15
	s_min_u32 s73, s73, s18
	s_branch .Lt2_f40
.Lt2_f41:
	s_max_u32 s73, s73, s78
	s_add_u32 s79, s48, 1
	s_cmp_ge_u32 s51, s79
	s_cbranch_scc1 .Lt2_l42
	s_cmp_ge_u32 s73, s79
	s_cbranch_scc1 .Lt2_l43
	v_readfirstlane_b32 s53, v8
	v_readlane_b32 s18, v8, 1
	s_add_u32 s18, s18, 15
	s_min_u32 s53, s53, s18
	s_max_u32 s73, s73, s53
	s_cmp_lt_u32 s73, s79
	s_cbranch_scc1 .Lt2_l42
.Lt2_l43:
	buffer_load_dwordx4 v[164:167], v176, s[20:23], s34 offen offset:0 sc1
	buffer_load_dwordx4 v[168:171], v177, s[20:23], s34 offen offset:0 sc1
	buffer_load_dwordx4 v[172:175], v178, s[20:23], s34 offen offset:0 sc1
	s_mov_b32 s51, s79
	s_mov_b32 s59, 1
	s_add_u32 s34, s34, s35
	s_xor_b32 s34, s34, s40
.Lt2_l42:
	s_cmp_gt_u32 s51, s78
	s_cbranch_scc1 .Lt2_l44
	s_waitcnt vmcnt(2)
	ds_write_b128 v179, v[164:167] offset:0
	s_waitcnt vmcnt(1)
	ds_write_b128 v179, v[168:171] offset:1024
	s_waitcnt vmcnt(0)
	ds_write_b128 v179, v[172:175] offset:2048
.Lt2_l44:
	s_waitcnt lgkmcnt(0)
	s_barrier
	ds_read_b128 v[16:19], v180 offset:0
	ds_read_b128 v[20:23], v180 offset:1024
	ds_read_b128 v[24:27], v180 offset:2048
	ds_read_b128 v[28:31], v180 offset:3072
	ds_read_b128 v[32:35], v180 offset:4096
	ds_read_b128 v[36:39], v180 offset:5120
	ds_read_b128 v[40:43], v180 offset:6144
	ds_read_b128 v[44:47], v180 offset:7168
	ds_read_b128 v[48:51], v180 offset:8192
	ds_read_b128 v[52:55], v180 offset:9216
	ds_read_b128 v[56:59], v180 offset:10240
	ds_read_b128 v[60:63], v180 offset:11264
.Lt2_l31:
	s_cmp_lt_u32 s48, 44
	s_cbranch_scc1 .Lt2_seg16
.Lt2_l15:
.Lt2_seg45:
	s_waitcnt lgkmcnt(11)
	v_mfma_f32_32x32x16_bf16 v[112:127], v[16:19], a[0:3], 0
	s_add_u32 s79, s48, 2
	s_cmp_ge_u32 s51, s79
	s_cbranch_scc1 .Lt2_l47
	s_cmp_ge_u32 s73, s79
	s_cbranch_scc1 .Lt2_l48
	s_waitcnt vmcnt(9)
	v_readfirstlane_b32 s53, v8
	v_readlane_b32 s18, v8, 1
	s_add_u32 s18, s18, 15
	s_min_u32 s53, s53, s18
	s_max_u32 s73, s73, s53
	s_cmp_lt_u32 s73, s79
	s_cbranch_scc1 .Lt2_l47
.Lt2_l48:
	buffer_load_dwordx4 v[164:167], v176, s[20:23], s34 offen offset:0 sc1
	buffer_load_dwordx4 v[168:171], v177, s[20:23], s34 offen offset:0 sc1
	buffer_load_dwordx4 v[172:175], v178, s[20:23], s34 offen offset:0 sc1
	s_mov_b32 s51, s79
	s_mov_b32 s59, 1
	s_add_u32 s34, s34, s35
	s_xor_b32 s34, s34, s40
.Lt2_l47:
	s_waitcnt lgkmcnt(10)
	v_mfma_f32_32x32x16_bf16 v[112:127], v[20:23], a[20:23], v[112:127]
	s_and_b32 s52, s37, 67108863
	s_cmp_lt_u32 s48, 8
	s_cselect_b32 s53, 67108864, 0
	s_add_u32 s52, s52, s53
	s_add_u32 s53, s48, 2
	s_cmp_ge_u32 s73, s53
	s_cbranch_scc1 .Lt2_l49
	buffer_load_dword v8, v12, s[20:23], 0 offen sc1
.Lt2_l49:
	s_waitcnt lgkmcnt(9)
	v_mfma_f32_32x32x16_bf16 v[112:127], v[24:27], a[40:43], v[112:127]
	v_cvt_pk_bf16_f32 v152, v128, v129
	v_cvt_pk_bf16_f32 v153, v130, v131
	s_waitcnt lgkmcnt(8)
	v_mfma_f32_32x32x16_bf16 v[112:127], v[28:31], a[60:63], v[112:127]
	v_cvt_pk_bf16_f32 v154, v132, v133
	v_cvt_pk_bf16_f32 v155, v134, v135
	s_waitcnt lgkmcnt(7)
	v_mfma_f32_32x32x16_bf16 v[112:127], v[32:35], a[80:83], v[112:127]
	buffer_store_dwordx4 v[152:155], v11, s[20:23], s56 offen offset:0 sc1
	s_waitcnt lgkmcnt(6)
	v_mfma_f32_32x32x16_bf16 v[112:127], v[36:39], a[100:103], v[112:127]
	v_cvt_pk_bf16_f32 v156, v136, v137
	v_cvt_pk_bf16_f32 v157, v138, v139
	s_waitcnt lgkmcnt(5)
	v_mfma_f32_32x32x16_bf16 v[112:127], v[40:43], a[120:123], v[112:127]
	buffer_store_dwordx4 v[152:155], v13, s[20:23], s55 offen offset:0 sc1
	s_waitcnt lgkmcnt(4)
	v_mfma_f32_32x32x16_bf16 v[112:127], v[44:47], a[140:143], v[112:127]
	v_cvt_pk_bf16_f32 v158, v140, v141
	v_cvt_pk_bf16_f32 v159, v142, v143
	s_waitcnt lgkmcnt(3)
	v_mfma_f32_32x32x16_bf16 v[112:127], v[48:51], a[160:163], v[112:127]
	s_waitcnt lgkmcnt(2)
	v_mfma_f32_32x32x16_bf16 v[112:127], v[52:55], a[180:183], v[112:127]
	buffer_store_dwordx4 v[156:159], v11, s[20:23], s56 offen offset:256 sc1
	s_waitcnt lgkmcnt(1)
	v_mfma_f32_32x32x16_bf16 v[112:127], v[56:59], a[200:203], v[112:127]
	s_waitcnt lgkmcnt(0)
	v_mfma_f32_32x32x16_bf16 v[112:127], v[60:63], a[220:223], v[112:127]
	buffer_store_dwordx4 v[156:159], v13, s[20:23], s55 offen offset:256 sc1
	s_add_u32 s55, s37, 0
	s_add_u32 s56, s52, 0
	v_mfma_f32_32x32x16_bf16 v[128:143], v[16:19], a[4:7], 0
	v_mfma_f32_32x32x16_bf16 v[128:143], v[20:23], a[24:27], v[128:143]
	v_mfma_f32_32x32x16_bf16 v[128:143], v[24:27], a[44:47], v[128:143]
	v_cvt_pk_bf16_f32 v144, v112, v113
	v_cvt_pk_bf16_f32 v145, v114, v115
	v_mfma_f32_32x32x16_bf16 v[128:143], v[28:31], a[64:67], v[128:143]
	v_cvt_pk_bf16_f32 v146, v116, v117
	v_cvt_pk_bf16_f32 v147, v118, v119
	v_mfma_f32_32x32x16_bf16 v[128:143], v[32:35], a[84:87], v[128:143]
	buffer_store_dwordx4 v[144:147], v11, s[20:23], s56 offen offset:0 sc1
	v_mfma_f32_32x32x16_bf16 v[128:143], v[36:39], a[104:107], v[128:143]
	v_cvt_pk_bf16_f32 v148, v120, v121
	v_cvt_pk_bf16_f32 v149, v122, v123
	v_mfma_f32_32x32x16_bf16 v[128:143], v[40:43], a[124:127], v[128:143]
	buffer_store_dwordx4 v[144:147], v13, s[20:23], s55 offen offset:0 sc1
	v_mfma_f32_32x32x16_bf16 v[128:143], v[44:47], a[144:147], v[128:143]
	v_cvt_pk_bf16_f32 v150, v124, v125
	v_cvt_pk_bf16_f32 v151, v126, v127
	v_mfma_f32_32x32x16_bf16 v[128:143], v[48:51], a[164:167], v[128:143]
	v_mfma_f32_32x32x16_bf16 v[128:143], v[52:55], a[184:187], v[128:143]
	buffer_store_dwordx4 v[148:151], v11, s[20:23], s56 offen offset:256 sc1
	v_mfma_f32_32x32x16_bf16 v[128:143], v[56:59], a[204:207], v[128:143]
	v_mfma_f32_32x32x16_bf16 v[128:143], v[60:63], a[224:227], v[128:143]
	buffer_store_dwordx4 v[148:151], v13, s[20:23], s55 offen offset:256 sc1
	s_add_u32 s55, s37, 163840
	s_add_u32 s56, s52, 131072
	v_mfma_f32_32x32x16_bf16 v[112:127], v[16:19], a[8:11], 0
	v_mfma_f32_32x32x16_bf16 v[112:127], v[20:23], a[28:31], v[112:127]
	s_add_u32 s79, s48, 2
	s_cmp_ge_u32 s51, s79
	s_cbranch_scc1 .Lt2_l50
	s_cmp_ge_u32 s73, s79
	s_cbranch_scc1 .Lt2_l51
	s_waitcnt vmcnt(8)
	v_readfirstlane_b32 s53, v8
	v_readlane_b32 s18, v8, 1
	s_add_u32 s18, s18, 15
	s_min_u32 s53, s53, s18
	s_max_u32 s73, s73, s53
	s_cmp_lt_u32 s73, s79
	s_cbranch_scc1 .Lt2_l50
.Lt2_l51:
	buffer_load_dwordx4 v[164:167], v176, s[20:23], s34 offen offset:0 sc1
	buffer_load_dwordx4 v[168:171], v177, s[20:23], s34 offen offset:0 sc1
	buffer_load_dwordx4 v[172:175], v178, s[20:23], s34 offen offset:0 sc1
	s_mov_b32 s51, s79
	s_mov_b32 s59, 1
	s_add_u32 s34, s34, s35
	s_xor_b32 s34, s34, s40
.Lt2_l50:
	v_mfma_f32_32x32x16_bf16 v[112:127], v[24:27], a[48:51], v[112:127]
	v_cvt_pk_bf16_f32 v152, v128, v129
	v_cvt_pk_bf16_f32 v153, v130, v131
	s_add_u32 s53, s48, 2
	s_cmp_ge_u32 s51, s53
	s_cselect_b64 exec, 0, 1
	v_mov_b32 v10, s53
	ds_write_b32 v181, v10 offset:0
	s_mov_b64 exec, -1
	v_mfma_f32_32x32x16_bf16 v[112:127], v[28:31], a[68:71], v[112:127]
	v_cvt_pk_bf16_f32 v154, v132, v133
	v_cvt_pk_bf16_f32 v155, v134, v135
	v_mfma_f32_32x32x16_bf16 v[112:127], v[32:35], a[88:91], v[112:127]
	buffer_store_dwordx4 v[152:155], v11, s[20:23], s56 offen offset:0 sc1
	v_mfma_f32_32x32x16_bf16 v[112:127], v[36:39], a[108:111], v[112:127]
	v_cvt_pk_bf16_f32 v156, v136, v137
	v_cvt_pk_bf16_f32 v157, v138, v139
	v_mfma_f32_32x32x16_bf16 v[112:127], v[40:43], a[128:131], v[112:127]
	buffer_store_dwordx4 v[152:155], v13, s[20:23], s55 offen offset:0 sc1
	v_mfma_f32_32x32x16_bf16 v[112:127], v[44:47], a[148:151], v[112:127]
	v_cvt_pk_bf16_f32 v158, v140, v141
	v_cvt_pk_bf16_f32 v159, v142, v143
	v_mfma_f32_32x32x16_bf16 v[112:127], v[48:51], a[168:171], v[112:127]
	v_mfma_f32_32x32x16_bf16 v[112:127], v[52:55], a[188:191], v[112:127]
	buffer_store_dwordx4 v[156:159], v11, s[20:23], s56 offen offset:256 sc1
	v_mfma_f32_32x32x16_bf16 v[112:127], v[56:59], a[208:211], v[112:127]
	s_add_u32 s53, s48, 3
	s_cmp_ge_u32 s73, s53
	s_cbranch_scc1 .Lt2_l52
	buffer_load_dword v8, v12, s[20:23], 0 offen sc1
.Lt2_l52:
	v_mfma_f32_32x32x16_bf16 v[112:127], v[60:63], a[228:231], v[112:127]
	buffer_store_dwordx4 v[156:159], v13, s[20:23], s55 offen offset:256 sc1
	s_add_u32 s55, s37, 327680
	s_add_u32 s56, s52, 262144
	v_mfma_f32_32x32x16_bf16 v[128:143], v[16:19], a[12:15], 0
	v_mfma_f32_32x32x16_bf16 v[128:143], v[20:23], a[32:35], v[128:143]
	v_mfma_f32_32x32x16_bf16 v[128:143], v[24:27], a[52:55], v[128:143]
	v_cvt_pk_bf16_f32 v144, v112, v113
	v_cvt_pk_bf16_f32 v145, v114, v115
	v_mfma_f32_32x32x16_bf16 v[128:143], v[28:31], a[72:75], v[128:143]
	v_cvt_pk_bf16_f32 v146, v116, v117
	v_cvt_pk_bf16_f32 v147, v118, v119
	v_mfma_f32_32x32x16_bf16 v[128:143], v[32:35], a[92:95], v[128:143]
	buffer_store_dwordx4 v[144:147], v11, s[20:23], s56 offen offset:0 sc1
	v_mfma_f32_32x32x16_bf16 v[128:143], v[36:39], a[112:115], v[128:143]
	v_cvt_pk_bf16_f32 v148, v120, v121
	v_cvt_pk_bf16_f32 v149, v122, v123
	v_mfma_f32_32x32x16_bf16 v[128:143], v[40:43], a[132:135], v[128:143]
	buffer_store_dwordx4 v[144:147], v13, s[20:23], s55 offen offset:0 sc1
	v_mfma_f32_32x32x16_bf16 v[128:143], v[44:47], a[152:155], v[128:143]
	v_cvt_pk_bf16_f32 v150, v124, v125
	v_cvt_pk_bf16_f32 v151, v126, v127
	v_mfma_f32_32x32x16_bf16 v[128:143], v[48:51], a[172:175], v[128:143]
	v_mfma_f32_32x32x16_bf16 v[128:143], v[52:55], a[192:195], v[128:143]
	buffer_store_dwordx4 v[148:151], v11, s[20:23], s56 offen offset:256 sc1
	v_mfma_f32_32x32x16_bf16 v[128:143], v[56:59], a[212:215], v[128:143]
	v_mfma_f32_32x32x16_bf16 v[128:143], v[60:63], a[232:235], v[128:143]
	buffer_store_dwordx4 v[148:151], v13, s[20:23], s55 offen offset:256 sc1
	s_add_u32 s55, s37, 491520
	s_add_u32 s56, s52, 393216
	s_waitcnt vmcnt(10)
	ds_write_b128 v179, v[164:167] offset:12288
	v_mfma_f32_32x32x16_bf16 v[112:127], v[16:19], a[16:19], 0
	s_waitcnt vmcnt(9)
	ds_write_b128 v179, v[168:171] offset:13312
	v_mfma_f32_32x32x16_bf16 v[112:127], v[20:23], a[36:39], v[112:127]
	s_waitcnt vmcnt(8)
	ds_write_b128 v179, v[172:175] offset:14336
	v_mfma_f32_32x32x16_bf16 v[112:127], v[24:27], a[56:59], v[112:127]
	v_cvt_pk_bf16_f32 v152, v128, v129
	v_cvt_pk_bf16_f32 v153, v130, v131
	s_waitcnt lgkmcnt(0)
	s_barrier
	v_mov_b32 v9, s48
	s_mov_b32 s62, s48
	s_mov_b64 exec, s[60:61]
	buffer_store_dword v9, off, s[20:23], s36 sc1
	s_mov_b64 exec, -1
	s_cmp_lg_u32 s48, s63
	s_cbranch_scc1 .Lt2_l53
	buffer_wbl2 sc1
.Lt2_l53:
	ds_read_b32 v10, v181 offset:0
	v_mfma_f32_32x32x16_bf16 v[112:127], v[28:31], a[76:79], v[112:127]
	v_cvt_pk_bf16_f32 v154, v132, v133
	v_cvt_pk_bf16_f32 v155, v134, v135
	ds_read_b128 v[64:67], v180 offset:12288
	ds_read_b128 v[68:71], v180 offset:13312
	v_mfma_f32_32x32x16_bf16 v[112:127], v[32:35], a[96:99], v[112:127]
	buffer_store_dwordx4 v[152:155], v11, s[20:23], s56 offen offset:0 sc1
	ds_read_b128 v[72:75], v180 offset:14336
	v_mfma_f32_32x32x16_bf16 v[112:127], v[36:39], a[116:119], v[112:127]
	v_cvt_pk_bf16_f32 v156, v136, v137
	v_cvt_pk_bf16_f32 v157, v138, v139
	ds_read_b128 v[76:79], v180 offset:15360
	v_mfma_f32_32x32x16_bf16 v[112:127], v[40:43], a[136:139], v[112:127]
	buffer_store_dwordx4 v[152:155], v13, s[20:23], s55 offen offset:0 sc1
	ds_read_b128 v[80:83], v180 offset:16384
	ds_read_b128 v[84:87], v180 offset:17408
	v_mfma_f32_32x32x16_bf16 v[112:127], v[44:47], a[156:159], v[112:127]
	v_cvt_pk_bf16_f32 v158, v140, v141
	v_cvt_pk_bf16_f32 v159, v142, v143
	ds_read_b128 v[88:91], v180 offset:18432
	v_mfma_f32_32x32x16_bf16 v[112:127], v[48:51], a[176:179], v[112:127]
	ds_read_b128 v[92:95], v180 offset:19456
	v_mfma_f32_32x32x16_bf16 v[112:127], v[52:55], a[196:199], v[112:127]
	buffer_store_dwordx4 v[156:159], v11, s[20:23], s56 offen offset:256 sc1
	ds_read_b128 v[96:99], v180 offset:20480
	ds_read_b128 v[100:103], v180 offset:21504
	v_mfma_f32_32x32x16_bf16 v[112:127], v[56:59], a[216:219], v[112:127]
	ds_read_b128 v[104:107], v180 offset:22528
	v_mfma_f32_32x32x16_bf16 v[112:127], v[60:63], a[236:239], v[112:127]
	buffer_store_dwordx4 v[156:159], v13, s[20:23], s55 offen offset:256 sc1
	s_add_u32 s55, s37, 655360
	s_add_u32 s56, s52, 524288
	ds_read_b128 v[108:111], v180 offset:23552
	s_waitcnt lgkmcnt(12)
	v_readfirstlane_b32 s53, v10
	s_add_u32 s37, s37, 4194304
	s_add_u32 s48, s48, 1
	s_cmp_le_u32 s53, s48
	s_cbranch_scc1 .Lt2_l46
	s_add_u32 s79, s48, 1
	s_cmp_ge_u32 s51, s79
	s_cbranch_scc1 .Lt2_l54
	s_cmp_ge_u32 s73, s79
	s_cbranch_scc1 .Lt2_l55
	s_waitcnt vmcnt(9)
	v_readfirstlane_b32 s53, v8
	v_readlane_b32 s18, v8, 1
	s_add_u32 s18, s18, 15
	s_min_u32 s53, s53, s18
	s_max_u32 s73, s73, s53
	s_cmp_lt_u32 s73, s79
	s_cbranch_scc1 .Lt2_l54
.Lt2_l55:
	buffer_load_dwordx4 v[164:167], v176, s[20:23], s34 offen offset:0 sc1
	buffer_load_dwordx4 v[168:171], v177, s[20:23], s34 offen offset:0 sc1
	buffer_load_dwordx4 v[172:175], v178, s[20:23], s34 offen offset:0 sc1
	s_mov_b32 s51, s79
	s_mov_b32 s59, 1
	s_add_u32 s34, s34, s35
	s_xor_b32 s34, s34, s40
.Lt2_l54:
	v_cvt_pk_bf16_f32 v144, v112, v113
	v_cvt_pk_bf16_f32 v145, v114, v115
	v_cvt_pk_bf16_f32 v146, v116, v117
	v_cvt_pk_bf16_f32 v147, v118, v119
	buffer_store_dwordx4 v[144:147], v11, s[20:23], s56 offen offset:0 sc1
	buffer_store_dwordx4 v[144:147], v13, s[20:23], s55 offen offset:0 sc1
	v_cvt_pk_bf16_f32 v148, v120, v121
	v_cvt_pk_bf16_f32 v149, v122, v123
	v_cvt_pk_bf16_f32 v150, v124, v125
	v_cvt_pk_bf16_f32 v151, v126, v127
	buffer_store_dwordx4 v[148:151], v11, s[20:23], s56 offen offset:256 sc1
	buffer_store_dwordx4 v[148:151], v13, s[20:23], s55 offen offset:256 sc1
	buffer_load_dword v8, v12, s[20:23], 0 offen sc1
	s_waitcnt vmcnt(0)
	s_waitcnt lgkmcnt(0)
	s_barrier
	v_mov_b32 v9, s48
	s_mov_b32 s62, s48
	s_mov_b64 exec, s[60:61]
	buffer_store_dword v9, off, s[20:23], s36 sc1
	s_mov_b64 exec, -1
	v_readfirstlane_b32 s53, v8
	v_readlane_b32 s18, v8, 1
	s_add_u32 s18, s18, 15
	s_min_u32 s53, s53, s18
	s_max_u32 s73, s73, s53
	s_add_u32 s78, s48, 1
.Lt2_f56:
	s_cmp_ge_u32 s73, s78
	s_cbranch_scc1 .Lt2_f57
	s_cmp_eq_u32 s69, 0
	s_cbranch_scc1 .Lt2_f57
	s_sub_u32 s69, s69, 1
	buffer_load_dword v9, v12, s[20:23], 0 offen sc1
	s_waitcnt vmcnt(0)
	v_readfirstlane_b32 s73, v9
	s_sleep 2
	v_readlane_b32 s18, v9, 1
	s_add_u32 s18, s18, 15
	s_min_u32 s73, s73, s18
	s_branch .Lt2_f56
.Lt2_f57:
	s_max_u32 s73, s73, s78
	s_add_u32 s79, s48, 1
	s_cmp_ge_u32 s51, s79
	s_cbranch_scc1 .Lt2_l58
	s_cmp_ge_u32 s73, s79
	s_cbranch_scc1 .Lt2_l59
	v_readfirstlane_b32 s53, v8
	v_readlane_b32 s18, v8, 1
	s_add_u32 s18, s18, 15
	s_min_u32 s53, s53, s18
	s_max_u32 s73, s73, s53
	s_cmp_lt_u32 s73, s79
	s_cbranch_scc1 .Lt2_l58
.Lt2_l59:
	buffer_load_dwordx4 v[164:167], v176, s[20:23], s34 offen offset:0 sc1
	buffer_load_dwordx4 v[168:171], v177, s[20:23], s34 offen offset:0 sc1
	buffer_load_dwordx4 v[172:175], v178, s[20:23], s34 offen offset:0 sc1
	s_mov_b32 s51, s79
	s_mov_b32 s59, 1
	s_add_u32 s34, s34, s35
	s_xor_b32 s34, s34, s40
.Lt2_l58:
	s_cmp_gt_u32 s51, s78
	s_cbranch_scc1 .Lt2_l60
	s_waitcnt vmcnt(2)
	ds_write_b128 v179, v[164:167] offset:12288
	s_waitcnt vmcnt(1)
	ds_write_b128 v179, v[168:171] offset:13312
	s_waitcnt vmcnt(0)
	ds_write_b128 v179, v[172:175] offset:14336
.Lt2_l60:
	s_waitcnt lgkmcnt(0)
	s_barrier
	ds_read_b128 v[64:67], v180 offset:12288
	ds_read_b128 v[68:71], v180 offset:13312
	ds_read_b128 v[72:75], v180 offset:14336
	ds_read_b128 v[76:79], v180 offset:15360
	ds_read_b128 v[80:83], v180 offset:16384
	ds_read_b128 v[84:87], v180 offset:17408
	ds_read_b128 v[88:91], v180 offset:18432
	ds_read_b128 v[92:95], v180 offset:19456
	ds_read_b128 v[96:99], v180 offset:20480
	ds_read_b128 v[100:103], v180 offset:21504
	ds_read_b128 v[104:107], v180 offset:22528
	ds_read_b128 v[108:111], v180 offset:23552
.Lt2_l46:
	s_waitcnt lgkmcnt(11)
	v_mfma_f32_32x32x16_bf16 v[128:143], v[64:67], a[0:3], 0
	s_add_u32 s79, s48, 2
	s_cmp_ge_u32 s51, s79
	s_cbranch_scc1 .Lt2_l62
	s_cmp_ge_u32 s73, s79
	s_cbranch_scc1 .Lt2_l63
	s_waitcnt vmcnt(9)
	v_readfirstlane_b32 s53, v8
	v_readlane_b32 s18, v8, 1
	s_add_u32 s18, s18, 15
	s_min_u32 s53, s53, s18
	s_max_u32 s73, s73, s53
	s_cmp_lt_u32 s73, s79
	s_cbranch_scc1 .Lt2_l62
.Lt2_l63:
	buffer_load_dwordx4 v[164:167], v176, s[20:23], s34 offen offset:0 sc1
	buffer_load_dwordx4 v[168:171], v177, s[20:23], s34 offen offset:0 sc1
	buffer_load_dwordx4 v[172:175], v178, s[20:23], s34 offen offset:0 sc1
	s_mov_b32 s51, s79
	s_mov_b32 s59, 1
	s_add_u32 s34, s34, s35
	s_xor_b32 s34, s34, s40
.Lt2_l62:
	s_waitcnt lgkmcnt(10)
	v_mfma_f32_32x32x16_bf16 v[128:143], v[68:71], a[20:23], v[128:143]
	s_and_b32 s52, s37, 67108863
	s_cmp_lt_u32 s48, 8
	s_cselect_b32 s53, 67108864, 0
	s_add_u32 s52, s52, s53
	s_add_u32 s53, s48, 2
	s_cmp_ge_u32 s73, s53
	s_cbranch_scc1 .Lt2_l64
	buffer_load_dword v8, v12, s[20:23], 0 offen sc1
.Lt2_l64:
	s_waitcnt lgkmcnt(9)
	v_mfma_f32_32x32x16_bf16 v[128:143], v[72:75], a[40:43], v[128:143]
	v_cvt_pk_bf16_f32 v144, v112, v113
	v_cvt_pk_bf16_f32 v145, v114, v115
	s_waitcnt lgkmcnt(8)
	v_mfma_f32_32x32x16_bf16 v[128:143], v[76:79], a[60:63], v[128:143]
	v_cvt_pk_bf16_f32 v146, v116, v117
	v_cvt_pk_bf16_f32 v147, v118, v119
	s_waitcnt lgkmcnt(7)
	v_mfma_f32_32x32x16_bf16 v[128:143], v[80:83], a[80:83], v[128:143]
	buffer_store_dwordx4 v[144:147], v11, s[20:23], s56 offen offset:0 sc1
	s_waitcnt lgkmcnt(6)
	v_mfma_f32_32x32x16_bf16 v[128:143], v[84:87], a[100:103], v[128:143]
	v_cvt_pk_bf16_f32 v148, v120, v121
	v_cvt_pk_bf16_f32 v149, v122, v123
	s_waitcnt lgkmcnt(5)
	v_mfma_f32_32x32x16_bf16 v[128:143], v[88:91], a[120:123], v[128:143]
	buffer_store_dwordx4 v[144:147], v13, s[20:23], s55 offen offset:0 sc1
	s_waitcnt lgkmcnt(4)
	v_mfma_f32_32x32x16_bf16 v[128:143], v[92:95], a[140:143], v[128:143]
	v_cvt_pk_bf16_f32 v150, v124, v125
	v_cvt_pk_bf16_f32 v151, v126, v127
	s_waitcnt lgkmcnt(3)
	v_mfma_f32_32x32x16_bf16 v[128:143], v[96:99], a[160:163], v[128:143]
	s_waitcnt lgkmcnt(2)
	v_mfma_f32_32x32x16_bf16 v[128:143], v[100:103], a[180:183], v[128:143]
	buffer_store_dwordx4 v[148:151], v11, s[20:23], s56 offen offset:256 sc1
	s_waitcnt lgkmcnt(1)
	v_mfma_f32_32x32x16_bf16 v[128:143], v[104:107], a[200:203], v[128:143]
	s_waitcnt lgkmcnt(0)
	v_mfma_f32_32x32x16_bf16 v[128:143], v[108:111], a[220:223], v[128:143]
	buffer_store_dwordx4 v[148:151], v13, s[20:23], s55 offen offset:256 sc1
	s_add_u32 s55, s37, 0
	s_add_u32 s56, s52, 0
	v_mfma_f32_32x32x16_bf16 v[112:127], v[64:67], a[4:7], 0
	v_mfma_f32_32x32x16_bf16 v[112:127], v[68:71], a[24:27], v[112:127]
	v_mfma_f32_32x32x16_bf16 v[112:127], v[72:75], a[44:47], v[112:127]
	v_cvt_pk_bf16_f32 v152, v128, v129
	v_cvt_pk_bf16_f32 v153, v130, v131
	v_mfma_f32_32x32x16_bf16 v[112:127], v[76:79], a[64:67], v[112:127]
	v_cvt_pk_bf16_f32 v154, v132, v133
	v_cvt_pk_bf16_f32 v155, v134, v135
	v_mfma_f32_32x32x16_bf16 v[112:127], v[80:83], a[84:87], v[112:127]
	buffer_store_dwordx4 v[152:155], v11, s[20:23], s56 offen offset:0 sc1
	v_mfma_f32_32x32x16_bf16 v[112:127], v[84:87], a[104:107], v[112:127]
	v_cvt_pk_bf16_f32 v156, v136, v137
	v_cvt_pk_bf16_f32 v157, v138, v139
	v_mfma_f32_32x32x16_bf16 v[112:127], v[88:91], a[124:127], v[112:127]
	buffer_store_dwordx4 v[152:155], v13, s[20:23], s55 offen offset:0 sc1
	v_mfma_f32_32x32x16_bf16 v[112:127], v[92:95], a[144:147], v[112:127]
	v_cvt_pk_bf16_f32 v158, v140, v141
	v_cvt_pk_bf16_f32 v159, v142, v143
	v_mfma_f32_32x32x16_bf16 v[112:127], v[96:99], a[164:167], v[112:127]
	v_mfma_f32_32x32x16_bf16 v[112:127], v[100:103], a[184:187], v[112:127]
	buffer_store_dwordx4 v[156:159], v11, s[20:23], s56 offen offset:256 sc1
	v_mfma_f32_32x32x16_bf16 v[112:127], v[104:107], a[204:207], v[112:127]
	v_mfma_f32_32x32x16_bf16 v[112:127], v[108:111], a[224:227], v[112:127]
	buffer_store_dwordx4 v[156:159], v13, s[20:23], s55 offen offset:256 sc1
	s_add_u32 s55, s37, 163840
	s_add_u32 s56, s52, 131072
	v_mfma_f32_32x32x16_bf16 v[128:143], v[64:67], a[8:11], 0
	v_mfma_f32_32x32x16_bf16 v[128:143], v[68:71], a[28:31], v[128:143]
	s_add_u32 s79, s48, 2
	s_cmp_ge_u32 s51, s79
	s_cbranch_scc1 .Lt2_l65
	s_cmp_ge_u32 s73, s79
	s_cbranch_scc1 .Lt2_l66
	s_waitcnt vmcnt(8)
	v_readfirstlane_b32 s53, v8
	v_readlane_b32 s18, v8, 1
	s_add_u32 s18, s18, 15
	s_min_u32 s53, s53, s18
	s_max_u32 s73, s73, s53
	s_cmp_lt_u32 s73, s79
	s_cbranch_scc1 .Lt2_l65
.Lt2_l66:
	buffer_load_dwordx4 v[164:167], v176, s[20:23], s34 offen offset:0 sc1
	buffer_load_dwordx4 v[168:171], v177, s[20:23], s34 offen offset:0 sc1
	buffer_load_dwordx4 v[172:175], v178, s[20:23], s34 offen offset:0 sc1
	s_mov_b32 s51, s79
	s_mov_b32 s59, 1
	s_add_u32 s34, s34, s35
	s_xor_b32 s34, s34, s40
.Lt2_l65:
	v_mfma_f32_32x32x16_bf16 v[128:143], v[72:75], a[48:51], v[128:143]
	v_cvt_pk_bf16_f32 v144, v112, v113
	v_cvt_pk_bf16_f32 v145, v114, v115
	s_add_u32 s53, s48, 2
	s_cmp_ge_u32 s51, s53
	s_cselect_b64 exec, 0, 1
	v_mov_b32 v10, s53
	ds_write_b32 v181, v10 offset:4
	s_mov_b64 exec, -1
	v_mfma_f32_32x32x16_bf16 v[128:143], v[76:79], a[68:71], v[128:143]
	v_cvt_pk_bf16_f32 v146, v116, v117
	v_cvt_pk_bf16_f32 v147, v118, v119
	v_mfma_f32_32x32x16_bf16 v[128:143], v[80:83], a[88:91], v[128:143]
	buffer_store_dwordx4 v[144:147], v11, s[20:23], s56 offen offset:0 sc1
	v_mfma_f32_32x32x16_bf16 v[128:143], v[84:87], a[108:111], v[128:143]
	v_cvt_pk_bf16_f32 v148, v120, v121
	v_cvt_pk_bf16_f32 v149, v122, v123
	v_mfma_f32_32x32x16_bf16 v[128:143], v[88:91], a[128:131], v[128:143]
	buffer_store_dwordx4 v[144:147], v13, s[20:23], s55 offen offset:0 sc1
	v_mfma_f32_32x32x16_bf16 v[128:143], v[92:95], a[148:151], v[128:143]
	v_cvt_pk_bf16_f32 v150, v124, v125
	v_cvt_pk_bf16_f32 v151, v126, v127
	v_mfma_f32_32x32x16_bf16 v[128:143], v[96:99], a[168:171], v[128:143]
	v_mfma_f32_32x32x16_bf16 v[128:143], v[100:103], a[188:191], v[128:143]
	buffer_store_dwordx4 v[148:151], v11, s[20:23], s56 offen offset:256 sc1
	v_mfma_f32_32x32x16_bf16 v[128:143], v[104:107], a[208:211], v[128:143]
	s_add_u32 s53, s48, 3
	s_cmp_ge_u32 s73, s53
	s_cbranch_scc1 .Lt2_l67
	buffer_load_dword v8, v12, s[20:23], 0 offen sc1
.Lt2_l67:
	v_mfma_f32_32x32x16_bf16 v[128:143], v[108:111], a[228:231], v[128:143]
	buffer_store_dwordx4 v[148:151], v13, s[20:23], s55 offen offset:256 sc1
	s_add_u32 s55, s37, 327680
	s_add_u32 s56, s52, 262144
	v_mfma_f32_32x32x16_bf16 v[112:127], v[64:67], a[12:15], 0
	v_mfma_f32_32x32x16_bf16 v[112:127], v[68:71], a[32:35], v[112:127]
	v_mfma_f32_32x32x16_bf16 v[112:127], v[72:75], a[52:55], v[112:127]
	v_cvt_pk_bf16_f32 v152, v128, v129
	v_cvt_pk_bf16_f32 v153, v130, v131
	v_mfma_f32_32x32x16_bf16 v[112:127], v[76:79], a[72:75], v[112:127]
	v_cvt_pk_bf16_f32 v154, v132, v133
	v_cvt_pk_bf16_f32 v155, v134, v135
	v_mfma_f32_32x32x16_bf16 v[112:127], v[80:83], a[92:95], v[112:127]
	buffer_store_dwordx4 v[152:155], v11, s[20:23], s56 offen offset:0 sc1
	v_mfma_f32_32x32x16_bf16 v[112:127], v[84:87], a[112:115], v[112:127]
	v_cvt_pk_bf16_f32 v156, v136, v137
	v_cvt_pk_bf16_f32 v157, v138, v139
	v_mfma_f32_32x32x16_bf16 v[112:127], v[88:91], a[132:135], v[112:127]
	buffer_store_dwordx4 v[152:155], v13, s[20:23], s55 offen offset:0 sc1
	v_mfma_f32_32x32x16_bf16 v[112:127], v[92:95], a[152:155], v[112:127]
	v_cvt_pk_bf16_f32 v158, v140, v141
	v_cvt_pk_bf16_f32 v159, v142, v143
	v_mfma_f32_32x32x16_bf16 v[112:127], v[96:99], a[172:175], v[112:127]
	v_mfma_f32_32x32x16_bf16 v[112:127], v[100:103], a[192:195], v[112:127]
	buffer_store_dwordx4 v[156:159], v11, s[20:23], s56 offen offset:256 sc1
	v_mfma_f32_32x32x16_bf16 v[112:127], v[104:107], a[212:215], v[112:127]
	v_mfma_f32_32x32x16_bf16 v[112:127], v[108:111], a[232:235], v[112:127]
	buffer_store_dwordx4 v[156:159], v13, s[20:23], s55 offen offset:256 sc1
	s_add_u32 s55, s37, 491520
	s_add_u32 s56, s52, 393216
	s_waitcnt vmcnt(10)
	ds_write_b128 v179, v[164:167] offset:0
	v_mfma_f32_32x32x16_bf16 v[128:143], v[64:67], a[16:19], 0
	s_waitcnt vmcnt(9)
	ds_write_b128 v179, v[168:171] offset:1024
	v_mfma_f32_32x32x16_bf16 v[128:143], v[68:71], a[36:39], v[128:143]
	s_waitcnt vmcnt(8)
	ds_write_b128 v179, v[172:175] offset:2048
	v_mfma_f32_32x32x16_bf16 v[128:143], v[72:75], a[56:59], v[128:143]
	v_cvt_pk_bf16_f32 v144, v112, v113
	v_cvt_pk_bf16_f32 v145, v114, v115
	s_waitcnt lgkmcnt(0)
	s_barrier
	v_mov_b32 v9, s48
	s_mov_b32 s62, s48
	s_mov_b64 exec, s[60:61]
	buffer_store_dword v9, off, s[20:23], s36 sc1
	s_mov_b64 exec, -1
	ds_read_b32 v10, v181 offset:4
	v_mfma_f32_32x32x16_bf16 v[128:143], v[76:79], a[76:79], v[128:143]
	v_cvt_pk_bf16_f32 v146, v116, v117
	v_cvt_pk_bf16_f32 v147, v118, v119
	ds_read_b128 v[16:19], v180 offset:0
	ds_read_b128 v[20:23], v180 offset:1024
	v_mfma_f32_32x32x16_bf16 v[128:143], v[80:83], a[96:99], v[128:143]
	buffer_store_dwordx4 v[144:147], v11, s[20:23], s56 offen offset:0 sc1
	ds_read_b128 v[24:27], v180 offset:2048
	v_mfma_f32_32x32x16_bf16 v[128:143], v[84:87], a[116:119], v[128:143]
	v_cvt_pk_bf16_f32 v148, v120, v121
	v_cvt_pk_bf16_f32 v149, v122, v123
	ds_read_b128 v[28:31], v180 offset:3072
	v_mfma_f32_32x32x16_bf16 v[128:143], v[88:91], a[136:139], v[128:143]
	buffer_store_dwordx4 v[144:147], v13, s[20:23], s55 offen offset:0 sc1
	ds_read_b128 v[32:35], v180 offset:4096
	ds_read_b128 v[36:39], v180 offset:5120
	v_mfma_f32_32x32x16_bf16 v[128:143], v[92:95], a[156:159], v[128:143]
	v_cvt_pk_bf16_f32 v150, v124, v125
	v_cvt_pk_bf16_f32 v151, v126, v127
	ds_read_b128 v[40:43], v180 offset:6144
	v_mfma_f32_32x32x16_bf16 v[128:143], v[96:99], a[176:179], v[128:143]
	ds_read_b128 v[44:47], v180 offset:7168
	v_mfma_f32_32x32x16_bf16 v[128:143], v[100:103], a[196:199], v[128:143]
	buffer_store_dwordx4 v[148:151], v11, s[20:23], s56 offen offset:256 sc1
	ds_read_b128 v[48:51], v180 offset:8192
	ds_read_b128 v[52:55], v180 offset:9216
	v_mfma_f32_32x32x16_bf16 v[128:143], v[104:107], a[216:219], v[128:143]
	ds_read_b128 v[56:59], v180 offset:10240
	v_mfma_f32_32x32x16_bf16 v[128:143], v[108:111], a[236:239], v[128:143]
	buffer_store_dwordx4 v[148:151], v13, s[20:23], s55 offen offset:256 sc1
	s_add_u32 s55, s37, 655360
	s_add_u32 s56, s52, 524288
	ds_read_b128 v[60:63], v180 offset:11264
	s_cmp_ge_u32 s48, 7
	s_cselect_b64 exec, -1, 0
	v_mov_b32 v13, v7
	s_mov_b64 exec, -1
	s_waitcnt lgkmcnt(12)
	v_readfirstlane_b32 s53, v10
	s_add_u32 s37, s37, 4194304
	s_add_u32 s48, s48, 1
	s_cmp_le_u32 s53, s48
	s_cbranch_scc1 .Lt2_l61
	s_cmp_eq_u32 s48, 64
	s_cbranch_scc1 .Lt2_out14
	s_add_u32 s79, s48, 1
	s_cmp_ge_u32 s51, s79
	s_cbranch_scc1 .Lt2_l68
	s_cmp_ge_u32 s73, s79
	s_cbranch_scc1 .Lt2_l69
	s_waitcnt vmcnt(9)
	v_readfirstlane_b32 s53, v8
	v_readlane_b32 s18, v8, 1
	s_add_u32 s18, s18, 15
	s_min_u32 s53, s53, s18
	s_max_u32 s73, s73, s53
	s_cmp_lt_u32 s73, s79
	s_cbranch_scc1 .Lt2_l68
.Lt2_l69:
	buffer_load_dwordx4 v[164:167], v176, s[20:23], s34 offen offset:0 sc1
	buffer_load_dwordx4 v[168:171], v177, s[20:23], s34 offen offset:0 sc1
	buffer_load_dwordx4 v[172:175], v178, s[20:23], s34 offen offset:0 sc1
	s_mov_b32 s51, s79
	s_mov_b32 s59, 1
	s_add_u32 s34, s34, s35
	s_xor_b32 s34, s34, s40
.Lt2_l68:
	v_cvt_pk_bf16_f32 v152, v128, v129
	v_cvt_pk_bf16_f32 v153, v130, v131
	v_cvt_pk_bf16_f32 v154, v132, v133
	v_cvt_pk_bf16_f32 v155, v134, v135
	buffer_store_dwordx4 v[152:155], v11, s[20:23], s56 offen offset:0 sc1
	buffer_store_dwordx4 v[152:155], v13, s[20:23], s55 offen offset:0 sc1
	v_cvt_pk_bf16_f32 v156, v136, v137
	v_cvt_pk_bf16_f32 v157, v138, v139
	v_cvt_pk_bf16_f32 v158, v140, v141
	v_cvt_pk_bf16_f32 v159, v142, v143
	buffer_store_dwordx4 v[156:159], v11, s[20:23], s56 offen offset:256 sc1
	buffer_store_dwordx4 v[156:159], v13, s[20:23], s55 offen offset:256 sc1
	buffer_load_dword v8, v12, s[20:23], 0 offen sc1
	s_waitcnt vmcnt(0)
	s_waitcnt lgkmcnt(0)
	s_barrier
	v_mov_b32 v9, s48
	s_mov_b32 s62, s48
	s_mov_b64 exec, s[60:61]
	buffer_store_dword v9, off, s[20:23], s36 sc1
	s_mov_b64 exec, -1
	v_readfirstlane_b32 s53, v8
	v_readlane_b32 s18, v8, 1
	s_add_u32 s18, s18, 15
	s_min_u32 s53, s53, s18
	s_max_u32 s73, s73, s53
	s_add_u32 s78, s48, 1
.Lt2_f70:
	s_cmp_ge_u32 s73, s78
	s_cbranch_scc1 .Lt2_f71
	s_cmp_eq_u32 s69, 0
	s_cbranch_scc1 .Lt2_f71
	s_sub_u32 s69, s69, 1
	buffer_load_dword v9, v12, s[20:23], 0 offen sc1
	s_waitcnt vmcnt(0)
	v_readfirstlane_b32 s73, v9
	s_sleep 2
	v_readlane_b32 s18, v9, 1
	s_add_u32 s18, s18, 15
	s_min_u32 s73, s73, s18
	s_branch .Lt2_f70
.Lt2_f71:
	s_max_u32 s73, s73, s78
	s_add_u32 s79, s48, 1
	s_cmp_ge_u32 s51, s79
	s_cbranch_scc1 .Lt2_l72
	s_cmp_ge_u32 s73, s79
	s_cbranch_scc1 .Lt2_l73
	v_readfirstlane_b32 s53, v8
	v_readlane_b32 s18, v8, 1
	s_add_u32 s18, s18, 15
	s_min_u32 s53, s53, s18
	s_max_u32 s73, s73, s53
	s_cmp_lt_u32 s73, s79
	s_cbranch_scc1 .Lt2_l72
.Lt2_l73:
	buffer_load_dwordx4 v[164:167], v176, s[20:23], s34 offen offset:0 sc1
	buffer_load_dwordx4 v[168:171], v177, s[20:23], s34 offen offset:0 sc1
	buffer_load_dwordx4 v[172:175], v178, s[20:23], s34 offen offset:0 sc1
	s_mov_b32 s51, s79
	s_mov_b32 s59, 1
	s_add_u32 s34, s34, s35
	s_xor_b32 s34, s34, s40
.Lt2_l72:
	s_cmp_gt_u32 s51, s78
	s_cbranch_scc1 .Lt2_l74
	s_waitcnt vmcnt(2)
	ds_write_b128 v179, v[164:167] offset:0
	s_waitcnt vmcnt(1)
	ds_write_b128 v179, v[168:171] offset:1024
	s_waitcnt vmcnt(0)
	ds_write_b128 v179, v[172:175] offset:2048
.Lt2_l74:
	s_waitcnt lgkmcnt(0)
	s_barrier
	ds_read_b128 v[16:19], v180 offset:0
	ds_read_b128 v[20:23], v180 offset:1024
	ds_read_b128 v[24:27], v180 offset:2048
	ds_read_b128 v[28:31], v180 offset:3072
	ds_read_b128 v[32:35], v180 offset:4096
	ds_read_b128 v[36:39], v180 offset:5120
	ds_read_b128 v[40:43], v180 offset:6144
	ds_read_b128 v[44:47], v180 offset:7168
	ds_read_b128 v[48:51], v180 offset:8192
	ds_read_b128 v[52:55], v180 offset:9216
	ds_read_b128 v[56:59], v180 offset:10240
	ds_read_b128 v[60:63], v180 offset:11264
.Lt2_l61:
	s_branch .Lt2_seg45
.Lt2_out14:
	v_cvt_pk_bf16_f32 v152, v128, v129
	v_cvt_pk_bf16_f32 v153, v130, v131
	v_cvt_pk_bf16_f32 v154, v132, v133
	v_cvt_pk_bf16_f32 v155, v134, v135
	buffer_store_dwordx4 v[152:155], v11, s[20:23], s56 offen offset:0 sc1
	buffer_store_dwordx4 v[152:155], v13, s[20:23], s55 offen offset:0 sc1
	v_cvt_pk_bf16_f32 v156, v136, v137
	v_cvt_pk_bf16_f32 v157, v138, v139
	v_cvt_pk_bf16_f32 v158, v140, v141
	v_cvt_pk_bf16_f32 v159, v142, v143
	buffer_store_dwordx4 v[156:159], v11, s[20:23], s56 offen offset:256 sc1
	buffer_store_dwordx4 v[156:159], v13, s[20:23], s55 offen offset:256 sc1
	s_waitcnt vmcnt(0)
	s_waitcnt lgkmcnt(0)
	s_barrier
	v_mov_b32 v9, s48
	s_mov_b32 s62, s48
	s_mov_b64 exec, s[60:61]
	buffer_store_dword v9, off, s[20:23], s36 sc1
	s_mov_b64 exec, -1
	s_branch .Lt2_end5
.Lt2_x4:
	s_waitcnt vmcnt(0) lgkmcnt(0)
	s_mov_b32 s47, -1
	s_mov_b32 m0, s19
.Lt2_end5:
	s_waitcnt vmcnt(0) lgkmcnt(0)
)ASM" : "+{v[0:15]}"(vi), "+{s[32:47]}"(si) : "{s[20:23]}"(rs), "{s[24:27]}"(rw) : "a0", "a1", "a2", "a3", "a4", "a5", "a6", "a7", "a8", "a9", "a10", "a11", "a12", "a13", "a14", "a15", "a16", "a17", "a18", "a19", "a20", "a21", "a22", "a23", "a24", "a25", "a26", "a27", "a28", "a29", "a30", "a31", "a32", "a33", "a34", "a35", "a36", "a37", "a38", "a39", "a40", "a41", "a42", "a43", "a44", "a45", "a46", "a47", "a48", "a49", "a50", "a51", "a52", "a53", "a54", "a55", "a56", "a57", "a58", "a59", "a60", "a61", "a62", "a63", "a64", "a65", "a66", "a67", "a68", "a69", "a70", "a71", "a72", "a73", "a74", "a75", "a76", "a77", "a78", "a79", "a80", "a81", "a82", "a83", "a84", "a85", "a86", "a87", "a88", "a89", "a90", "a91", "a92", "a93", "a94", "a95", "a96", "a97", "a98", "a99", "a100", "a101", "a102", "a103", "a104", "a105", "a106", "a107", "a108", "a109", "a110", "a111", "a112", "a113", "a114", "a115", "a116", "a117", "a118", "a119", "a120", "a121", "a122", "a123", "a124", "a125", "a126", "a127", "a128", "a129", "a130", "a131", "a132", "a133", "a134", "a135", "a136", "a137", "a138", "a139", "a140", "a141", "a142", "a143", "a144", "a145", "a146", "a147", "a148", "a149", "a150", "a151", "a152", "a153", "a154", "a155", "a156", "a157", "a158", "a159", "a160", "a161", "a162", "a163", "a164", "a165", "a166", "a167", "a168", "a169", "a170", "a171", "a172", "a173", "a174", "a175", "a176", "a177", "a178", "a179", "a180", "a181", "a182", "a183", "a184", "a185", "a186", "a187", "a188", "a189", "a190", "a191", "a192", "a193", "a194", "a195", "a196", "a197", "a198", "a199", "a200", "a201", "a202", "a203", "a204", "a205", "a206", "a207", "a208", "a209", "a210", "a211", "a212", "a213", "a214", "a215", "a216", "a217", "a218", "a219", "a220", "a221", "a222", "a223", "a224", "a225", "a226", "a227", "a228", "a229", "a230", "a231", "a232", "a233", "a234", "a235", "a236", "a237", "a238", "a239", "s18", "s19", "s48", "s51", "s52", "s53", "s55", "s56", "s59", "s60", "s61", "s62", "s63", "s64", "s69", "s70", "s71", "s72", "s73", "s76", "s78", "s79", "s98", "s99", "v16", "v17", "v18", "v19", "v20", "v21", "v22", "v23", "v24", "v25", "v26", "v27", "v28", "v29", "v30", "v31", "v32", "v33", "v34", "v35", "v36", "v37", "v38", "v39", "v40", "v41", "v42", "v43", "v44", "v45", "v46", "v47", "v48", "v49", "v50", "v51", "v52", "v53", "v54", "v55", "v56", "v57", "v58", "v59", "v60", "v61", "v62", "v63", "v64", "v65", "v66", "v67", "v68", "v69", "v70", "v71", "v72", "v73", "v74", "v75", "v76", "v77", "v78", "v79", "v80", "v81", "v82", "v83", "v84", "v85", "v86", "v87", "v88", "v89", "v90", "v91", "v92", "v93", "v94", "v95", "v96", "v97", "v98", "v99", "v100", "v101", "v102", "v103", "v104", "v105", "v106", "v107", "v108", "v109", "v110", "v111", "v112", "v113", "v114", "v115", "v116", "v117", "v118", "v119", "v120", "v121", "v122", "v123", "v124", "v125", "v126", "v127", "v128", "v129", "v130", "v131", "v132", "v133", "v134", "v135", "v136", "v137", "v138", "v139", "v140", "v141", "v142", "v143", "v144", "v145", "v146", "v147", "v148", "v149", "v150", "v151", "v152", "v153", "v154", "v155", "v156", "v157", "v158", "v159", "v160", "v161", "v162", "v163", "v164", "v165", "v166", "v167", "v168", "v169", "v170", "v171", "v172", "v173", "v174", "v175", "v176", "v177", "v178", "v179", "v180", "v181", "v182", "vcc", "scc", "memory");
  return si[15] == ~0u ? 2 : si[11] >> 13;
}
DI void role_qk(KArgs& a, const int h, const unsigned hp, ldsp lds, const int lane, const int wv) {
  const int mat = wv >> 1, hf = wv & 1, g = lane >> 4, c = lane & 15, q = c >> 2, p = c & 3;
  const int half = (c >> 1) & 1, arow = 2 * (c >> 2) + (c & 1);
  const unsigned sd = 0;
  const u32x2 rg = rings(sd);
  const int pm = q & 1 ? 3 - p : p;
  const unsigned reg = la(lds) + wv * WREG, qimg = la(lds) + OFF_IMG, kimg = qimg + 8 * IRS;
  u32x16 vi, si;
  vi[0] = wsrc(lane, 16 * hf, h >> 3); vi[1] = wsrc(lane, 16 * hf + 1, h >> 3);
  vi[3] = la(lds) + 2 * mat * WREG + wblk(lane, 6 * hf); vi[2] = la(lds) + 2 * mat * WREG + wblk(lane, 6 * hf + 2);
  vi[4] = half ? 0xEEEE : 0x4444;
  vi[5] = g * 256 + arow * 32 + half * 16;
  vi[6] = reg + lane * 16;
  vi[7] = (mat ? kimg + 4 * c : qimg + 32 * (c >> 3) + 16 * (c & 1) + 8 * ((c >> 2) & 1) + 4 * half) + 2 * g * IRS + hf * 64;
  vi[8] = kimg + (mat ? arow : 2 * (((q ^ pm) >> 1) + 2 * (q & 1)) + (pm & 1)) * IRS + g * 16;
  vi[9] = mat ? 16 * hf + c - 8 * g : qimg + (q + 4 * ((c ^ hf) & 1)) * IRS + g * 32 + half * 16;
  vi[10] = mat ? pxoff(hf, h) + lane * 16 : U_PMX + qb(q) + (h >> 3) * 32768 + (h & 7) * 512 + (pm >> 1) * 256 + wv * 128 + g * 32 + (pm & 1) * 16;
  vi[11] = mat ? pmoff(hf, h) + c * 16 + g * 4 : pm == (q & 2) ? U_PMM + qb(q) + (h >> 1) * 256 + wv * 128 + (h & 1) * 64 + g * 16 : OOB;
  vi[12] = lane * 128;
  vi[13] = lane * 16;
  vi[14] = la(lds) + g * 256 + (mat ? c : (pm >> 1) * 8 + q * 2 + (pm & 1)) * 16;
  vi[15] = mat ? (lane < 24 ? 1024 + lane * 128 : OOB) : U_RM + (lane < 40 ? lane : 0) * 128;
  si = (u32x16)0u;
  si[0] = h * 384; si[1] = reg; si[2] = opq(wv); si[3] = opq(hp); si[4] = h & 1 ? 12288 : 4096; si[5] = opq(wv);
  si[6] = rg.x + U_QM + h * 128; si[7] = flag1(sd, h + 32 * (1 - hf)); si[8] = t1p(sd, h + 32 * (1 - hf), 0) + nb(sd, 0); si[9] = qimg + 384;
  si[10] = hf * WREG; si[11] = WREG + hf * 6144; si[12] = la(lds) + LDS_B - 10240; si[13] = qb(h >> 3); si[14] = rg.x; si[15] = dbgo(h, wv);
#pragma unroll
  for (int i = 0; i < 16; ++i) si[i] = rfl(si[i]);
  u32x4 rs = rsw(a.ws, WS_RANGE), rw = rsw(mat ? a.Wk : a.Wq, 640u * 12288u);
  asm volatile(R"ASM(
	s_mov_b32 s79, 196608
	v_xor_b32 v124, 32, v3
	v_xor_b32 v125, 32, v2
	s_mov_b32 s19, m0
	s_mov_b32 s18, 12566528
	s_mov_b32 s69, 0x20000
	buffer_load_dword v117, off, s[20:23], s18 offset:3968 sc1
	s_add_u32 m0, s33, 0
	s_nop 0
	buffer_load_dwordx4 v0, s[24:27], s32 offen offset:0 nt lds
	s_add_u32 m0, s33, 1920
	s_nop 0
	buffer_load_dwordx4 v0, s[24:27], s32 offen offset:128 nt lds
	s_add_u32 m0, s33, 3840
	s_nop 0
	buffer_load_dwordx4 v0, s[24:27], s32 offen offset:256 nt lds
	s_add_u32 m0, s33, 1024
	s_nop 0
	buffer_load_dwordx4 v1, s[24:27], s32 offen offset:0 nt lds
	s_add_u32 m0, s33, 2944
	s_nop 0
	buffer_load_dwordx4 v1, s[24:27], s32 offen offset:128 nt lds
	s_add_u32 m0, s33, 4864
	s_nop 0
	buffer_load_dwordx4 v1, s[24:27], s32 offen offset:256 nt lds
	s_add_u32 s32, s32, s79
	s_add_u32 m0, s33, 6144
	s_nop 0
	buffer_load_dwordx4 v0, s[24:27], s32 offen offset:0 nt lds
	s_add_u32 m0, s33, 8064
	s_nop 0
	buffer_load_dwordx4 v0, s[24:27], s32 offen offset:128 nt lds
	s_add_u32 m0, s33, 9984
	s_nop 0
	buffer_load_dwordx4 v0, s[24:27], s32 offen offset:256 nt lds
	s_add_u32 m0, s33, 7168
	s_nop 0
	buffer_load_dwordx4 v1, s[24:27], s32 offen offset:0 nt lds
	s_add_u32 m0, s33, 9088
	s_nop 0
	buffer_load_dwordx4 v1, s[24:27], s32 offen offset:128 nt lds
	s_add_u32 m0, s33, 11008
	s_nop 0
	buffer_load_dwordx4 v1, s[24:27], s32 offen offset:256 nt lds
	s_add_u32 s32, s32, 589824
	s_waitcnt vmcnt(12)
	v_readfirstlane_b32 s70, v117
	s_and_b32 s18, s35, 255
	s_add_u32 s70, s70, 0x9e377900
	s_andn2_b32 s70, s70, 255
	s_or_b32 s70, s70, s18
	s_lshr_b32 s64, s35, 8
	s_and_b32 s71, s64, 1024
	s_add_u32 s71, s71, 12566528
	s_add_u32 s64, s64, 12566528
	v_mov_b32 v117, 16
	v_xor_b32 v117, s70, v117
	s_bitcmp0_b32 s64, 11
	s_cselect_b64 exec, 1, 0
	buffer_store_dword v117, off, s[20:23], s64 sc1
	s_mov_b64 exec, -1
	s_add_u32 m0, s33, 12288
	s_nop 0
	buffer_load_dwordx4 v0, s[24:27], s32 offen offset:0 nt lds
	s_add_u32 m0, s33, 14208
	s_nop 0
	buffer_load_dwordx4 v0, s[24:27], s32 offen offset:128 nt lds
	s_add_u32 m0, s33, 16128
	s_nop 0
	buffer_load_dwordx4 v0, s[24:27], s32 offen offset:256 nt lds
	s_add_u32 m0, s33, 13312
	s_nop 0
	buffer_load_dwordx4 v1, s[24:27], s32 offen offset:0 nt lds
	s_add_u32 m0, s33, 15232
	s_nop 0
	buffer_load_dwordx4 v1, s[24:27], s32 offen offset:128 nt lds
	s_add_u32 m0, s33, 17152
	s_nop 0
	buffer_load_dwordx4 v1, s[24:27], s32 offen offset:256 nt lds
	s_add_u32 s32, s32, s79
	s_add_u32 m0, s33, 18432
	s_nop 0
	buffer_load_dwordx4 v0, s[24:27], s32 offen offset:0 nt lds
	s_add_u32 m0, s33, 20352
	s_nop 0
	buffer_load_dwordx4 v0, s[24:27], s32 offen offset:128 nt lds
	s_add_u32 m0, s33, 22272
	s_nop 0
	buffer_load_dwordx4 v0, s[24:27], s32 offen offset:256 nt lds
	s_add_u32 m0, s33, 19456
	s_nop 0
	buffer_load_dwordx4 v1, s[24:27], s32 offen offset:0 nt lds
	s_add_u32 m0, s33, 21376
	s_nop 0
	buffer_load_dwordx4 v1, s[24:27], s32 offen offset:128 nt lds
	s_add_u32 m0, s33, 23296
	s_nop 0
	buffer_load_dwordx4 v1, s[24:27], s32 offen offset:256 nt lds
	s_add_u32 s32, s32, 589824
	s_add_u32 m0, s33, 24576
	s_nop 0
	buffer_load_dwordx4 v0, s[24:27], s32 offen offset:0 nt lds
	s_add_u32 m0, s33, 26496
	s_nop 0
	buffer_load_dwordx4 v0, s[24:27], s32 offen offset:128 nt lds
	s_add_u32 m0, s33, 28416
	s_nop 0
	buffer_load_dwordx4 v0, s[24:27], s32 offen offset:256 nt lds
	s_add_u32 m0, s33, 25600
	s_nop 0
	buffer_load_dwordx4 v1, s[24:27], s32 offen offset:0 nt lds
	s_add_u32 m0, s33, 27520
	s_nop 0
	buffer_load_dwordx4 v1, s[24:27], s32 offen offset:128 nt lds
	s_add_u32 m0, s33, 29440
	s_nop 0
	buffer_load_dwordx4 v1, s[24:27], s32 offen offset:256 nt lds
	s_add_u32 s32, s32, s79
	s_add_u32 m0, s33, 30720
	s_nop 0
	buffer_load_dwordx4 v0, s[24:27], s32 offen offset:0 nt lds
	s_add_u32 m0, s33, 32640
	s_nop 0
	buffer_load_dwordx4 v0, s[24:27], s32 offen offset:128 nt lds
	s_add_u32 m0, s33, 34560
	s_nop 0
	buffer_load_dwordx4 v0, s[24:27], s32 offen offset:256 nt lds
	s_add_u32 m0, s33, 31744
	s_nop 0
	buffer_load_dwordx4 v1, s[24:27], s32 offen offset:0 nt lds
	s_add_u32 m0, s33, 33664
	s_nop 0
	buffer_load_dwordx4 v1, s[24:27], s32 offen offset:128 nt lds
	s_add_u32 m0, s33, 35584
	s_nop 0
	buffer_load_dwordx4 v1, s[24:27], s32 offen offset:256 nt lds
	s_add_u32 s32, s32, 589824
	s_waitcnt vmcnt(24)
	s_barrier
	s_cmp_ge_u32 s37, 2
	s_cbranch_scc1 .Lqk_v4
	s_cmp_eq_u32 s37, 1
	s_cbranch_scc1 .Lqk_v3
	ds_read_b64_tr_b16 a[0:1], v3 offset:0
	ds_read_b64_tr_b16 a[2:3], v3 offset:1024
	ds_read_b64_tr_b16 a[4:5], v3 offset:36864
	ds_read_b64_tr_b16 a[6:7], v3 offset:37888
	ds_read_b64_tr_b16 a[8:9], v124 offset:0
	ds_read_b64_tr_b16 a[10:11], v124 offset:1024
	ds_read_b64_tr_b16 a[12:13], v124 offset:36864
	ds_read_b64_tr_b16 a[14:15], v124 offset:37888
	ds_read_b64_tr_b16 a[16:17], v2 offset:0
	ds_read_b64_tr_b16 a[18:19], v2 offset:1024
	ds_read_b64_tr_b16 a[20:21], v2 offset:36864
	ds_read_b64_tr_b16 a[22:23], v2 offset:37888
	ds_read_b64_tr_b16 a[24:25], v125 offset:0
	ds_read_b64_tr_b16 a[26:27], v125 offset:1024
	ds_read_b64_tr_b16 a[28:29], v125 offset:36864
	ds_read_b64_tr_b16 a[30:31], v125 offset:37888
	ds_read_b64_tr_b16 a[32:33], v3 offset:2048
	ds_read_b64_tr_b16 a[34:35], v3 offset:3072
	ds_read_b64_tr_b16 a[36:37], v3 offset:38912
	ds_read_b64_tr_b16 a[38:39], v3 offset:39936
	ds_read_b64_tr_b16 a[40:41], v124 offset:2048
	ds_read_b64_tr_b16 a[42:43], v124 offset:3072
	ds_read_b64_tr_b16 a[44:45], v124 offset:38912
	ds_read_b64_tr_b16 a[46:47], v124 offset:39936
	s_branch .Lqk_v2
.Lqk_v3:
	ds_read_b64_tr_b16 v[64:65], v3 offset:0
	ds_read_b64_tr_b16 v[66:67], v3 offset:1024
	ds_read_b64_tr_b16 v[68:69], v3 offset:36864
	ds_read_b64_tr_b16 v[70:71], v3 offset:37888
	ds_read_b64_tr_b16 v[72:73], v124 offset:0
	ds_read_b64_tr_b16 v[74:75], v124 offset:1024
	ds_read_b64_tr_b16 v[76:77], v124 offset:36864
	ds_read_b64_tr_b16 v[78:79], v124 offset:37888
	ds_read_b64_tr_b16 v[80:81], v2 offset:0
	ds_read_b64_tr_b16 v[82:83], v2 offset:1024
	ds_read_b64_tr_b16 v[84:85], v2 offset:36864
	ds_read_b64_tr_b16 v[86:87], v2 offset:37888
	ds_read_b64_tr_b16 v[88:89], v125 offset:0
	ds_read_b64_tr_b16 v[90:91], v125 offset:1024
	ds_read_b64_tr_b16 v[92:93], v125 offset:36864
	ds_read_b64_tr_b16 v[94:95], v125 offset:37888
	ds_read_b64_tr_b16 v[96:97], v3 offset:2048
	ds_read_b64_tr_b16 v[98:99], v3 offset:3072
	ds_read_b64_tr_b16 v[100:101], v3 offset:38912
	ds_read_b64_tr_b16 v[102:103], v3 offset:39936
	ds_read_b64_tr_b16 v[104:105], v124 offset:2048
	ds_read_b64_tr_b16 v[106:107], v124 offset:3072
	ds_read_b64_tr_b16 v[108:109], v124 offset:38912
	ds_read_b64_tr_b16 v[110:111], v124 offset:39936
	s_branch .Lqk_v2
.Lqk_v4:
	s_cmp_eq_u32 s37, 3
	s_cbranch_scc1 .Lqk_v5
	ds_read_b64_tr_b16 a[192:193], v3 offset:0
	ds_read_b64_tr_b16 a[194:195], v3 offset:1024
	ds_read_b64_tr_b16 a[196:197], v3 offset:36864
	ds_read_b64_tr_b16 a[198:199], v3 offset:37888
	ds_read_b64_tr_b16 a[200:201], v124 offset:0
	ds_read_b64_tr_b16 a[202:203], v124 offset:1024
	ds_read_b64_tr_b16 a[204:205], v124 offset:36864
	ds_read_b64_tr_b16 a[206:207], v124 offset:37888
	ds_read_b64_tr_b16 a[208:209], v2 offset:0
	ds_read_b64_tr_b16 a[210:211], v2 offset:1024
	ds_read_b64_tr_b16 a[212:213], v2 offset:36864
	ds_read_b64_tr_b16 a[214:215], v2 offset:37888
	ds_read_b64_tr_b16 a[216:217], v125 offset:0
	ds_read_b64_tr_b16 a[218:219], v125 offset:1024
	ds_read_b64_tr_b16 a[220:221], v125 offset:36864
	ds_read_b64_tr_b16 a[222:223], v125 offset:37888
	ds_read_b64_tr_b16 a[224:225], v3 offset:2048
	ds_read_b64_tr_b16 a[226:227], v3 offset:3072
	ds_read_b64_tr_b16 a[228:229], v3 offset:38912
	ds_read_b64_tr_b16 a[230:231], v3 offset:39936
	ds_read_b64_tr_b16 a[232:233], v124 offset:2048
	ds_read_b64_tr_b16 a[234:235], v124 offset:3072
	ds_read_b64_tr_b16 a[236:237], v124 offset:38912
	ds_read_b64_tr_b16 a[238:239], v124 offset:39936
	s_branch .Lqk_v2
.Lqk_v5:
	ds_read_b64_tr_b16 v[16:17], v3 offset:0
	ds_read_b64_tr_b16 v[18:19], v3 offset:1024
	ds_read_b64_tr_b16 v[20:21], v3 offset:36864
	ds_read_b64_tr_b16 v[22:23], v3 offset:37888
	ds_read_b64_tr_b16 v[24:25], v124 offset:0
	ds_read_b64_tr_b16 v[26:27], v124 offset:1024
	ds_read_b64_tr_b16 v[28:29], v124 offset:36864
	ds_read_b64_tr_b16 v[30:31], v124 offset:37888
	ds_read_b64_tr_b16 v[32:33], v2 offset:0
	ds_read_b64_tr_b16 v[34:35], v2 offset:1024
	ds_read_b64_tr_b16 v[36:37], v2 offset:36864
	ds_read_b64_tr_b16 v[38:39], v2 offset:37888
	ds_read_b64_tr_b16 v[40:41], v125 offset:0
	ds_read_b64_tr_b16 v[42:43], v125 offset:1024
	ds_read_b64_tr_b16 v[44:45], v125 offset:36864
	ds_read_b64_tr_b16 v[46:47], v125 offset:37888
	ds_read_b64_tr_b16 v[48:49], v3 offset:2048
	ds_read_b64_tr_b16 v[50:51], v3 offset:3072
	ds_read_b64_tr_b16 v[52:53], v3 offset:38912
	ds_read_b64_tr_b16 v[54:55], v3 offset:39936
	ds_read_b64_tr_b16 v[56:57], v124 offset:2048
	ds_read_b64_tr_b16 v[58:59], v124 offset:3072
	ds_read_b64_tr_b16 v[60:61], v124 offset:38912
	ds_read_b64_tr_b16 v[62:63], v124 offset:39936
.Lqk_v2:
	s_waitcnt lgkmcnt(0)
	s_barrier
	s_add_u32 m0, s33, 0
	s_nop 0
	buffer_load_dwordx4 v0, s[24:27], s32 offen offset:0 nt lds
	s_add_u32 m0, s33, 1920
	s_nop 0
	buffer_load_dwordx4 v0, s[24:27], s32 offen offset:128 nt lds
	s_add_u32 m0, s33, 3840
	s_nop 0
	buffer_load_dwordx4 v0, s[24:27], s32 offen offset:256 nt lds
	s_add_u32 m0, s33, 1024
	s_nop 0
	buffer_load_dwordx4 v1, s[24:27], s32 offen offset:0 nt lds
	s_add_u32 m0, s33, 2944
	s_nop 0
	buffer_load_dwordx4 v1, s[24:27], s32 offen offset:128 nt lds
	s_add_u32 m0, s33, 4864
	s_nop 0
	buffer_load_dwordx4 v1, s[24:27], s32 offen offset:256 nt lds
	s_add_u32 s32, s32, s79
	s_add_u32 m0, s33, 6144
	s_nop 0
	buffer_load_dwordx4 v0, s[24:27], s32 offen offset:0 nt lds
	s_add_u32 m0, s33, 8064
	s_nop 0
	buffer_load_dwordx4 v0, s[24:27], s32 offen offset:128 nt lds
	s_add_u32 m0, s33, 9984
	s_nop 0
	buffer_load_dwordx4 v0, s[24:27], s32 offen offset:256 nt lds
	s_add_u32 m0, s33, 7168
	s_nop 0
	buffer_load_dwordx4 v1, s[24:27], s32 offen offset:0 nt lds
	s_add_u32 m0, s33, 9088
	s_nop 0
	buffer_load_dwordx4 v1, s[24:27], s32 offen offset:128 nt lds
	s_add_u32 m0, s33, 11008
	s_nop 0
	buffer_load_dwordx4 v1, s[24:27], s32 offen offset:256 nt lds
	s_add_u32 s32, s32, 589824
	s_waitcnt vmcnt(24)
	s_barrier
	s_cmp_ge_u32 s37, 2
	s_cbranch_scc1 .Lqk_v8
	s_cmp_eq_u32 s37, 1
	s_cbranch_scc1 .Lqk_v7
	ds_read_b64_tr_b16 a[48:49], v3 offset:12288
	ds_read_b64_tr_b16 a[50:51], v3 offset:13312
	ds_read_b64_tr_b16 a[52:53], v3 offset:49152
	ds_read_b64_tr_b16 a[54:55], v3 offset:50176
	ds_read_b64_tr_b16 a[56:57], v124 offset:12288
	ds_read_b64_tr_b16 a[58:59], v124 offset:13312
	ds_read_b64_tr_b16 a[60:61], v124 offset:49152
	ds_read_b64_tr_b16 a[62:63], v124 offset:50176
	ds_read_b64_tr_b16 a[64:65], v2 offset:12288
	ds_read_b64_tr_b16 a[66:67], v2 offset:13312
	ds_read_b64_tr_b16 a[68:69], v2 offset:49152
	ds_read_b64_tr_b16 a[70:71], v2 offset:50176
	ds_read_b64_tr_b16 a[72:73], v125 offset:12288
	ds_read_b64_tr_b16 a[74:75], v125 offset:13312
	ds_read_b64_tr_b16 a[76:77], v125 offset:49152
	ds_read_b64_tr_b16 a[78:79], v125 offset:50176
	ds_read_b64_tr_b16 a[80:81], v3 offset:14336
	ds_read_b64_tr_b16 a[82:83], v3 offset:15360
	ds_read_b64_tr_b16 a[84:85], v3 offset:51200
	ds_read_b64_tr_b16 a[86:87], v3 offset:52224
	ds_read_b64_tr_b16 a[88:89], v124 offset:14336
	ds_read_b64_tr_b16 a[90:91], v124 offset:15360
	ds_read_b64_tr_b16 a[92:93], v124 offset:51200
	ds_read_b64_tr_b16 a[94:95], v124 offset:52224
	s_branch .Lqk_v6
.Lqk_v7:
	ds_read_b64_tr_b16 v[160:161], v3 offset:12288
	ds_read_b64_tr_b16 v[162:163], v3 offset:13312
	ds_read_b64_tr_b16 v[164:165], v3 offset:49152
	ds_read_b64_tr_b16 v[166:167], v3 offset:50176
	ds_read_b64_tr_b16 v[168:169], v124 offset:12288
	ds_read_b64_tr_b16 v[170:171], v124 offset:13312
	ds_read_b64_tr_b16 v[172:173], v124 offset:49152
	ds_read_b64_tr_b16 v[174:175], v124 offset:50176
	ds_read_b64_tr_b16 v[176:177], v2 offset:12288
	ds_read_b64_tr_b16 v[178:179], v2 offset:13312
	ds_read_b64_tr_b16 v[180:181], v2 offset:49152
	ds_read_b64_tr_b16 v[182:183], v2 offset:50176
	ds_read_b64_tr_b16 v[184:185], v125 offset:12288
	ds_read_b64_tr_b16 v[186:187], v125 offset:13312
	ds_read_b64_tr_b16 v[188:189], v125 offset:49152
	ds_read_b64_tr_b16 v[190:191], v125 offset:50176
	ds_read_b64_tr_b16 v[192:193], v3 offset:14336
	ds_read_b64_tr_b16 v[194:195], v3 offset:15360
	ds_read_b64_tr_b16 v[196:197], v3 offset:51200
	ds_read_b64_tr_b16 v[198:199], v3 offset:52224
	ds_read_b64_tr_b16 v[200:201], v124 offset:14336
	ds_read_b64_tr_b16 v[202:203], v124 offset:15360
	ds_read_b64_tr_b16 v[204:205], v124 offset:51200
	ds_read_b64_tr_b16 v[206:207], v124 offset:52224
	s_branch .Lqk_v6
.Lqk_v8:
	s_cmp_eq_u32 s37, 3
	s_cbranch_scc1 .Lqk_v9
	ds_read_b64_tr_b16 a[240:241], v3 offset:12288
	ds_read_b64_tr_b16 a[242:243], v3 offset:13312
	ds_read_b64_tr_b16 a[244:245], v3 offset:49152
	ds_read_b64_tr_b16 a[246:247], v3 offset:50176
	ds_read_b64_tr_b16 a[248:249], v124 offset:12288
	ds_read_b64_tr_b16 a[250:251], v124 offset:13312
	ds_read_b64_tr_b16 a[252:253], v124 offset:49152
	ds_read_b64_tr_b16 a[254:255], v124 offset:50176
	ds_read_b64_tr_b16 v[128:129], v2 offset:12288
	ds_read_b64_tr_b16 v[130:131], v2 offset:13312
	ds_read_b64_tr_b16 v[132:133], v2 offset:49152
	ds_read_b64_tr_b16 v[134:135], v2 offset:50176
	ds_read_b64_tr_b16 v[136:137], v125 offset:12288
	ds_read_b64_tr_b16 v[138:139], v125 offset:13312
	ds_read_b64_tr_b16 v[140:141], v125 offset:49152
	ds_read_b64_tr_b16 v[142:143], v125 offset:50176
	ds_read_b64_tr_b16 v[144:145], v3 offset:14336
	ds_read_b64_tr_b16 v[146:147], v3 offset:15360
	ds_read_b64_tr_b16 v[148:149], v3 offset:51200
	ds_read_b64_tr_b16 v[150:151], v3 offset:52224
	ds_read_b64_tr_b16 v[152:153], v124 offset:14336
	ds_read_b64_tr_b16 v[154:155], v124 offset:15360
	ds_read_b64_tr_b16 v[156:157], v124 offset:51200
	ds_read_b64_tr_b16 v[158:159], v124 offset:52224
	s_branch .Lqk_v6
.Lqk_v9:
	ds_read_b64_tr_b16 a[144:145], v3 offset:12288
	ds_read_b64_tr_b16 a[146:147], v3 offset:13312
	ds_read_b64_tr_b16 a[148:149], v3 offset:49152
	ds_read_b64_tr_b16 a[150:151], v3 offset:50176
	ds_read_b64_tr_b16 a[152:153], v124 offset:12288
	ds_read_b64_tr_b16 a[154:155], v124 offset:13312
	ds_read_b64_tr_b16 a[156:157], v124 offset:49152
	ds_read_b64_tr_b16 a[158:159], v124 offset:50176
	ds_read_b64_tr_b16 a[160:161], v2 offset:12288
	ds_read_b64_tr_b16 a[162:163], v2 offset:13312
	ds_read_b64_tr_b16 a[164:165], v2 offset:49152
	ds_read_b64_tr_b16 a[166:167], v2 offset:50176
	ds_read_b64_tr_b16 a[168:169], v125 offset:12288
	ds_read_b64_tr_b16 a[170:171], v125 offset:13312
	ds_read_b64_tr_b16 a[172:173], v125 offset:49152
	ds_read_b64_tr_b16 a[174:175], v125 offset:50176
	ds_read_b64_tr_b16 a[176:177], v3 offset:14336
	ds_read_b64_tr_b16 a[178:179], v3 offset:15360
	ds_read_b64_tr_b16 a[180:181], v3 offset:51200
	ds_read_b64_tr_b16 a[182:183], v3 offset:52224
	ds_read_b64_tr_b16 a[184:185], v124 offset:14336
	ds_read_b64_tr_b16 a[186:187], v124 offset:15360
	ds_read_b64_tr_b16 a[188:189], v124 offset:51200
	ds_read_b64_tr_b16 a[190:191], v124 offset:52224
.Lqk_v6:
	s_waitcnt lgkmcnt(0)
	s_barrier
	s_add_u32 m0, s33, 12288
	s_nop 0
	buffer_load_dwordx4 v0, s[24:27], s32 offen offset:0 nt lds
	s_add_u32 m0, s33, 14208
	s_nop 0
	buffer_load_dwordx4 v0, s[24:27], s32 offen offset:128 nt lds
	s_add_u32 m0, s33, 16128
	s_nop 0
	buffer_load_dwordx4 v0, s[24:27], s32 offen offset:256 nt lds
	s_add_u32 m0, s33, 13312
	s_nop 0
	buffer_load_dwordx4 v1, s[24:27], s32 offen offset:0 nt lds
	s_add_u32 m0, s33, 15232
	s_nop 0
	buffer_load_dwordx4 v1, s[24:27], s32 offen offset:128 nt lds
	s_add_u32 m0, s33, 17152
	s_nop 0
	buffer_load_dwordx4 v1, s[24:27], s32 offen offset:256 nt lds
	s_add_u32 s32, s32, s79
	s_add_u32 m0, s33, 18432
	s_nop 0
	buffer_load_dwordx4 v0, s[24:27], s32 offen offset:0 nt lds
	s_add_u32 m0, s33, 20352
	s_nop 0
	buffer_load_dwordx4 v0, s[24:27], s32 offen offset:128 nt lds
	s_add_u32 m0, s33, 22272
	s_nop 0
	buffer_load_dwordx4 v0, s[24:27], s32 offen offset:256 nt lds
	s_add_u32 m0, s33, 19456
	s_nop 0
	buffer_load_dwordx4 v1, s[24:27], s32 offen offset:0 nt lds
	s_add_u32 m0, s33, 21376
	s_nop 0
	buffer_load_dwordx4 v1, s[24:27], s32 offen offset:128 nt lds
	s_add_u32 m0, s33, 23296
	s_nop 0
	buffer_load_dwordx4 v1, s[24:27], s32 offen offset:256 nt lds
	s_add_u32 s32, s32, 589824
	s_waitcnt vmcnt(24)
	s_barrier
	s_cmp_ge_u32 s37, 2
	s_cbranch_scc1 .Lqk_v12
	s_cmp_eq_u32 s37, 1
	s_cbranch_scc1 .Lqk_v11
	ds_read_b64_tr_b16 v[16:17], v3 offset:24576
	ds_read_b64_tr_b16 v[18:19], v3 offset:25600
	ds_read_b64_tr_b16 v[20:21], v3 offset:61440
	ds_read_b64_tr_b16 v[22:23], v3 offset:62464
	ds_read_b64_tr_b16 v[24:25], v124 offset:24576
	ds_read_b64_tr_b16 v[26:27], v124 offset:25600
	ds_read_b64_tr_b16 v[28:29], v124 offset:61440
	ds_read_b64_tr_b16 v[30:31], v124 offset:62464
	ds_read_b64_tr_b16 v[32:33], v2 offset:24576
	ds_read_b64_tr_b16 v[34:35], v2 offset:25600
	ds_read_b64_tr_b16 v[36:37], v2 offset:61440
	ds_read_b64_tr_b16 v[38:39], v2 offset:62464
	ds_read_b64_tr_b16 v[40:41], v125 offset:24576
	ds_read_b64_tr_b16 v[42:43], v125 offset:25600
	ds_read_b64_tr_b16 v[44:45], v125 offset:61440
	ds_read_b64_tr_b16 v[46:47], v125 offset:62464
	ds_read_b64_tr_b16 v[48:49], v3 offset:26624
	ds_read_b64_tr_b16 v[50:51], v3 offset:27648
	ds_read_b64_tr_b16 v[52:53], v3 offset:63488
	ds_read_b64_tr_b16 v[54:55], v3 offset:64512
	ds_read_b64_tr_b16 v[56:57], v124 offset:26624
	ds_read_b64_tr_b16 v[58:59], v124 offset:27648
	ds_read_b64_tr_b16 v[60:61], v124 offset:63488
	ds_read_b64_tr_b16 v[62:63], v124 offset:64512
	s_branch .Lqk_v10
.Lqk_v11:
	ds_read_b64_tr_b16 a[0:1], v3 offset:24576
	ds_read_b64_tr_b16 a[2:3], v3 offset:25600
	ds_read_b64_tr_b16 a[4:5], v3 offset:61440
	ds_read_b64_tr_b16 a[6:7], v3 offset:62464
	ds_read_b64_tr_b16 a[8:9], v124 offset:24576
	ds_read_b64_tr_b16 a[10:11], v124 offset:25600
	ds_read_b64_tr_b16 a[12:13], v124 offset:61440
	ds_read_b64_tr_b16 a[14:15], v124 offset:62464
	ds_read_b64_tr_b16 a[16:17], v2 offset:24576
	ds_read_b64_tr_b16 a[18:19], v2 offset:25600
	ds_read_b64_tr_b16 a[20:21], v2 offset:61440
	ds_read_b64_tr_b16 a[22:23], v2 offset:62464
	ds_read_b64_tr_b16 a[24:25], v125 offset:24576
	ds_read_b64_tr_b16 a[26:27], v125 offset:25600
	ds_read_b64_tr_b16 a[28:29], v125 offset:61440
	ds_read_b64_tr_b16 a[30:31], v125 offset:62464
	ds_read_b64_tr_b16 a[32:33], v3 offset:26624
	ds_read_b64_tr_b16 a[34:35], v3 offset:27648
	ds_read_b64_tr_b16 a[36:37], v3 offset:63488
	ds_read_b64_tr_b16 a[38:39], v3 offset:64512
	ds_read_b64_tr_b16 a[40:41], v124 offset:26624
	ds_read_b64_tr_b16 a[42:43], v124 offset:27648
	ds_read_b64_tr_b16 a[44:45], v124 offset:63488
	ds_read_b64_tr_b16 a[46:47], v124 offset:64512
	s_branch .Lqk_v10
.Lqk_v12:
	s_cmp_eq_u32 s37, 3
	s_cbranch_scc1 .Lqk_v13
	ds_read_b64_tr_b16 v[64:65], v3 offset:24576
	ds_read_b64_tr_b16 v[66:67], v3 offset:25600
	ds_read_b64_tr_b16 v[68:69], v3 offset:61440
	ds_read_b64_tr_b16 v[70:71], v3 offset:62464
	ds_read_b64_tr_b16 v[72:73], v124 offset:24576
	ds_read_b64_tr_b16 v[74:75], v124 offset:25600
	ds_read_b64_tr_b16 v[76:77], v124 offset:61440
	ds_read_b64_tr_b16 v[78:79], v124 offset:62464
	ds_read_b64_tr_b16 v[80:81], v2 offset:24576
	ds_read_b64_tr_b16 v[82:83], v2 offset:25600
	ds_read_b64_tr_b16 v[84:85], v2 offset:61440
	ds_read_b64_tr_b16 v[86:87], v2 offset:62464
	ds_read_b64_tr_b16 v[88:89], v125 offset:24576
	ds_read_b64_tr_b16 v[90:91], v125 offset:25600
	ds_read_b64_tr_b16 v[92:93], v125 offset:61440
	ds_read_b64_tr_b16 v[94:95], v125 offset:62464
	ds_read_b64_tr_b16 v[96:97], v3 offset:26624
	ds_read_b64_tr_b16 v[98:99], v3 offset:27648
	ds_read_b64_tr_b16 v[100:101], v3 offset:63488
	ds_read_b64_tr_b16 v[102:103], v3 offset:64512
	ds_read_b64_tr_b16 v[104:105], v124 offset:26624
	ds_read_b64_tr_b16 v[106:107], v124 offset:27648
	ds_read_b64_tr_b16 v[108:109], v124 offset:63488
	ds_read_b64_tr_b16 v[110:111], v124 offset:64512
	s_branch .Lqk_v10
.Lqk_v13:
	ds_read_b64_tr_b16 a[192:193], v3 offset:24576
	ds_read_b64_tr_b16 a[194:195], v3 offset:25600
	ds_read_b64_tr_b16 a[196:197], v3 offset:61440
	ds_read_b64_tr_b16 a[198:199], v3 offset:62464
	ds_read_b64_tr_b16 a[200:201], v124 offset:24576
	ds_read_b64_tr_b16 a[202:203], v124 offset:25600
	ds_read_b64_tr_b16 a[204:205], v124 offset:61440
	ds_read_b64_tr_b16 a[206:207], v124 offset:62464
	ds_read_b64_tr_b16 a[208:209], v2 offset:24576
	ds_read_b64_tr_b16 a[210:211], v2 offset:25600
	ds_read_b64_tr_b16 a[212:213], v2 offset:61440
	ds_read_b64_tr_b16 a[214:215], v2 offset:62464
	ds_read_b64_tr_b16 a[216:217], v125 offset:24576
	ds_read_b64_tr_b16 a[218:219], v125 offset:25600
	ds_read_b64_tr_b16 a[220:221], v125 offset:61440
	ds_read_b64_tr_b16 a[222:223], v125 offset:62464
	ds_read_b64_tr_b16 a[224:225], v3 offset:26624
	ds_read_b64_tr_b16 a[226:227], v3 offset:27648
	ds_read_b64_tr_b16 a[228:229], v3 offset:63488
	ds_read_b64_tr_b16 a[230:231], v3 offset:64512
	ds_read_b64_tr_b16 a[232:233], v124 offset:26624
	ds_read_b64_tr_b16 a[234:235], v124 offset:27648
	ds_read_b64_tr_b16 a[236:237], v124 offset:63488
	ds_read_b64_tr_b16 a[238:239], v124 offset:64512
.Lqk_v10:
	s_waitcnt lgkmcnt(0)
	s_barrier
	s_add_u32 m0, s33, 24576
	s_nop 0
	buffer_load_dwordx4 v0, s[24:27], s32 offen offset:0 nt lds
	s_add_u32 m0, s33, 26496
	s_nop 0
	buffer_load_dwordx4 v0, s[24:27], s32 offen offset:128 nt lds
	s_add_u32 m0, s33, 28416
	s_nop 0
	buffer_load_dwordx4 v0, s[24:27], s32 offen offset:256 nt lds
	s_add_u32 m0, s33, 25600
	s_nop 0
	buffer_load_dwordx4 v1, s[24:27], s32 offen offset:0 nt lds
	s_add_u32 m0, s33, 27520
	s_nop 0
	buffer_load_dwordx4 v1, s[24:27], s32 offen offset:128 nt lds
	s_add_u32 m0, s33, 29440
	s_nop 0
	buffer_load_dwordx4 v1, s[24:27], s32 offen offset:256 nt lds
	s_add_u32 s32, s32, s79
	s_add_u32 m0, s33, 30720
	s_nop 0
	buffer_load_dwordx4 v0, s[24:27], s32 offen offset:0 nt lds
	s_add_u32 m0, s33, 32640
	s_nop 0
	buffer_load_dwordx4 v0, s[24:27], s32 offen offset:128 nt lds
	s_add_u32 m0, s33, 34560
	s_nop 0
	buffer_load_dwordx4 v0, s[24:27], s32 offen offset:256 nt lds
	s_add_u32 m0, s33, 31744
	s_nop 0
	buffer_load_dwordx4 v1, s[24:27], s32 offen offset:0 nt lds
	s_add_u32 m0, s33, 33664
	s_nop 0
	buffer_load_dwordx4 v1, s[24:27], s32 offen offset:128 nt lds
	s_add_u32 m0, s33, 35584
	s_nop 0
	buffer_load_dwordx4 v1, s[24:27], s32 offen offset:256 nt lds
	s_add_u32 s32, s32, 589824
	buffer_load_dwordx4 v[112:115], v13, s[20:23], s71 offen offset:0 sc1
	buffer_load_dword v116, off, s[20:23], s71 offset:2048 sc1
	s_waitcnt vmcnt(26)
	s_barrier
	s_cmp_ge_u32 s37, 2
	s_cbranch_scc1 .Lqk_v16
	s_cmp_eq_u32 s37, 1
	s_cbranch_scc1 .Lqk_v15
	ds_read_b64_tr_b16 a[144:145], v3 offset:0
	ds_read_b64_tr_b16 a[146:147], v3 offset:1024
	ds_read_b64_tr_b16 a[148:149], v3 offset:36864
	ds_read_b64_tr_b16 a[150:151], v3 offset:37888
	ds_read_b64_tr_b16 a[152:153], v124 offset:0
	ds_read_b64_tr_b16 a[154:155], v124 offset:1024
	ds_read_b64_tr_b16 a[156:157], v124 offset:36864
	ds_read_b64_tr_b16 a[158:159], v124 offset:37888
	ds_read_b64_tr_b16 a[160:161], v2 offset:0
	ds_read_b64_tr_b16 a[162:163], v2 offset:1024
	ds_read_b64_tr_b16 a[164:165], v2 offset:36864
	ds_read_b64_tr_b16 a[166:167], v2 offset:37888
	ds_read_b64_tr_b16 a[168:169], v125 offset:0
	ds_read_b64_tr_b16 a[170:171], v125 offset:1024
	ds_read_b64_tr_b16 a[172:173], v125 offset:36864
	ds_read_b64_tr_b16 a[174:175], v125 offset:37888
	ds_read_b64_tr_b16 a[176:177], v3 offset:2048
	ds_read_b64_tr_b16 a[178:179], v3 offset:3072
	ds_read_b64_tr_b16 a[180:181], v3 offset:38912
	ds_read_b64_tr_b16 a[182:183], v3 offset:39936
	ds_read_b64_tr_b16 a[184:185], v124 offset:2048
	ds_read_b64_tr_b16 a[186:187], v124 offset:3072
	ds_read_b64_tr_b16 a[188:189], v124 offset:38912
	ds_read_b64_tr_b16 a[190:191], v124 offset:39936
	s_branch .Lqk_v14
.Lqk_v15:
	ds_read_b64_tr_b16 a[48:49], v3 offset:0
	ds_read_b64_tr_b16 a[50:51], v3 offset:1024
	ds_read_b64_tr_b16 a[52:53], v3 offset:36864
	ds_read_b64_tr_b16 a[54:55], v3 offset:37888
	ds_read_b64_tr_b16 a[56:57], v124 offset:0
	ds_read_b64_tr_b16 a[58:59], v124 offset:1024
	ds_read_b64_tr_b16 a[60:61], v124 offset:36864
	ds_read_b64_tr_b16 a[62:63], v124 offset:37888
	ds_read_b64_tr_b16 a[64:65], v2 offset:0
	ds_read_b64_tr_b16 a[66:67], v2 offset:1024
	ds_read_b64_tr_b16 a[68:69], v2 offset:36864
	ds_read_b64_tr_b16 a[70:71], v2 offset:37888
	ds_read_b64_tr_b16 a[72:73], v125 offset:0
	ds_read_b64_tr_b16 a[74:75], v125 offset:1024
	ds_read_b64_tr_b16 a[76:77], v125 offset:36864
	ds_read_b64_tr_b16 a[78:79], v125 offset:37888
	ds_read_b64_tr_b16 a[80:81], v3 offset:2048
	ds_read_b64_tr_b16 a[82:83], v3 offset:3072
	ds_read_b64_tr_b16 a[84:85], v3 offset:38912
	ds_read_b64_tr_b16 a[86:87], v3 offset:39936
	ds_read_b64_tr_b16 a[88:89], v124 offset:2048
	ds_read_b64_tr_b16 a[90:91], v124 offset:3072
	ds_read_b64_tr_b16 a[92:93], v124 offset:38912
	ds_read_b64_tr_b16 a[94:95], v124 offset:39936
	s_branch .Lqk_v14
.Lqk_v16:
	s_cmp_eq_u32 s37, 3
	s_cbranch_scc1 .Lqk_v17
	ds_read_b64_tr_b16 v[160:161], v3 offset:0
	ds_read_b64_tr_b16 v[162:163], v3 offset:1024
	ds_read_b64_tr_b16 v[164:165], v3 offset:36864
	ds_read_b64_tr_b16 v[166:167], v3 offset:37888
	ds_read_b64_tr_b16 v[168:169], v124 offset:0
	ds_read_b64_tr_b16 v[170:171], v124 offset:1024
	ds_read_b64_tr_b16 v[172:173], v124 offset:36864
	ds_read_b64_tr_b16 v[174:175], v124 offset:37888
	ds_read_b64_tr_b16 v[176:177], v2 offset:0
	ds_read_b64_tr_b16 v[178:179], v2 offset:1024
	ds_read_b64_tr_b16 v[180:181], v2 offset:36864
	ds_read_b64_tr_b16 v[182:183], v2 offset:37888
	ds_read_b64_tr_b16 v[184:185], v125 offset:0
	ds_read_b64_tr_b16 v[186:187], v125 offset:1024
	ds_read_b64_tr_b16 v[188:189], v125 offset:36864
	ds_read_b64_tr_b16 v[190:191], v125 offset:37888
	ds_read_b64_tr_b16 v[192:193], v3 offset:2048
	ds_read_b64_tr_b16 v[194:195], v3 offset:3072
	ds_read_b64_tr_b16 v[196:197], v3 offset:38912
	ds_read_b64_tr_b16 v[198:199], v3 offset:39936
	ds_read_b64_tr_b16 v[200:201], v124 offset:2048
	ds_read_b64_tr_b16 v[202:203], v124 offset:3072
	ds_read_b64_tr_b16 v[204:205], v124 offset:38912
	ds_read_b64_tr_b16 v[206:207], v124 offset:39936
	s_branch .Lqk_v14
.Lqk_v17:
	ds_read_b64_tr_b16 a[240:241], v3 offset:0
	ds_read_b64_tr_b16 a[242:243], v3 offset:1024
	ds_read_b64_tr_b16 a[244:245], v3 offset:36864
	ds_read_b64_tr_b16 a[246:247], v3 offset:37888
	ds_read_b64_tr_b16 a[248:249], v124 offset:0
	ds_read_b64_tr_b16 a[250:251], v124 offset:1024
	ds_read_b64_tr_b16 a[252:253], v124 offset:36864
	ds_read_b64_tr_b16 a[254:255], v124 offset:37888
	ds_read_b64_tr_b16 v[128:129], v2 offset:0
	ds_read_b64_tr_b16 v[130:131], v2 offset:1024
	ds_read_b64_tr_b16 v[132:133], v2 offset:36864
	ds_read_b64_tr_b16 v[134:135], v2 offset:37888
	ds_read_b64_tr_b16 v[136:137], v125 offset:0
	ds_read_b64_tr_b16 v[138:139], v125 offset:1024
	ds_read_b64_tr_b16 v[140:141], v125 offset:36864
	ds_read_b64_tr_b16 v[142:143], v125 offset:37888
	ds_read_b64_tr_b16 v[144:145], v3 offset:2048
	ds_read_b64_tr_b16 v[146:147], v3 offset:3072
	ds_read_b64_tr_b16 v[148:149], v3 offset:38912
	ds_read_b64_tr_b16 v[150:151], v3 offset:39936
	ds_read_b64_tr_b16 v[152:153], v124 offset:2048
	ds_read_b64_tr_b16 v[154:155], v124 offset:3072
	ds_read_b64_tr_b16 v[156:157], v124 offset:38912
	ds_read_b64_tr_b16 v[158:159], v124 offset:39936
.Lqk_v14:
	s_waitcnt lgkmcnt(0)
	s_barrier
	s_add_u32 m0, s33, 0
	s_nop 0
	buffer_load_dwordx4 v0, s[24:27], s32 offen offset:0 nt lds
	s_add_u32 m0, s33, 1920
	s_nop 0
	buffer_load_dwordx4 v0, s[24:27], s32 offen offset:128 nt lds
	s_add_u32 m0, s33, 3840
	s_nop 0
	buffer_load_dwordx4 v0, s[24:27], s32 offen offset:256 nt lds
	s_add_u32 m0, s33, 1024
	s_nop 0
	buffer_load_dwordx4 v1, s[24:27], s32 offen offset:0 nt lds
	s_add_u32 m0, s33, 2944
	s_nop 0
	buffer_load_dwordx4 v1, s[24:27], s32 offen offset:128 nt lds
	s_add_u32 m0, s33, 4864
	s_nop 0
	buffer_load_dwordx4 v1, s[24:27], s32 offen offset:256 nt lds
	s_add_u32 s32, s32, s79
	s_add_u32 m0, s33, 6144
	s_nop 0
	buffer_load_dwordx4 v0, s[24:27], s32 offen offset:0 nt lds
	s_add_u32 m0, s33, 8064
	s_nop 0
	buffer_load_dwordx4 v0, s[24:27], s32 offen offset:128 nt lds
	s_add_u32 m0, s33, 9984
	s_nop 0
	buffer_load_dwordx4 v0, s[24:27], s32 offen offset:256 nt lds
	s_add_u32 m0, s33, 7168
	s_nop 0
	buffer_load_dwordx4 v1, s[24:27], s32 offen offset:0 nt lds
	s_add_u32 m0, s33, 9088
	s_nop 0
	buffer_load_dwordx4 v1, s[24:27], s32 offen offset:128 nt lds
	s_add_u32 m0, s33, 11008
	s_nop 0
	buffer_load_dwordx4 v1, s[24:27], s32 offen offset:256 nt lds
	s_add_u32 s32, s32, 589824
	s_waitcnt vmcnt(26)
	s_barrier
	s_cmp_ge_u32 s37, 2
	s_cbranch_scc1 .Lqk_v20
	s_cmp_eq_u32 s37, 1
	s_cbranch_scc1 .Lqk_v19
	ds_read_b64_tr_b16 a[192:193], v3 offset:12288
	ds_read_b64_tr_b16 a[194:195], v3 offset:13312
	ds_read_b64_tr_b16 a[196:197], v3 offset:49152
	ds_read_b64_tr_b16 a[198:199], v3 offset:50176
	ds_read_b64_tr_b16 a[200:201], v124 offset:12288
	ds_read_b64_tr_b16 a[202:203], v124 offset:13312
	ds_read_b64_tr_b16 a[204:205], v124 offset:49152
	ds_read_b64_tr_b16 a[206:207], v124 offset:50176
	ds_read_b64_tr_b16 a[208:209], v2 offset:12288
	ds_read_b64_tr_b16 a[210:211], v2 offset:13312
	ds_read_b64_tr_b16 a[212:213], v2 offset:49152
	ds_read_b64_tr_b16 a[214:215], v2 offset:50176
	ds_read_b64_tr_b16 a[216:217], v125 offset:12288
	ds_read_b64_tr_b16 a[218:219], v125 offset:13312
	ds_read_b64_tr_b16 a[220:221], v125 offset:49152
	ds_read_b64_tr_b16 a[222:223], v125 offset:50176
	ds_read_b64_tr_b16 a[224:225], v3 offset:14336
	ds_read_b64_tr_b16 a[226:227], v3 offset:15360
	ds_read_b64_tr_b16 a[228:229], v3 offset:51200
	ds_read_b64_tr_b16 a[230:231], v3 offset:52224
	ds_read_b64_tr_b16 a[232:233], v124 offset:14336
	ds_read_b64_tr_b16 a[234:235], v124 offset:15360
	ds_read_b64_tr_b16 a[236:237], v124 offset:51200
	ds_read_b64_tr_b16 a[238:239], v124 offset:52224
	s_branch .Lqk_v18
.Lqk_v19:
	ds_read_b64_tr_b16 v[16:17], v3 offset:12288
	ds_read_b64_tr_b16 v[18:19], v3 offset:13312
	ds_read_b64_tr_b16 v[20:21], v3 offset:49152
	ds_read_b64_tr_b16 v[22:23], v3 offset:50176
	ds_read_b64_tr_b16 v[24:25], v124 offset:12288
	ds_read_b64_tr_b16 v[26:27], v124 offset:13312
	ds_read_b64_tr_b16 v[28:29], v124 offset:49152
	ds_read_b64_tr_b16 v[30:31], v124 offset:50176
	ds_read_b64_tr_b16 v[32:33], v2 offset:12288
	ds_read_b64_tr_b16 v[34:35], v2 offset:13312
	ds_read_b64_tr_b16 v[36:37], v2 offset:49152
	ds_read_b64_tr_b16 v[38:39], v2 offset:50176
	ds_read_b64_tr_b16 v[40:41], v125 offset:12288
	ds_read_b64_tr_b16 v[42:43], v125 offset:13312
	ds_read_b64_tr_b16 v[44:45], v125 offset:49152
	ds_read_b64_tr_b16 v[46:47], v125 offset:50176
	ds_read_b64_tr_b16 v[48:49], v3 offset:14336
	ds_read_b64_tr_b16 v[50:51], v3 offset:15360
	ds_read_b64_tr_b16 v[52:53], v3 offset:51200
	ds_read_b64_tr_b16 v[54:55], v3 offset:52224
	ds_read_b64_tr_b16 v[56:57], v124 offset:14336
	ds_read_b64_tr_b16 v[58:59], v124 offset:15360
	ds_read_b64_tr_b16 v[60:61], v124 offset:51200
	ds_read_b64_tr_b16 v[62:63], v124 offset:52224
	s_branch .Lqk_v18
.Lqk_v20:
	s_cmp_eq_u32 s37, 3
	s_cbranch_scc1 .Lqk_v21
	ds_read_b64_tr_b16 a[0:1], v3 offset:12288
	ds_read_b64_tr_b16 a[2:3], v3 offset:13312
	ds_read_b64_tr_b16 a[4:5], v3 offset:49152
	ds_read_b64_tr_b16 a[6:7], v3 offset:50176
	ds_read_b64_tr_b16 a[8:9], v124 offset:12288
	ds_read_b64_tr_b16 a[10:11], v124 offset:13312
	ds_read_b64_tr_b16 a[12:13], v124 offset:49152
	ds_read_b64_tr_b16 a[14:15], v124 offset:50176
	ds_read_b64_tr_b16 a[16:17], v2 offset:12288
	ds_read_b64_tr_b16 a[18:19], v2 offset:13312
	ds_read_b64_tr_b16 a[20:21], v2 offset:49152
	ds_read_b64_tr_b16 a[22:23], v2 offset:50176
	ds_read_b64_tr_b16 a[24:25], v125 offset:12288
	ds_read_b64_tr_b16 a[26:27], v125 offset:13312
	ds_read_b64_tr_b16 a[28:29], v125 offset:49152
	ds_read_b64_tr_b16 a[30:31], v125 offset:50176
	ds_read_b64_tr_b16 a[32:33], v3 offset:14336
	ds_read_b64_tr_b16 a[34:35], v3 offset:15360
	ds_read_b64_tr_b16 a[36:37], v3 offset:51200
	ds_read_b64_tr_b16 a[38:39], v3 offset:52224
	ds_read_b64_tr_b16 a[40:41], v124 offset:14336
	ds_read_b64_tr_b16 a[42:43], v124 offset:15360
	ds_read_b64_tr_b16 a[44:45], v124 offset:51200
	ds_read_b64_tr_b16 a[46:47], v124 offset:52224
	s_branch .Lqk_v18
.Lqk_v21:
	ds_read_b64_tr_b16 v[64:65], v3 offset:12288
	ds_read_b64_tr_b16 v[66:67], v3 offset:13312
	ds_read_b64_tr_b16 v[68:69], v3 offset:49152
	ds_read_b64_tr_b16 v[70:71], v3 offset:50176
	ds_read_b64_tr_b16 v[72:73], v124 offset:12288
	ds_read_b64_tr_b16 v[74:75], v124 offset:13312
	ds_read_b64_tr_b16 v[76:77], v124 offset:49152
	ds_read_b64_tr_b16 v[78:79], v124 offset:50176
	ds_read_b64_tr_b16 v[80:81], v2 offset:12288
	ds_read_b64_tr_b16 v[82:83], v2 offset:13312
	ds_read_b64_tr_b16 v[84:85], v2 offset:49152
	ds_read_b64_tr_b16 v[86:87], v2 offset:50176
	ds_read_b64_tr_b16 v[88:89], v125 offset:12288
	ds_read_b64_tr_b16 v[90:91], v125 offset:13312
	ds_read_b64_tr_b16 v[92:93], v125 offset:49152
	ds_read_b64_tr_b16 v[94:95], v125 offset:50176
	ds_read_b64_tr_b16 v[96:97], v3 offset:14336
	ds_read_b64_tr_b16 v[98:99], v3 offset:15360
	ds_read_b64_tr_b16 v[100:101], v3 offset:51200
	ds_read_b64_tr_b16 v[102:103], v3 offset:52224
	ds_read_b64_tr_b16 v[104:105], v124 offset:14336
	ds_read_b64_tr_b16 v[106:107], v124 offset:15360
	ds_read_b64_tr_b16 v[108:109], v124 offset:51200
	ds_read_b64_tr_b16 v[110:111], v124 offset:52224
.Lqk_v18:
	s_waitcnt lgkmcnt(0)
	s_barrier
	s_add_u32 m0, s33, 12288
	s_nop 0
	buffer_load_dwordx4 v0, s[24:27], s32 offen offset:0 nt lds
	s_add_u32 m0, s33, 14208
	s_nop 0
	buffer_load_dwordx4 v0, s[24:27], s32 offen offset:128 nt lds
	s_add_u32 m0, s33, 16128
	s_nop 0
	buffer_load_dwordx4 v0, s[24:27], s32 offen offset:256 nt lds
	s_add_u32 m0, s33, 13312
	s_nop 0
	buffer_load_dwordx4 v1, s[24:27], s32 offen offset:0 nt lds
	s_add_u32 m0, s33, 15232
	s_nop 0
	buffer_load_dwordx4 v1, s[24:27], s32 offen offset:128 nt lds
	s_add_u32 m0, s33, 17152
	s_nop 0
	buffer_load_dwordx4 v1, s[24:27], s32 offen offset:256 nt lds
	s_add_u32 s32, s32, s79
	s_add_u32 m0, s33, 18432
	s_nop 0
	buffer_load_dwordx4 v0, s[24:27], s32 offen offset:0 nt lds
	s_add_u32 m0, s33, 20352
	s_nop 0
	buffer_load_dwordx4 v0, s[24:27], s32 offen offset:128 nt lds
	s_add_u32 m0, s33, 22272
	s_nop 0
	buffer_load_dwordx4 v0, s[24:27], s32 offen offset:256 nt lds
	s_add_u32 m0, s33, 19456
	s_nop 0
	buffer_load_dwordx4 v1, s[24:27], s32 offen offset:0 nt lds
	s_add_u32 m0, s33, 21376
	s_nop 0
	buffer_load_dwordx4 v1, s[24:27], s32 offen offset:128 nt lds
	s_add_u32 m0, s33, 23296
	s_nop 0
	buffer_load_dwordx4 v1, s[24:27], s32 offen offset:256 nt lds
	s_add_u32 s32, s32, 589824
	s_waitcnt vmcnt(26)
	s_barrier
	s_cmp_ge_u32 s37, 2
	s_cbranch_scc1 .Lqk_v24
	s_cmp_eq_u32 s37, 1
	s_cbranch_scc1 .Lqk_v23
	ds_read_b64_tr_b16 a[240:241], v3 offset:24576
	ds_read_b64_tr_b16 a[242:243], v3 offset:25600
	ds_read_b64_tr_b16 a[244:245], v3 offset:61440
	ds_read_b64_tr_b16 a[246:247], v3 offset:62464
	ds_read_b64_tr_b16 a[248:249], v124 offset:24576
	ds_read_b64_tr_b16 a[250:251], v124 offset:25600
	ds_read_b64_tr_b16 a[252:253], v124 offset:61440
	ds_read_b64_tr_b16 a[254:255], v124 offset:62464
	ds_read_b64_tr_b16 v[128:129], v2 offset:24576
	ds_read_b64_tr_b16 v[130:131], v2 offset:25600
	ds_read_b64_tr_b16 v[132:133], v2 offset:61440
	ds_read_b64_tr_b16 v[134:135], v2 offset:62464
	ds_read_b64_tr_b16 v[136:137], v125 offset:24576
	ds_read_b64_tr_b16 v[138:139], v125 offset:25600
	ds_read_b64_tr_b16 v[140:141], v125 offset:61440
	ds_read_b64_tr_b16 v[142:143], v125 offset:62464
	ds_read_b64_tr_b16 v[144:145], v3 offset:26624
	ds_read_b64_tr_b16 v[146:147], v3 offset:27648
	ds_read_b64_tr_b16 v[148:149], v3 offset:63488
	ds_read_b64_tr_b16 v[150:151], v3 offset:64512
	ds_read_b64_tr_b16 v[152:153], v124 offset:26624
	ds_read_b64_tr_b16 v[154:155], v124 offset:27648
	ds_read_b64_tr_b16 v[156:157], v124 offset:63488
	ds_read_b64_tr_b16 v[158:159], v124 offset:64512
	s_branch .Lqk_v22
.Lqk_v23:
	ds_read_b64_tr_b16 a[144:145], v3 offset:24576
	ds_read_b64_tr_b16 a[146:147], v3 offset:25600
	ds_read_b64_tr_b16 a[148:149], v3 offset:61440
	ds_read_b64_tr_b16 a[150:151], v3 offset:62464
	ds_read_b64_tr_b16 a[152:153], v124 offset:24576
	ds_read_b64_tr_b16 a[154:155], v124 offset:25600
	ds_read_b64_tr_b16 a[156:157], v124 offset:61440
	ds_read_b64_tr_b16 a[158:159], v124 offset:62464
	ds_read_b64_tr_b16 a[160:161], v2 offset:24576
	ds_read_b64_tr_b16 a[162:163], v2 offset:25600
	ds_read_b64_tr_b16 a[164:165], v2 offset:61440
	ds_read_b64_tr_b16 a[166:167], v2 offset:62464
	ds_read_b64_tr_b16 a[168:169], v125 offset:24576
	ds_read_b64_tr_b16 a[170:171], v125 offset:25600
	ds_read_b64_tr_b16 a[172:173], v125 offset:61440
	ds_read_b64_tr_b16 a[174:175], v125 offset:62464
	ds_read_b64_tr_b16 a[176:177], v3 offset:26624
	ds_read_b64_tr_b16 a[178:179], v3 offset:27648
	ds_read_b64_tr_b16 a[180:181], v3 offset:63488
	ds_read_b64_tr_b16 a[182:183], v3 offset:64512
	ds_read_b64_tr_b16 a[184:185], v124 offset:26624
	ds_read_b64_tr_b16 a[186:187], v124 offset:27648
	ds_read_b64_tr_b16 a[188:189], v124 offset:63488
	ds_read_b64_tr_b16 a[190:191], v124 offset:64512
	s_branch .Lqk_v22
.Lqk_v24:
	s_cmp_eq_u32 s37, 3
	s_cbranch_scc1 .Lqk_v25
	ds_read_b64_tr_b16 a[48:49], v3 offset:24576
	ds_read_b64_tr_b16 a[50:51], v3 offset:25600
	ds_read_b64_tr_b16 a[52:53], v3 offset:61440
	ds_read_b64_tr_b16 a[54:55], v3 offset:62464
	ds_read_b64_tr_b16 a[56:57], v124 offset:24576
	ds_read_b64_tr_b16 a[58:59], v124 offset:25600
	ds_read_b64_tr_b16 a[60:61], v124 offset:61440
	ds_read_b64_tr_b16 a[62:63], v124 offset:62464
	ds_read_b64_tr_b16 a[64:65], v2 offset:24576
	ds_read_b64_tr_b16 a[66:67], v2 offset:25600
	ds_read_b64_tr_b16 a[68:69], v2 offset:61440
	ds_read_b64_tr_b16 a[70:71], v2 offset:62464
	ds_read_b64_tr_b16 a[72:73], v125 offset:24576
	ds_read_b64_tr_b16 a[74:75], v125 offset:25600
	ds_read_b64_tr_b16 a[76:77], v125 offset:61440
	ds_read_b64_tr_b16 a[78:79], v125 offset:62464
	ds_read_b64_tr_b16 a[80:81], v3 offset:26624
	ds_read_b64_tr_b16 a[82:83], v3 offset:27648
	ds_read_b64_tr_b16 a[84:85], v3 offset:63488
	ds_read_b64_tr_b16 a[86:87], v3 offset:64512
	ds_read_b64_tr_b16 a[88:89], v124 offset:26624
	ds_read_b64_tr_b16 a[90:91], v124 offset:27648
	ds_read_b64_tr_b16 a[92:93], v124 offset:63488
	ds_read_b64_tr_b16 a[94:95], v124 offset:64512
	s_branch .Lqk_v22
.Lqk_v25:
	ds_read_b64_tr_b16 v[160:161], v3 offset:24576
	ds_read_b64_tr_b16 v[162:163], v3 offset:25600
	ds_read_b64_tr_b16 v[164:165], v3 offset:61440
	ds_read_b64_tr_b16 v[166:167], v3 offset:62464
	ds_read_b64_tr_b16 v[168:169], v124 offset:24576
	ds_read_b64_tr_b16 v[170:171], v124 offset:25600
	ds_read_b64_tr_b16 v[172:173], v124 offset:61440
	ds_read_b64_tr_b16 v[174:175], v124 offset:62464
	ds_read_b64_tr_b16 v[176:177], v2 offset:24576
	ds_read_b64_tr_b16 v[178:179], v2 offset:25600
	ds_read_b64_tr_b16 v[180:181], v2 offset:61440
	ds_read_b64_tr_b16 v[182:183], v2 offset:62464
	ds_read_b64_tr_b16 v[184:185], v125 offset:24576
	ds_read_b64_tr_b16 v[186:187], v125 offset:25600
	ds_read_b64_tr_b16 v[188:189], v125 offset:61440
	ds_read_b64_tr_b16 v[190:191], v125 offset:62464
	ds_read_b64_tr_b16 v[192:193], v3 offset:26624
	ds_read_b64_tr_b16 v[194:195], v3 offset:27648
	ds_read_b64_tr_b16 v[196:197], v3 offset:63488
	ds_read_b64_tr_b16 v[198:199], v3 offset:64512
	ds_read_b64_tr_b16 v[200:201], v124 offset:26624
	ds_read_b64_tr_b16 v[202:203], v124 offset:27648
	ds_read_b64_tr_b16 v[204:205], v124 offset:63488
	ds_read_b64_tr_b16 v[206:207], v124 offset:64512
.Lqk_v22:
	s_waitcnt lgkmcnt(0)
	s_barrier
	s_add_u32 m0, s33, 24576
	s_nop 0
	buffer_load_dwordx4 v0, s[24:27], s32 offen offset:0 nt lds
	s_add_u32 m0, s33, 26496
	s_nop 0
	buffer_load_dwordx4 v0, s[24:27], s32 offen offset:128 nt lds
	s_add_u32 m0, s33, 28416
	s_nop 0
	buffer_load_dwordx4 v0, s[24:27], s32 offen offset:256 nt lds
	s_add_u32 m0, s33, 25600
	s_nop 0
	buffer_load_dwordx4 v1, s[24:27], s32 offen offset:0 nt lds
	s_add_u32 m0, s33, 27520
	s_nop 0
	buffer_load_dwordx4 v1, s[24:27], s32 offen offset:128 nt lds
	s_add_u32 m0, s33, 29440
	s_nop 0
	buffer_load_dwordx4 v1, s[24:27], s32 offen offset:256 nt lds
	s_add_u32 s32, s32, s79
	s_add_u32 m0, s33, 30720
	s_nop 0
	buffer_load_dwordx4 v0, s[24:27], s32 offen offset:0 nt lds
	s_add_u32 m0, s33, 32640
	s_nop 0
	buffer_load_dwordx4 v0, s[24:27], s32 offen offset:128 nt lds
	s_add_u32 m0, s33, 34560
	s_nop 0
	buffer_load_dwordx4 v0, s[24:27], s32 offen offset:256 nt lds
	s_add_u32 m0, s33, 31744
	s_nop 0
	buffer_load_dwordx4 v1, s[24:27], s32 offen offset:0 nt lds
	s_add_u32 m0, s33, 33664
	s_nop 0
	buffer_load_dwordx4 v1, s[24:27], s32 offen offset:128 nt lds
	s_add_u32 m0, s33, 35584
	s_nop 0
	buffer_load_dwordx4 v1, s[24:27], s32 offen offset:256 nt lds
	s_add_u32 s32, s32, 589824
.Lqk_h26:
	s_waitcnt vmcnt(37)
	v_xor_b32 v112, s70, v112
	v_xor_b32 v113, s70, v113
	v_xor_b32 v114, s70, v114
	v_xor_b32 v115, s70, v115
	v_or3_b32 v117, v112, v113, v114
	v_or_b32 v117, v117, v115
	v_and_b32 v118, 0xffffff07, v117
	v_cmp_ne_u32 vcc, 0, v118
	s_cbranch_vccz .Lqk_h27
	v_cmp_lt_u32 vcc, 0xff, v117
	s_cbranch_vccz .Lqk_x1
	s_cmp_eq_u32 s69, 0
	s_cbranch_scc1 .Lqk_h27
	s_sub_u32 s69, s69, 1
	s_sleep 2
	buffer_load_dwordx4 v[112:115], v13, s[20:23], s71 offen offset:0 sc1
	buffer_load_dword v116, off, s[20:23], s71 offset:2048 sc1
	s_waitcnt vmcnt(0)
	s_branch .Lqk_h26
.Lqk_h27:
	v_and_b32 v118, 64, v112
	v_cmp_ne_u32 vcc, 0, v118
	s_bcnt1_i32_b64 s72, vcc
	v_and_b32 v118, 64, v113
	v_cmp_ne_u32 vcc, 0, v118
	s_bcnt1_i32_b64 s18, vcc
	s_add_u32 s72, s72, s18
	v_and_b32 v118, 64, v114
	v_cmp_ne_u32 vcc, 0, v118
	s_bcnt1_i32_b64 s18, vcc
	s_add_u32 s72, s72, s18
	v_and_b32 v118, 64, v115
	v_cmp_ne_u32 vcc, 0, v118
	s_bcnt1_i32_b64 s18, vcc
	s_add_u32 s72, s72, s18
	s_cmp_ge_u32 s72, 20
	s_cselect_b32 s72, 8192, 0
	v_and_b32 v118, 32, v112
	v_cmp_ne_u32 vcc, 0, v118
	s_bcnt1_i32_b64 s66, vcc
	v_and_b32 v118, 32, v113
	v_cmp_ne_u32 vcc, 0, v118
	s_bcnt1_i32_b64 s18, vcc
	s_add_u32 s66, s66, s18
	v_and_b32 v118, 32, v114
	v_cmp_ne_u32 vcc, 0, v118
	s_bcnt1_i32_b64 s18, vcc
	s_add_u32 s66, s66, s18
	v_and_b32 v118, 32, v115
	v_cmp_ne_u32 vcc, 0, v118
	s_bcnt1_i32_b64 s18, vcc
	s_add_u32 s66, s66, s18
	s_cmp_ge_u32 s66, 20
	s_cselect_b32 s66, 4096, 0
	v_and_b32 v118, 128, v112
	v_cmp_ne_u32 vcc, 0, v118
	s_bcnt1_i32_b64 s67, vcc
	v_and_b32 v118, 128, v113
	v_cmp_ne_u32 vcc, 0, v118
	s_bcnt1_i32_b64 s18, vcc
	s_add_u32 s67, s67, s18
	v_and_b32 v118, 128, v114
	v_cmp_ne_u32 vcc, 0, v118
	s_bcnt1_i32_b64 s18, vcc
	s_add_u32 s67, s67, s18
	v_and_b32 v118, 128, v115
	v_cmp_ne_u32 vcc, 0, v118
	s_bcnt1_i32_b64 s18, vcc
	s_add_u32 s67, s67, s18
	s_cmp_ge_u32 s67, 20
	s_cselect_b32 s67, 16384, 0
	s_or_b32 s66, s66, s67
	s_xor_b32 s38, s72, s38
	s_xor_b32 s39, s72, s39
	s_xor_b32 s40, s72, s40
	s_xor_b32 s46, s72, s46
	s_waitcnt vmcnt(36)
	v_readfirstlane_b32 s35, v116
	s_lshl_b32 s48, s34, 1
	s_add_u32 s48, s48, 0
	s_and_b32 s48, s48, 7
	s_lshr_b32 s76, s48, 2
	s_mul_i32 s76, s76, 28
	s_add_u32 s48, s48, s76
	s_lshl_b32 s48, s48, 10
	s_add_u32 s48, s48, s45
	s_lshl_b32 s49, s34, 1
	s_add_u32 s49, s49, 1
	s_and_b32 s49, s49, 7
	s_lshr_b32 s76, s49, 2
	s_mul_i32 s76, s76, 28
	s_add_u32 s49, s49, s76
	s_lshl_b32 s49, s49, 10
	s_add_u32 s49, s49, s45
	s_lshr_b32 s50, s34, 1
	s_add_u32 s50, s50, 8
	s_lshr_b32 s76, s50, 2
	s_mul_i32 s76, s76, 28
	s_add_u32 s50, s50, s76
	s_lshl_b32 s50, s50, 10
	s_add_u32 s50, s50, s45
	s_lshl_b32 s51, s34, 1
	s_add_u32 s51, s51, 0
	s_and_b32 s51, s51, 7
	s_lshl_b32 s51, s51, 10
	s_add_u32 s51, s51, s44
	s_lshr_b32 s52, s34, 1
	s_add_u32 s52, s52, 8
	s_lshl_b32 s52, s52, 10
	s_add_u32 s52, s52, s44
	s_lshl_b32 s53, s34, 1
	s_add_u32 s53, s53, 2
	s_and_b32 s53, s53, 7
	s_lshl_b32 s53, s53, 10
	s_add_u32 s53, s53, s44
	s_lshl_b32 s54, s34, 1
	s_add_u32 s54, s54, 4
	s_and_b32 s54, s54, 7
	s_lshl_b32 s54, s54, 10
	s_add_u32 s54, s54, s44
	s_lshl_b32 s55, s34, 1
	s_add_u32 s55, s55, 6
	s_and_b32 s55, s55, 7
	s_lshl_b32 s55, s55, 10
	s_add_u32 s55, s55, s44
	s_lshr_b32 s56, s34, 1
	s_sub_u32 s56, 9, s56
	s_lshl_b32 s56, s56, 10
	s_add_u32 s56, s56, s44
	s_mov_b32 s59, 0
	s_mov_b64 s[28:29], s[20:21]
	s_mov_b32 s31, s23
	s_mov_b32 s73, 0
	s_mov_b32 s69, 0x20000
	v_mov_b64 v[120:121], -1
	v_mov_b64 v[122:123], -1
	s_cmp_ge_u32 s37, 2
	s_cbranch_scc1 .Lqk_l29
	v_xor_b32 v10, s66, v10
	v_xor_b32 v11, s66, v11
	s_add_u32 s80, s46, 0
	buffer_store_dwordx4 v[120:123], v10, s[20:23], s80 offen sc1
	buffer_store_dwordx4 v[120:123], v11, s[20:23], s80 offen sc1
	s_add_u32 s80, s46, 4194304
	buffer_store_dwordx4 v[120:123], v10, s[20:23], s80 offen sc1
	buffer_store_dwordx4 v[120:123], v11, s[20:23], s80 offen sc1
	s_add_u32 s80, s46, 8388608
	buffer_store_dwordx4 v[120:123], v10, s[20:23], s80 offen sc1
	buffer_store_dwordx4 v[120:123], v11, s[20:23], s80 offen sc1
	s_branch .Lqk_l30
.Lqk_l29:
	s_and_b32 s80, s40, 8192
	s_xor_b32 s80, s80, 127934464
	buffer_store_dwordx4 v[120:123], v10, s[20:23], s80 offen sc1
	buffer_store_dword v120, v11, s[20:23], s80 offen sc1
	s_and_b32 s80, s40, 8192
	s_xor_b32 s80, s80, 130023424
	buffer_store_dwordx4 v[120:123], v10, s[20:23], s80 offen sc1
	buffer_store_dword v120, v11, s[20:23], s80 offen sc1
	s_and_b32 s80, s40, 8192
	s_xor_b32 s80, s80, 132128768
	buffer_store_dwordx4 v[120:123], v10, s[20:23], s80 offen sc1
	buffer_store_dword v120, v11, s[20:23], s80 offen sc1
.Lqk_l30:
	s_waitcnt vmcnt(30)
	s_barrier
	s_cmp_ge_u32 s37, 2
	s_cbranch_scc1 .Lqk_v33
	s_cmp_eq_u32 s37, 1
	s_cbranch_scc1 .Lqk_v32
	ds_read_b64_tr_b16 v[64:65], v3 offset:0
	ds_read_b64_tr_b16 v[66:67], v3 offset:1024
	ds_read_b64_tr_b16 v[68:69], v3 offset:36864
	ds_read_b64_tr_b16 v[70:71], v3 offset:37888
	ds_read_b64_tr_b16 v[72:73], v124 offset:0
	ds_read_b64_tr_b16 v[74:75], v124 offset:1024
	ds_read_b64_tr_b16 v[76:77], v124 offset:36864
	ds_read_b64_tr_b16 v[78:79], v124 offset:37888
	ds_read_b64_tr_b16 v[80:81], v2 offset:0
	ds_read_b64_tr_b16 v[82:83], v2 offset:1024
	ds_read_b64_tr_b16 v[84:85], v2 offset:36864
	ds_read_b64_tr_b16 v[86:87], v2 offset:37888
	ds_read_b64_tr_b16 v[88:89], v125 offset:0
	ds_read_b64_tr_b16 v[90:91], v125 offset:1024
	ds_read_b64_tr_b16 v[92:93], v125 offset:36864
	ds_read_b64_tr_b16 v[94:95], v125 offset:37888
	ds_read_b64_tr_b16 v[96:97], v3 offset:2048
	ds_read_b64_tr_b16 v[98:99], v3 offset:3072
	ds_read_b64_tr_b16 v[100:101], v3 offset:38912
	ds_read_b64_tr_b16 v[102:103], v3 offset:39936
	ds_read_b64_tr_b16 v[104:105], v124 offset:2048
	ds_read_b64_tr_b16 v[106:107], v124 offset:3072
	ds_read_b64_tr_b16 v[108:109], v124 offset:38912
	ds_read_b64_tr_b16 v[110:111], v124 offset:39936
	s_branch .Lqk_v31
.Lqk_v32:
	ds_read_b64_tr_b16 a[192:193], v3 offset:0
	ds_read_b64_tr_b16 a[194:195], v3 offset:1024
	ds_read_b64_tr_b16 a[196:197], v3 offset:36864
	ds_read_b64_tr_b16 a[198:199], v3 offset:37888
	ds_read_b64_tr_b16 a[200:201], v124 offset:0
	ds_read_b64_tr_b16 a[202:203], v124 offset:1024
	ds_read_b64_tr_b16 a[204:205], v124 offset:36864
	ds_read_b64_tr_b16 a[206:207], v124 offset:37888
	ds_read_b64_tr_b16 a[208:209], v2 offset:0
	ds_read_b64_tr_b16 a[210:211], v2 offset:1024
	ds_read_b64_tr_b16 a[212:213], v2 offset:36864
	ds_read_b64_tr_b16 a[214:215], v2 offset:37888
	ds_read_b64_tr_b16 a[216:217], v125 offset:0
	ds_read_b64_tr_b16 a[218:219], v125 offset:1024
	ds_read_b64_tr_b16 a[220:221], v125 offset:36864
	ds_read_b64_tr_b16 a[222:223], v125 offset:37888
	ds_read_b64_tr_b16 a[224:225], v3 offset:2048
	ds_read_b64_tr_b16 a[226:227], v3 offset:3072
	ds_read_b64_tr_b16 a[228:229], v3 offset:38912
	ds_read_b64_tr_b16 a[230:231], v3 offset:39936
	ds_read_b64_tr_b16 a[232:233], v124 offset:2048
	ds_read_b64_tr_b16 a[234:235], v124 offset:3072
	ds_read_b64_tr_b16 a[236:237], v124 offset:38912
	ds_read_b64_tr_b16 a[238:239], v124 offset:39936
	s_branch .Lqk_v31
.Lqk_v33:
	s_cmp_eq_u32 s37, 3
	s_cbranch_scc1 .Lqk_v34
	ds_read_b64_tr_b16 v[16:17], v3 offset:0
	ds_read_b64_tr_b16 v[18:19], v3 offset:1024
	ds_read_b64_tr_b16 v[20:21], v3 offset:36864
	ds_read_b64_tr_b16 v[22:23], v3 offset:37888
	ds_read_b64_tr_b16 v[24:25], v124 offset:0
	ds_read_b64_tr_b16 v[26:27], v124 offset:1024
	ds_read_b64_tr_b16 v[28:29], v124 offset:36864
	ds_read_b64_tr_b16 v[30:31], v124 offset:37888
	ds_read_b64_tr_b16 v[32:33], v2 offset:0
	ds_read_b64_tr_b16 v[34:35], v2 offset:1024
	ds_read_b64_tr_b16 v[36:37], v2 offset:36864
	ds_read_b64_tr_b16 v[38:39], v2 offset:37888
	ds_read_b64_tr_b16 v[40:41], v125 offset:0
	ds_read_b64_tr_b16 v[42:43], v125 offset:1024
	ds_read_b64_tr_b16 v[44:45], v125 offset:36864
	ds_read_b64_tr_b16 v[46:47], v125 offset:37888
	ds_read_b64_tr_b16 v[48:49], v3 offset:2048
	ds_read_b64_tr_b16 v[50:51], v3 offset:3072
	ds_read_b64_tr_b16 v[52:53], v3 offset:38912
	ds_read_b64_tr_b16 v[54:55], v3 offset:39936
	ds_read_b64_tr_b16 v[56:57], v124 offset:2048
	ds_read_b64_tr_b16 v[58:59], v124 offset:3072
	ds_read_b64_tr_b16 v[60:61], v124 offset:38912
	ds_read_b64_tr_b16 v[62:63], v124 offset:39936
	s_branch .Lqk_v31
.Lqk_v34:
	ds_read_b64_tr_b16 a[0:1], v3 offset:0
	ds_read_b64_tr_b16 a[2:3], v3 offset:1024
	ds_read_b64_tr_b16 a[4:5], v3 offset:36864
	ds_read_b64_tr_b16 a[6:7], v3 offset:37888
	ds_read_b64_tr_b16 a[8:9], v124 offset:0
	ds_read_b64_tr_b16 a[10:11], v124 offset:1024
	ds_read_b64_tr_b16 a[12:13], v124 offset:36864
	ds_read_b64_tr_b16 a[14:15], v124 offset:37888
	ds_read_b64_tr_b16 a[16:17], v2 offset:0
	ds_read_b64_tr_b16 a[18:19], v2 offset:1024
	ds_read_b64_tr_b16 a[20:21], v2 offset:36864
	ds_read_b64_tr_b16 a[22:23], v2 offset:37888
	ds_read_b64_tr_b16 a[24:25], v125 offset:0
	ds_read_b64_tr_b16 a[26:27], v125 offset:1024
	ds_read_b64_tr_b16 a[28:29], v125 offset:36864
	ds_read_b64_tr_b16 a[30:31], v125 offset:37888
	ds_read_b64_tr_b16 a[32:33], v3 offset:2048
	ds_read_b64_tr_b16 a[34:35], v3 offset:3072
	ds_read_b64_tr_b16 a[36:37], v3 offset:38912
	ds_read_b64_tr_b16 a[38:39], v3 offset:39936
	ds_read_b64_tr_b16 a[40:41], v124 offset:2048
	ds_read_b64_tr_b16 a[42:43], v124 offset:3072
	ds_read_b64_tr_b16 a[44:45], v124 offset:38912
	ds_read_b64_tr_b16 a[46:47], v124 offset:39936
.Lqk_v31:
	s_waitcnt lgkmcnt(0)
	s_barrier
	buffer_load_dword v119, off, s[20:23], s39 sc1
	s_add_u32 m0, s33, 0
	s_nop 0
	buffer_load_dwordx4 v0, s[24:27], s32 offen offset:0 nt lds
	s_add_u32 m0, s33, 1920
	s_nop 0
	buffer_load_dwordx4 v0, s[24:27], s32 offen offset:128 nt lds
	s_add_u32 m0, s33, 3840
	s_nop 0
	buffer_load_dwordx4 v0, s[24:27], s32 offen offset:256 nt lds
	s_add_u32 m0, s33, 1024
	s_nop 0
	buffer_load_dwordx4 v1, s[24:27], s32 offen offset:0 nt lds
	s_add_u32 m0, s33, 2944
	s_nop 0
	buffer_load_dwordx4 v1, s[24:27], s32 offen offset:128 nt lds
	s_add_u32 m0, s33, 4864
	s_nop 0
	buffer_load_dwordx4 v1, s[24:27], s32 offen offset:256 nt lds
	s_add_u32 s32, s32, s79
	s_add_u32 m0, s33, 6144
	s_nop 0
	buffer_load_dwordx4 v0, s[24:27], s32 offen offset:0 nt lds
	s_add_u32 m0, s33, 8064
	s_nop 0
	buffer_load_dwordx4 v0, s[24:27], s32 offen offset:128 nt lds
	s_add_u32 m0, s33, 9984
	s_nop 0
	buffer_load_dwordx4 v0, s[24:27], s32 offen offset:256 nt lds
	s_add_u32 m0, s33, 7168
	s_nop 0
	buffer_load_dwordx4 v1, s[24:27], s32 offen offset:0 nt lds
	s_add_u32 m0, s33, 9088
	s_nop 0
	buffer_load_dwordx4 v1, s[24:27], s32 offen offset:128 nt lds
	s_add_u32 m0, s33, 11008
	s_nop 0
	buffer_load_dwordx4 v1, s[24:27], s32 offen offset:256 nt lds
	s_add_u32 s32, s32, 589824
	s_waitcnt vmcnt(31)
	s_barrier
	s_cmp_ge_u32 s37, 2
	s_cbranch_scc1 .Lqk_v37
	s_cmp_eq_u32 s37, 1
	s_cbranch_scc1 .Lqk_v36
	ds_read_b64_tr_b16 v[160:161], v3 offset:12288
	ds_read_b64_tr_b16 v[162:163], v3 offset:13312
	ds_read_b64_tr_b16 v[164:165], v3 offset:49152
	ds_read_b64_tr_b16 v[166:167], v3 offset:50176
	ds_read_b64_tr_b16 v[168:169], v124 offset:12288
	ds_read_b64_tr_b16 v[170:171], v124 offset:13312
	ds_read_b64_tr_b16 v[172:173], v124 offset:49152
	ds_read_b64_tr_b16 v[174:175], v124 offset:50176
	ds_read_b64_tr_b16 v[176:177], v2 offset:12288
	ds_read_b64_tr_b16 v[178:179], v2 offset:13312
	ds_read_b64_tr_b16 v[180:181], v2 offset:49152
	ds_read_b64_tr_b16 v[182:183], v2 offset:50176
	ds_read_b64_tr_b16 v[184:185], v125 offset:12288
	ds_read_b64_tr_b16 v[186:187], v125 offset:13312
	ds_read_b64_tr_b16 v[188:189], v125 offset:49152
	ds_read_b64_tr_b16 v[190:191], v125 offset:50176
	ds_read_b64_tr_b16 v[192:193], v3 offset:14336
	ds_read_b64_tr_b16 v[194:195], v3 offset:15360
	ds_read_b64_tr_b16 v[196:197], v3 offset:51200
	ds_read_b64_tr_b16 v[198:199], v3 offset:52224
	ds_read_b64_tr_b16 v[200:201], v124 offset:14336
	ds_read_b64_tr_b16 v[202:203], v124 offset:15360
	ds_read_b64_tr_b16 v[204:205], v124 offset:51200
	ds_read_b64_tr_b16 v[206:207], v124 offset:52224
	s_branch .Lqk_v35
.Lqk_v36:
	ds_read_b64_tr_b16 a[240:241], v3 offset:12288
	ds_read_b64_tr_b16 a[242:243], v3 offset:13312
	ds_read_b64_tr_b16 a[244:245], v3 offset:49152
	ds_read_b64_tr_b16 a[246:247], v3 offset:50176
	ds_read_b64_tr_b16 a[248:249], v124 offset:12288
	ds_read_b64_tr_b16 a[250:251], v124 offset:13312
	ds_read_b64_tr_b16 a[252:253], v124 offset:49152
	ds_read_b64_tr_b16 a[254:255], v124 offset:50176
	ds_read_b64_tr_b16 v[128:129], v2 offset:12288
	ds_read_b64_tr_b16 v[130:131], v2 offset:13312
	ds_read_b64_tr_b16 v[132:133], v2 offset:49152
	ds_read_b64_tr_b16 v[134:135], v2 offset:50176
	ds_read_b64_tr_b16 v[136:137], v125 offset:12288
	ds_read_b64_tr_b16 v[138:139], v125 offset:13312
	ds_read_b64_tr_b16 v[140:141], v125 offset:49152
	ds_read_b64_tr_b16 v[142:143], v125 offset:50176
	ds_read_b64_tr_b16 v[144:145], v3 offset:14336
	ds_read_b64_tr_b16 v[146:147], v3 offset:15360
	ds_read_b64_tr_b16 v[148:149], v3 offset:51200
	ds_read_b64_tr_b16 v[150:151], v3 offset:52224
	ds_read_b64_tr_b16 v[152:153], v124 offset:14336
	ds_read_b64_tr_b16 v[154:155], v124 offset:15360
	ds_read_b64_tr_b16 v[156:157], v124 offset:51200
	ds_read_b64_tr_b16 v[158:159], v124 offset:52224
	s_branch .Lqk_v35
.Lqk_v37:
	s_cmp_eq_u32 s37, 3
	s_cbranch_scc1 .Lqk_v38
	ds_read_b64_tr_b16 a[144:145], v3 offset:12288
	ds_read_b64_tr_b16 a[146:147], v3 offset:13312
	ds_read_b64_tr_b16 a[148:149], v3 offset:49152
	ds_read_b64_tr_b16 a[150:151], v3 offset:50176
	ds_read_b64_tr_b16 a[152:153], v124 offset:12288
	ds_read_b64_tr_b16 a[154:155], v124 offset:13312
	ds_read_b64_tr_b16 a[156:157], v124 offset:49152
	ds_read_b64_tr_b16 a[158:159], v124 offset:50176
	ds_read_b64_tr_b16 a[160:161], v2 offset:12288
	ds_read_b64_tr_b16 a[162:163], v2 offset:13312
	ds_read_b64_tr_b16 a[164:165], v2 offset:49152
	ds_read_b64_tr_b16 a[166:167], v2 offset:50176
	ds_read_b64_tr_b16 a[168:169], v125 offset:12288
	ds_read_b64_tr_b16 a[170:171], v125 offset:13312
	ds_read_b64_tr_b16 a[172:173], v125 offset:49152
	ds_read_b64_tr_b16 a[174:175], v125 offset:50176
	ds_read_b64_tr_b16 a[176:177], v3 offset:14336
	ds_read_b64_tr_b16 a[178:179], v3 offset:15360
	ds_read_b64_tr_b16 a[180:181], v3 offset:51200
	ds_read_b64_tr_b16 a[182:183], v3 offset:52224
	ds_read_b64_tr_b16 a[184:185], v124 offset:14336
	ds_read_b64_tr_b16 a[186:187], v124 offset:15360
	ds_read_b64_tr_b16 a[188:189], v124 offset:51200
	ds_read_b64_tr_b16 a[190:191], v124 offset:52224
	s_branch .Lqk_v35
.Lqk_v38:
	ds_read_b64_tr_b16 a[48:49], v3 offset:12288
	ds_read_b64_tr_b16 a[50:51], v3 offset:13312
	ds_read_b64_tr_b16 a[52:53], v3 offset:49152
	ds_read_b64_tr_b16 a[54:55], v3 offset:50176
	ds_read_b64_tr_b16 a[56:57], v124 offset:12288
	ds_read_b64_tr_b16 a[58:59], v124 offset:13312
	ds_read_b64_tr_b16 a[60:61], v124 offset:49152
	ds_read_b64_tr_b16 a[62:63], v124 offset:50176
	ds_read_b64_tr_b16 a[64:65], v2 offset:12288
	ds_read_b64_tr_b16 a[66:67], v2 offset:13312
	ds_read_b64_tr_b16 a[68:69], v2 offset:49152
	ds_read_b64_tr_b16 a[70:71], v2 offset:50176
	ds_read_b64_tr_b16 a[72:73], v125 offset:12288
	ds_read_b64_tr_b16 a[74:75], v125 offset:13312
	ds_read_b64_tr_b16 a[76:77], v125 offset:49152
	ds_read_b64_tr_b16 a[78:79], v125 offset:50176
	ds_read_b64_tr_b16 a[80:81], v3 offset:14336
	ds_read_b64_tr_b16 a[82:83], v3 offset:15360
	ds_read_b64_tr_b16 a[84:85], v3 offset:51200
	ds_read_b64_tr_b16 a[86:87], v3 offset:52224
	ds_read_b64_tr_b16 a[88:89], v124 offset:14336
	ds_read_b64_tr_b16 a[90:91], v124 offset:15360
	ds_read_b64_tr_b16 a[92:93], v124 offset:51200
	ds_read_b64_tr_b16 a[94:95], v124 offset:52224
.Lqk_v35:
	s_waitcnt lgkmcnt(0)
	s_barrier
	ds_write_b128 v6, v[64:67] offset:12288
	ds_write_b128 v6, v[68:71] offset:13312
	ds_write_b128 v6, v[72:75] offset:14336
	ds_write_b128 v6, v[76:79] offset:15360
	ds_write_b128 v6, v[80:83] offset:16384
	ds_write_b128 v6, v[84:87] offset:17408
	ds_write_b128 v6, v[88:91] offset:18432
	ds_write_b128 v6, v[92:95] offset:19456
	ds_write_b128 v6, v[96:99] offset:20480
	ds_write_b128 v6, v[100:103] offset:21504
	ds_write_b128 v6, v[104:107] offset:22528
	ds_write_b128 v6, v[108:111] offset:23552
	s_waitcnt vmcnt(19)
	s_barrier
	s_cmp_ge_u32 s37, 2
	s_cbranch_scc1 .Lqk_v41
	s_cmp_eq_u32 s37, 1
	s_cbranch_scc1 .Lqk_v40
	ds_read_b64_tr_b16 a[96:97], v3 offset:24576
	ds_read_b64_tr_b16 a[98:99], v3 offset:25600
	ds_read_b64_tr_b16 a[100:101], v3 offset:61440
	ds_read_b64_tr_b16 a[102:103], v3 offset:62464
	ds_read_b64_tr_b16 a[104:105], v124 offset:24576
	ds_read_b64_tr_b16 a[106:107], v124 offset:25600
	ds_read_b64_tr_b16 a[108:109], v124 offset:61440
	ds_read_b64_tr_b16 a[110:111], v124 offset:62464
	ds_read_b64_tr_b16 a[112:113], v2 offset:24576
	ds_read_b64_tr_b16 a[114:115], v2 offset:25600
	ds_read_b64_tr_b16 a[116:117], v2 offset:61440
	ds_read_b64_tr_b16 a[118:119], v2 offset:62464
	ds_read_b64_tr_b16 a[120:121], v125 offset:24576
	ds_read_b64_tr_b16 a[122:123], v125 offset:25600
	ds_read_b64_tr_b16 a[124:125], v125 offset:61440
	ds_read_b64_tr_b16 a[126:127], v125 offset:62464
	ds_read_b64_tr_b16 a[128:129], v3 offset:26624
	ds_read_b64_tr_b16 a[130:131], v3 offset:27648
	ds_read_b64_tr_b16 a[132:133], v3 offset:63488
	ds_read_b64_tr_b16 a[134:135], v3 offset:64512
	ds_read_b64_tr_b16 a[136:137], v124 offset:26624
	ds_read_b64_tr_b16 a[138:139], v124 offset:27648
	ds_read_b64_tr_b16 a[140:141], v124 offset:63488
	ds_read_b64_tr_b16 a[142:143], v124 offset:64512
	s_branch .Lqk_v39
.Lqk_v40:
	ds_read_b64_tr_b16 a[96:97], v3 offset:24576
	ds_read_b64_tr_b16 a[98:99], v3 offset:25600
	ds_read_b64_tr_b16 a[100:101], v3 offset:61440
	ds_read_b64_tr_b16 a[102:103], v3 offset:62464
	ds_read_b64_tr_b16 a[104:105], v124 offset:24576
	ds_read_b64_tr_b16 a[106:107], v124 offset:25600
	ds_read_b64_tr_b16 a[108:109], v124 offset:61440
	ds_read_b64_tr_b16 a[110:111], v124 offset:62464
	ds_read_b64_tr_b16 a[112:113], v2 offset:24576
	ds_read_b64_tr_b16 a[114:115], v2 offset:25600
	ds_read_b64_tr_b16 a[116:117], v2 offset:61440
	ds_read_b64_tr_b16 a[118:119], v2 offset:62464
	ds_read_b64_tr_b16 a[120:121], v125 offset:24576
	ds_read_b64_tr_b16 a[122:123], v125 offset:25600
	ds_read_b64_tr_b16 a[124:125], v125 offset:61440
	ds_read_b64_tr_b16 a[126:127], v125 offset:62464
	ds_read_b64_tr_b16 a[128:129], v3 offset:26624
	ds_read_b64_tr_b16 a[130:131], v3 offset:27648
	ds_read_b64_tr_b16 a[132:133], v3 offset:63488
	ds_read_b64_tr_b16 a[134:135], v3 offset:64512
	ds_read_b64_tr_b16 a[136:137], v124 offset:26624
	ds_read_b64_tr_b16 a[138:139], v124 offset:27648
	ds_read_b64_tr_b16 a[140:141], v124 offset:63488
	ds_read_b64_tr_b16 a[142:143], v124 offset:64512
	s_branch .Lqk_v39
.Lqk_v41:
	s_cmp_eq_u32 s37, 3
	s_cbranch_scc1 .Lqk_v42
	ds_read_b64_tr_b16 v[208:209], v3 offset:24576
	ds_read_b64_tr_b16 v[210:211], v3 offset:25600
	ds_read_b64_tr_b16 v[212:213], v3 offset:61440
	ds_read_b64_tr_b16 v[214:215], v3 offset:62464
	ds_read_b64_tr_b16 v[216:217], v124 offset:24576
	ds_read_b64_tr_b16 v[218:219], v124 offset:25600
	ds_read_b64_tr_b16 v[220:221], v124 offset:61440
	ds_read_b64_tr_b16 v[222:223], v124 offset:62464
	ds_read_b64_tr_b16 v[224:225], v2 offset:24576
	ds_read_b64_tr_b16 v[226:227], v2 offset:25600
	ds_read_b64_tr_b16 v[228:229], v2 offset:61440
	ds_read_b64_tr_b16 v[230:231], v2 offset:62464
	ds_read_b64_tr_b16 v[232:233], v125 offset:24576
	ds_read_b64_tr_b16 v[234:235], v125 offset:25600
	ds_read_b64_tr_b16 v[236:237], v125 offset:61440
	ds_read_b64_tr_b16 v[238:239], v125 offset:62464
	ds_read_b64_tr_b16 v[240:241], v3 offset:26624
	ds_read_b64_tr_b16 v[242:243], v3 offset:27648
	ds_read_b64_tr_b16 v[244:245], v3 offset:63488
	ds_read_b64_tr_b16 v[246:247], v3 offset:64512
	ds_read_b64_tr_b16 v[248:249], v124 offset:26624
	ds_read_b64_tr_b16 v[250:251], v124 offset:27648
	ds_read_b64_tr_b16 v[252:253], v124 offset:63488
	ds_read_b64_tr_b16 v[254:255], v124 offset:64512
	s_branch .Lqk_v39
.Lqk_v42:
	ds_read_b64_tr_b16 v[208:209], v3 offset:24576
	ds_read_b64_tr_b16 v[210:211], v3 offset:25600
	ds_read_b64_tr_b16 v[212:213], v3 offset:61440
	ds_read_b64_tr_b16 v[214:215], v3 offset:62464
	ds_read_b64_tr_b16 v[216:217], v124 offset:24576
	ds_read_b64_tr_b16 v[218:219], v124 offset:25600
	ds_read_b64_tr_b16 v[220:221], v124 offset:61440
	ds_read_b64_tr_b16 v[222:223], v124 offset:62464
	ds_read_b64_tr_b16 v[224:225], v2 offset:24576
	ds_read_b64_tr_b16 v[226:227], v2 offset:25600
	ds_read_b64_tr_b16 v[228:229], v2 offset:61440
	ds_read_b64_tr_b16 v[230:231], v2 offset:62464
	ds_read_b64_tr_b16 v[232:233], v125 offset:24576
	ds_read_b64_tr_b16 v[234:235], v125 offset:25600
	ds_read_b64_tr_b16 v[236:237], v125 offset:61440
	ds_read_b64_tr_b16 v[238:239], v125 offset:62464
	ds_read_b64_tr_b16 v[240:241], v3 offset:26624
	ds_read_b64_tr_b16 v[242:243], v3 offset:27648
	ds_read_b64_tr_b16 v[244:245], v3 offset:63488
	ds_read_b64_tr_b16 v[246:247], v3 offset:64512
	ds_read_b64_tr_b16 v[248:249], v124 offset:26624
	ds_read_b64_tr_b16 v[250:251], v124 offset:27648
	ds_read_b64_tr_b16 v[252:253], v124 offset:63488
	ds_read_b64_tr_b16 v[254:255], v124 offset:64512
.Lqk_v39:
	s_waitcnt lgkmcnt(0)
	s_barrier
	ds_write_b128 v6, v[16:19] offset:24576
	ds_write_b128 v6, v[20:23] offset:25600
	ds_write_b128 v6, v[24:27] offset:26624
	ds_write_b128 v6, v[28:31] offset:27648
	ds_write_b128 v6, v[32:35] offset:28672
	ds_write_b128 v6, v[36:39] offset:29696
	ds_write_b128 v6, v[40:43] offset:30720
	ds_write_b128 v6, v[44:47] offset:31744
	ds_write_b128 v6, v[48:51] offset:32768
	ds_write_b128 v6, v[52:55] offset:33792
	ds_write_b128 v6, v[56:59] offset:34816
	ds_write_b128 v6, v[60:63] offset:35840
	s_waitcnt vmcnt(12)
	v_readfirstlane_b32 s73, v119
	s_lshr_b32 s18, s37, 1
	s_min_u32 s18, s18, s73
	s_cmp_lg_u32 s18, 0
	s_cselect_b32 s30, s22, 0
	buffer_load_dword v119, off, s[20:23], s39 sc1
	s_waitcnt lgkmcnt(0)
	s_add_u32 s80, s40, 0
	buffer_load_dwordx4 v[64:67], v13, s[28:31], s80 offen offset:0 sc1
	buffer_load_dwordx4 v[68:71], v13, s[28:31], s80 offen offset:1024 sc1
	buffer_load_dwordx4 v[72:75], v13, s[28:31], s80 offen offset:2048 sc1
	buffer_load_dwordx4 v[76:79], v13, s[28:31], s80 offen offset:3072 sc1
	s_add_u32 s80, s40, s36
	buffer_load_dwordx4 v[80:83], v13, s[28:31], s80 offen offset:0 sc1
	buffer_load_dwordx4 v[84:87], v13, s[28:31], s80 offen offset:1024 sc1
	buffer_load_dwordx4 v[88:91], v13, s[28:31], s80 offen offset:2048 sc1
	buffer_load_dwordx4 v[92:95], v13, s[28:31], s80 offen offset:3072 sc1
	s_add_u32 s80, s40, 16384
	buffer_load_dwordx4 v[96:99], v13, s[28:31], s80 offen offset:0 sc1
	buffer_load_dwordx4 v[100:103], v13, s[28:31], s80 offen offset:1024 sc1
	buffer_load_dwordx4 v[104:107], v13, s[28:31], s80 offen offset:2048 sc1
	buffer_load_dwordx4 v[108:111], v13, s[28:31], s80 offen offset:3072 sc1
	s_waitcnt vmcnt(13)
	s_barrier
	s_cmp_ge_u32 s37, 2
	s_cbranch_scc1 .Lqk_v45
	s_cmp_eq_u32 s37, 1
	s_cbranch_scc1 .Lqk_v44
	ds_read_b64_tr_b16 v[208:209], v3 offset:0
	ds_read_b64_tr_b16 v[210:211], v3 offset:1024
	ds_read_b64_tr_b16 v[212:213], v3 offset:36864
	ds_read_b64_tr_b16 v[214:215], v3 offset:37888
	ds_read_b64_tr_b16 v[216:217], v124 offset:0
	ds_read_b64_tr_b16 v[218:219], v124 offset:1024
	ds_read_b64_tr_b16 v[220:221], v124 offset:36864
	ds_read_b64_tr_b16 v[222:223], v124 offset:37888
	ds_read_b64_tr_b16 v[224:225], v2 offset:0
	ds_read_b64_tr_b16 v[226:227], v2 offset:1024
	ds_read_b64_tr_b16 v[228:229], v2 offset:36864
	ds_read_b64_tr_b16 v[230:231], v2 offset:37888
	ds_read_b64_tr_b16 v[232:233], v125 offset:0
	ds_read_b64_tr_b16 v[234:235], v125 offset:1024
	ds_read_b64_tr_b16 v[236:237], v125 offset:36864
	ds_read_b64_tr_b16 v[238:239], v125 offset:37888
	ds_read_b64_tr_b16 v[240:241], v3 offset:2048
	ds_read_b64_tr_b16 v[242:243], v3 offset:3072
	ds_read_b64_tr_b16 v[244:245], v3 offset:38912
	ds_read_b64_tr_b16 v[246:247], v3 offset:39936
	ds_read_b64_tr_b16 v[248:249], v124 offset:2048
	ds_read_b64_tr_b16 v[250:251], v124 offset:3072
	ds_read_b64_tr_b16 v[252:253], v124 offset:38912
	ds_read_b64_tr_b16 v[254:255], v124 offset:39936
	s_branch .Lqk_v43
.Lqk_v44:
	ds_read_b64_tr_b16 v[208:209], v3 offset:0
	ds_read_b64_tr_b16 v[210:211], v3 offset:1024
	ds_read_b64_tr_b16 v[212:213], v3 offset:36864
	ds_read_b64_tr_b16 v[214:215], v3 offset:37888
	ds_read_b64_tr_b16 v[216:217], v124 offset:0
	ds_read_b64_tr_b16 v[218:219], v124 offset:1024
	ds_read_b64_tr_b16 v[220:221], v124 offset:36864
	ds_read_b64_tr_b16 v[222:223], v124 offset:37888
	ds_read_b64_tr_b16 v[224:225], v2 offset:0
	ds_read_b64_tr_b16 v[226:227], v2 offset:1024
	ds_read_b64_tr_b16 v[228:229], v2 offset:36864
	ds_read_b64_tr_b16 v[230:231], v2 offset:37888
	ds_read_b64_tr_b16 v[232:233], v125 offset:0
	ds_read_b64_tr_b16 v[234:235], v125 offset:1024
	ds_read_b64_tr_b16 v[236:237], v125 offset:36864
	ds_read_b64_tr_b16 v[238:239], v125 offset:37888
	ds_read_b64_tr_b16 v[240:241], v3 offset:2048
	ds_read_b64_tr_b16 v[242:243], v3 offset:3072
	ds_read_b64_tr_b16 v[244:245], v3 offset:38912
	ds_read_b64_tr_b16 v[246:247], v3 offset:39936
	ds_read_b64_tr_b16 v[248:249], v124 offset:2048
	ds_read_b64_tr_b16 v[250:251], v124 offset:3072
	ds_read_b64_tr_b16 v[252:253], v124 offset:38912
	ds_read_b64_tr_b16 v[254:255], v124 offset:39936
	s_branch .Lqk_v43
.Lqk_v45:
	s_cmp_eq_u32 s37, 3
	s_cbranch_scc1 .Lqk_v46
	ds_read_b64_tr_b16 a[96:97], v3 offset:0
	ds_read_b64_tr_b16 a[98:99], v3 offset:1024
	ds_read_b64_tr_b16 a[100:101], v3 offset:36864
	ds_read_b64_tr_b16 a[102:103], v3 offset:37888
	ds_read_b64_tr_b16 a[104:105], v124 offset:0
	ds_read_b64_tr_b16 a[106:107], v124 offset:1024
	ds_read_b64_tr_b16 a[108:109], v124 offset:36864
	ds_read_b64_tr_b16 a[110:111], v124 offset:37888
	ds_read_b64_tr_b16 a[112:113], v2 offset:0
	ds_read_b64_tr_b16 a[114:115], v2 offset:1024
	ds_read_b64_tr_b16 a[116:117], v2 offset:36864
	ds_read_b64_tr_b16 a[118:119], v2 offset:37888
	ds_read_b64_tr_b16 a[120:121], v125 offset:0
	ds_read_b64_tr_b16 a[122:123], v125 offset:1024
	ds_read_b64_tr_b16 a[124:125], v125 offset:36864
	ds_read_b64_tr_b16 a[126:127], v125 offset:37888
	ds_read_b64_tr_b16 a[128:129], v3 offset:2048
	ds_read_b64_tr_b16 a[130:131], v3 offset:3072
	ds_read_b64_tr_b16 a[132:133], v3 offset:38912
	ds_read_b64_tr_b16 a[134:135], v3 offset:39936
	ds_read_b64_tr_b16 a[136:137], v124 offset:2048
	ds_read_b64_tr_b16 a[138:139], v124 offset:3072
	ds_read_b64_tr_b16 a[140:141], v124 offset:38912
	ds_read_b64_tr_b16 a[142:143], v124 offset:39936
	s_branch .Lqk_v43
.Lqk_v46:
	ds_read_b64_tr_b16 a[96:97], v3 offset:0
	ds_read_b64_tr_b16 a[98:99], v3 offset:1024
	ds_read_b64_tr_b16 a[100:101], v3 offset:36864
	ds_read_b64_tr_b16 a[102:103], v3 offset:37888
	ds_read_b64_tr_b16 a[104:105], v124 offset:0
	ds_read_b64_tr_b16 a[106:107], v124 offset:1024
	ds_read_b64_tr_b16 a[108:109], v124 offset:36864
	ds_read_b64_tr_b16 a[110:111], v124 offset:37888
	ds_read_b64_tr_b16 a[112:113], v2 offset:0
	ds_read_b64_tr_b16 a[114:115], v2 offset:1024
	ds_read_b64_tr_b16 a[116:117], v2 offset:36864
	ds_read_b64_tr_b16 a[118:119], v2 offset:37888
	ds_read_b64_tr_b16 a[120:121], v125 offset:0
	ds_read_b64_tr_b16 a[122:123], v125 offset:1024
	ds_read_b64_tr_b16 a[124:125], v125 offset:36864
	ds_read_b64_tr_b16 a[126:127], v125 offset:37888
	ds_read_b64_tr_b16 a[128:129], v3 offset:2048
	ds_read_b64_tr_b16 a[130:131], v3 offset:3072
	ds_read_b64_tr_b16 a[132:133], v3 offset:38912
	ds_read_b64_tr_b16 a[134:135], v3 offset:39936
	ds_read_b64_tr_b16 a[136:137], v124 offset:2048
	ds_read_b64_tr_b16 a[138:139], v124 offset:3072
	ds_read_b64_tr_b16 a[140:141], v124 offset:38912
	ds_read_b64_tr_b16 a[142:143], v124 offset:39936
.Lqk_v43:
	s_waitcnt lgkmcnt(0)
	s_barrier
	v_mov_b32 v117, s70
	s_bitcmp0_b32 s64, 11
	s_cselect_b64 exec, 1, 0
	buffer_store_dword v117, off, s[20:23], s64 sc1
	s_mov_b64 exec, -1
	v_mov_b32 v0, s41
	v_mov_b32 v1, 0
	ds_write_b32 v0, v1
	s_cmp_ge_u32 s37, 2
	s_cbranch_scc1 .Lqk_tk47
	s_waitcnt vmcnt(0)
	s_waitcnt lgkmcnt(0)
	s_barrier
	s_memrealtime s[98:99]
	s_waitcnt lgkmcnt(0)
	s_mov_b32 s58, 0
	s_mov_b32 s74, 0
	s_mov_b32 s75, 0
	s_mov_b32 s19, 0
	s_mov_b32 s63, 128
	s_mov_b32 s65, 72
	s_mov_b32 s69, 0x20000
	s_sub_u32 s76, s98, s35
	s_cmp_gt_u32 s76, 0x10000
	s_cselect_b32 s60, s98, s35
	s_sub_u32 s60, s60, s63
.Lqk_c49:
	s_add_u32 s60, s60, 80
	s_sub_u32 s18, s60, s98
	s_cmp_lt_i32 s18, 0
	s_cbranch_scc1 .Lqk_c49
	s_sub_u32 s18, s60, s35
	v_mov_b32 v126, s41
	v_mov_b32 v127, s18
	ds_max_u32 v126, v127
.Lqk_step50:
	s_add_u32 s76, s58, 0
	s_and_b32 s76, s76, 7
	s_lshl_b32 s76, s76, 22
	s_add_u32 s76, s76, s46
	s_add_u32 s71, s76, 67584
	s_sub_u32 s77, s76, s59
	s_mov_b32 s59, s76
	s_add_u32 s48, s48, s77
	s_add_u32 s49, s49, s77
	s_add_u32 s50, s50, s77
	v_add_u32 v3, s74, v14
	ds_read_b128 v[40:43], v6 offset:24576
	ds_read_b128 v[44:47], v6 offset:25600
	ds_read_b128 v[48:51], v6 offset:12288
	ds_read_b128 v[52:55], v6 offset:13312
	ds_read_b128 v[56:59], v6 offset:26624
	ds_read_b128 v[60:63], v6 offset:27648
	v_add_u32 v124, s51, v13
	v_add_u32 v125, s52, v13
	v_add_u32 v80, s53, v13
	v_add_u32 v88, s54, v13
	v_add_u32 v96, s55, v13
	v_add_u32 v100, s56, v13
	v_mov_b64 v[16:17], 0
	v_mov_b64 v[18:19], 0
	v_mov_b64 v[20:21], 0
	v_mov_b64 v[22:23], 0
	v_mov_b64 v[24:25], 0
	v_mov_b64 v[26:27], 0
	v_mov_b64 v[28:29], 0
	v_mov_b64 v[30:31], 0
	v_mov_b64 v[32:33], 0
	v_mov_b64 v[34:35], 0
	v_mov_b64 v[36:37], 0
	v_mov_b64 v[38:39], 0
	s_cmp_lg_u32 s37, 1
	s_cselect_b32 s82, 2, 0
	s_memrealtime s[98:99]
	s_waitcnt lgkmcnt(0)
.Lqk_b052:
	s_mov_b32 s72, s60
	s_sub_u32 s18, s60, s98
	s_sub_u32 s18, s18, 8
	s_cmp_gt_u32 s18, 0xfa0
	s_cbranch_scc1 .Lqk_b052c
	s_cmp_eq_u32 s82, 2
	s_cbranch_scc1 .Lqk_b052r
	s_cmp_le_u32 s18, 6
	s_cbranch_scc1 .Lqk_w53b
.Lqk_w53:
	s_memrealtime s[98:99]
	s_waitcnt lgkmcnt(0)
	s_sub_u32 s18, s60, s98
	s_cmp_gt_i32 s18, 14
	s_cbranch_scc1 .Lqk_w53
.Lqk_w53b:
	s_memrealtime s[0:1]
	s_nop 1
	s_memrealtime s[2:3]
	s_nop 1
	s_memrealtime s[4:5]
	s_nop 0
	s_memrealtime s[6:7]
	s_nop 1
	s_memrealtime s[76:77]
	s_nop 1
	s_memrealtime s[78:79]
	s_nop 0
	s_memrealtime s[80:81]
	s_nop 1
	s_memrealtime s[98:99]
	s_lshl_b32 s18, s60, 3
	s_waitcnt lgkmcnt(0)
	s_add_u32 s0, s0, s2
	s_add_u32 s4, s4, s6
	s_add_u32 s76, s76, s78
	s_add_u32 s80, s80, s98
	s_add_u32 s0, s0, s4
	s_add_u32 s76, s76, s80
	s_getpc_b64 s[2:3]
.Lqk_w54:
	s_sub_u32 s18, s0, s18
	s_add_u32 s18, s18, s76
	s_mul_i32 s18, s18, 88
	s_ashr_i32 s18, s18, 8
	s_max_i32 s18, s18, -40
	s_min_i32 s18, s18, -8
	s_lshl2_add_u32 s18, s18, .Lqk_w55-.Lqk_w54+160
	s_add_u32 s2, s2, s18
	s_addc_u32 s3, s3, 0
	s_cmp_lg_u32 s82, 0
	s_setpc_b64 s[2:3]
	.p2align 6
.Lqk_w55:
	s_nop 1
	s_nop 1
	s_nop 1
	s_nop 1
	s_nop 1
	s_nop 1
	s_nop 1
	s_nop 1
	s_nop 1
	s_nop 1
	s_nop 1
	s_nop 1
	s_nop 1
	s_nop 1
	s_nop 1
	s_nop 1
	s_nop 1
	s_nop 1
	s_nop 1
	s_nop 1
	s_nop 1
	s_nop 1
	s_nop 1
	s_nop 1
	s_nop 1
	s_nop 1
	s_nop 1
	s_nop 1
	s_nop 1
	s_nop 1
	s_nop 1
	s_nop 1
.Lqk_b052x:
	s_cbranch_scc1 .Lqk_b052g
.Lqk_b052r:
	s_barrier
.Lqk_b052g:
	buffer_load_dwordx4 v[64:67], v5, s[20:23], s48 offen sc1
	s_nop 3
	buffer_load_dwordx4 v[68:71], v5, s[20:23], s49 offen sc1
	s_nop 3
	buffer_load_dwordx4 v[72:75], v5, s[20:23], s50 offen sc1
	s_waitcnt vmcnt(2)
	v_smfmac_f32_16x16x64_bf16 v[16:19], v[64:67], a[0:7], v4
	ds_write_b128 v124, v[64:67] offset:0
	v_smfmac_f32_16x16x64_bf16 v[20:23], v[64:67], a[8:15], v4
	v_smfmac_f32_16x16x64_bf16 v[24:27], v[64:67], a[16:23], v4
	v_smfmac_f32_16x16x64_bf16 v[28:31], v[64:67], a[24:31], v4
	v_smfmac_f32_16x16x64_bf16 v[32:35], v[64:67], a[32:39], v4
	s_waitcnt vmcnt(1)
	ds_write_b128 v124, v[68:71] offset:1024
	v_smfmac_f32_16x16x64_bf16 v[36:39], v[64:67], a[40:47], v4
	v_smfmac_f32_16x16x64_bf16 v[16:19], v[68:71], a[48:55], v4
	v_smfmac_f32_16x16x64_bf16 v[20:23], v[68:71], a[56:63], v4
	v_smfmac_f32_16x16x64_bf16 v[24:27], v[68:71], a[64:71], v4
	s_waitcnt vmcnt(0)
	ds_write_b128 v125, v[72:75] offset:0
	v_max3_u32 v126, v64, v68, v72
	v_smfmac_f32_16x16x64_bf16 v[28:31], v[68:71], a[72:79], v4
	v_cmp_eq_u32 vcc, -1, v126
	v_smfmac_f32_16x16x64_bf16 v[32:35], v[68:71], a[80:87], v4
	v_smfmac_f32_16x16x64_bf16 v[36:39], v[68:71], a[88:95], v4
	s_cbranch_vccnz .Lqk_f051
.Lqk_f051c:
	s_waitcnt lgkmcnt(0)
	s_barrier
	v_smfmac_f32_16x16x64_bf16 v[16:19], v[72:75], a[96:103], v4
	ds_read_b128 v[76:79], v80 offset:0
	ds_read_b128 v[80:83], v80 offset:1024
	v_smfmac_f32_16x16x64_bf16 v[20:23], v[72:75], a[104:111], v4
	ds_read_b128 v[84:87], v88 offset:0
	ds_read_b128 v[88:91], v88 offset:1024
	v_smfmac_f32_16x16x64_bf16 v[24:27], v[72:75], a[112:119], v4
	ds_read_b128 v[92:95], v96 offset:0
	ds_read_b128 v[96:99], v96 offset:1024
	ds_read_b128 v[104:107], v3 offset:0
	v_smfmac_f32_16x16x64_bf16 v[28:31], v[72:75], a[120:127], v4
	ds_read_b128 v[100:103], v100 offset:0
	ds_read_b128 v[108:111], v3 offset:3072
	v_smfmac_f32_16x16x64_bf16 v[32:35], v[72:75], a[128:135], v4
	ds_read_b128 v[112:115], v3 offset:1024
	v_smfmac_f32_16x16x64_bf16 v[36:39], v[72:75], a[136:143], v4
	s_waitcnt lgkmcnt(9)
	v_smfmac_f32_16x16x64_bf16 v[16:19], v[76:79], v[40:47], v4
	ds_read_b128 v[40:43], v6 offset:14336
	ds_read_b128 v[44:47], v6 offset:15360
	s_waitcnt lgkmcnt(10)
	v_smfmac_f32_16x16x64_bf16 v[16:19], v[80:83], a[144:151], v4
	s_waitcnt lgkmcnt(4)
	v_smfmac_f32_16x16x64_bf16 v[16:19], v[84:87], a[192:199], v4
	ds_read_b128 v[116:119], v3 offset:4096
	v_smfmac_f32_16x16x64_bf16 v[16:19], v[88:91], a[240:247], v4
	ds_read_b128 v[120:123], v3 offset:2048
	v_smfmac_f32_16x16x64_bf16 v[16:19], v[92:95], v[48:55], v4
	ds_read_b128 v[48:51], v6 offset:28672
	ds_read_b128 v[52:55], v6 offset:29696
	v_smfmac_f32_16x16x64_bf16 v[16:19], v[96:99], v[160:167], v4
	ds_read_b128 v[124:127], v3 offset:5120
	v_smfmac_f32_16x16x64_bf16 v[16:19], v[100:103], v[208:215], v4
	s_waitcnt lgkmcnt(5)
	v_smfmac_f32_16x16x64_bf16 v[20:23], v[76:79], v[56:63], v4
	ds_read_b128 v[56:59], v6 offset:16384
	ds_read_b128 v[60:63], v6 offset:17408
	v_smfmac_f32_16x16x64_bf16 v[20:23], v[92:95], v[40:47], v4
	ds_read_b128 v[40:43], v6 offset:30720
	ds_read_b128 v[44:47], v6 offset:31744
	v_smfmac_f32_16x16x64_bf16 v[20:23], v[80:83], a[152:159], v4
	v_add_f32 v16, v16, v18
	v_smfmac_f32_16x16x64_bf16 v[20:23], v[84:87], a[200:207], v4
	v_add_f32 v17, v17, v19
	v_smfmac_f32_16x16x64_bf16 v[20:23], v[88:91], a[248:255], v4
	s_barrier
	v_smfmac_f32_16x16x64_bf16 v[20:23], v[96:99], v[168:175], v4
	ds_read_b128 v[64:67], v3 offset:6144
	v_smfmac_f32_16x16x64_bf16 v[20:23], v[100:103], v[216:223], v4
	s_waitcnt lgkmcnt(3)
	ds_read_b128 v[68:71], v3 offset:9216
	v_smfmac_f32_16x16x64_bf16 v[24:27], v[76:79], v[48:55], v4
	ds_read_b128 v[48:51], v6 offset:18432
	ds_read_b128 v[52:55], v6 offset:19456
	v_smfmac_f32_16x16x64_bf16 v[24:27], v[92:95], v[56:63], v4
	ds_read_b128 v[56:59], v6 offset:32768
	ds_read_b128 v[60:63], v6 offset:33792
	v_smfmac_f32_16x16x64_bf16 v[24:27], v[80:83], a[160:167], v4
	v_add_f32 v20, v20, v22
	v_smfmac_f32_16x16x64_bf16 v[24:27], v[84:87], a[208:215], v4
	v_add_f32 v21, v21, v23
	v_smfmac_f32_16x16x64_bf16 v[24:27], v[88:91], v[128:135], v4
	v_cvt_pk_bf16_f32 v18, v16, v20
	v_smfmac_f32_16x16x64_bf16 v[24:27], v[96:99], v[176:183], v4
	v_cvt_pk_bf16_f32 v19, v17, v21
	ds_write2_b32 v7, v18, v19 offset0:0 offset1:104
	v_smfmac_f32_16x16x64_bf16 v[24:27], v[100:103], v[224:231], v4
	s_waitcnt lgkmcnt(3)
	ds_read_b128 v[72:75], v3 offset:7168
	v_smfmac_f32_16x16x64_bf16 v[28:31], v[76:79], v[40:47], v4
	ds_read_b128 v[40:43], v6 offset:20480
	ds_read_b128 v[44:47], v6 offset:21504
	v_smfmac_f32_16x16x64_bf16 v[28:31], v[92:95], v[48:55], v4
	ds_read_b128 v[48:51], v6 offset:34816
	ds_read_b128 v[52:55], v6 offset:35840
	buffer_load_dword v1, off, s[20:23], s71 sc1
	v_smfmac_f32_16x16x64_bf16 v[28:31], v[80:83], a[168:175], v4
	v_add_f32 v24, v24, v26
	v_smfmac_f32_16x16x64_bf16 v[28:31], v[84:87], a[216:223], v4
	v_add_f32 v25, v25, v27
	v_smfmac_f32_16x16x64_bf16 v[28:31], v[88:91], v[136:143], v4
	v_mov_b64 v[16:17], 0
	v_smfmac_f32_16x16x64_bf16 v[28:31], v[96:99], v[184:191], v4
	v_mov_b64 v[18:19], 0
	v_smfmac_f32_16x16x64_bf16 v[28:31], v[100:103], v[232:239], v4
	s_waitcnt lgkmcnt(2)
	v_mov_b64 v[20:21], 0
	v_smfmac_f32_16x16x64_bf16 v[32:35], v[76:79], v[56:63], v4
	ds_read_b128 v[56:59], v6 offset:22528
	ds_read_b128 v[60:63], v6 offset:23552
	v_smfmac_f32_16x16x64_bf16 v[32:35], v[92:95], v[40:47], v4
	v_smfmac_f32_16x16x64_bf16 v[32:35], v[80:83], a[176:183], v4
	v_add_f32 v28, v28, v30
	v_smfmac_f32_16x16x64_bf16 v[32:35], v[84:87], a[224:231], v4
	v_add_f32 v29, v29, v31
	v_smfmac_f32_16x16x64_bf16 v[32:35], v[88:91], v[144:151], v4
	v_cvt_pk_bf16_f32 v26, v24, v28
	v_smfmac_f32_16x16x64_bf16 v[32:35], v[96:99], v[192:199], v4
	v_cvt_pk_bf16_f32 v27, v25, v29
	ds_write2_b32 v7, v26, v27 offset0:32 offset1:136
	v_smfmac_f32_16x16x64_bf16 v[32:35], v[100:103], v[240:247], v4
	s_waitcnt lgkmcnt(1)
	v_mov_b64 v[22:23], 0
	v_smfmac_f32_16x16x64_bf16 v[36:39], v[76:79], v[48:55], v4
	ds_read_b128 v[76:79], v3 offset:10240
	v_smfmac_f32_16x16x64_bf16 v[36:39], v[92:95], v[56:63], v4
	v_smfmac_f32_16x16x64_bf16 v[36:39], v[80:83], a[184:191], v4
	v_add_f32 v32, v32, v34
	ds_read_b128 v[80:83], v3 offset:8192
	v_smfmac_f32_16x16x64_bf16 v[36:39], v[84:87], a[232:239], v4
	v_add_f32 v33, v33, v35
	ds_read_b128 v[84:87], v3 offset:11264
	v_smfmac_f32_16x16x64_bf16 v[36:39], v[88:91], v[152:159], v4
	v_mov_b64 v[92:93], 0
	v_smfmac_f32_16x16x64_bf16 v[36:39], v[96:99], v[200:207], v4
	v_mov_b64 v[94:95], 0
	s_waitcnt lgkmcnt(3)
	s_barrier
	v_smfmac_f32_16x16x64_bf16 v[36:39], v[100:103], v[248:255], v4
	ds_read_b128 v[24:27], v9 offset:0
	ds_read_b128 v[40:43], v8 offset:0
	ds_read_b128 v[44:47], v8 offset:64
	ds_read_b128 v[28:31], v9 offset:128
	ds_read_b128 v[48:51], v8 offset:128
	ds_read_b128 v[52:55], v8 offset:192
	s_nop 1
	v_pk_add_f32 v[36:37], v[36:37], v[38:39]
	v_cvt_pk_bf16_f32 v34, v32, v36
	v_cvt_pk_bf16_f32 v35, v33, v37
	ds_write2_b32 v7, v34, v35 offset0:64 offset1:168
	s_waitcnt lgkmcnt(0)
	s_barrier
	ds_read_b128 v[88:91], v9 offset:256
	ds_read_b128 v[56:59], v8 offset:256
	ds_read_b128 v[60:63], v8 offset:320
	v_smfmac_f32_16x16x64_bf16 v[16:19], v[24:27], v[104:111], v4
	v_smfmac_f32_16x16x64_bf16 v[20:23], v[24:27], v[64:71], v4
	v_smfmac_f32_16x16x64_bf16 v[92:95], v[24:27], v[40:47], v4
	s_mov_b32 s78, 0x3dd53b94
	v_mov_b32 v3, s41
	v_smfmac_f32_16x16x64_bf16 v[16:19], v[28:31], v[112:119], v4
	v_smfmac_f32_16x16x64_bf16 v[20:23], v[28:31], v[72:79], v4
	v_smfmac_f32_16x16x64_bf16 v[92:95], v[28:31], v[48:55], v4
	s_waitcnt lgkmcnt(2)
	v_smfmac_f32_16x16x64_bf16 v[16:19], v[88:91], v[120:127], v4
	v_smfmac_f32_16x16x64_bf16 v[20:23], v[88:91], v[80:87], v4
	s_waitcnt lgkmcnt(0)
	v_smfmac_f32_16x16x64_bf16 v[92:95], v[88:91], v[56:63], v4
	s_nop 1
	v_add_f32 v32, v16, v18
	s_nop 1
	v_add_f32 v33, v20, v22
	s_nop 1
	v_add_f32 v34, v92, v94
	v_max3_f32 v35, v32, v33, v34
	s_nop 1
	v_max_f32_dpp v35, v35, v35 row_ror:8 row_mask:0xf bank_mask:0xf
	s_nop 1
	v_max_f32_dpp v35, v35, v35 row_ror:4 row_mask:0xf bank_mask:0xf
	s_nop 1
	v_max_f32_dpp v35, v35, v35 row_ror:2 row_mask:0xf bank_mask:0xf
	s_nop 1
	v_max_f32_dpp v35, v35, v35 row_ror:1 row_mask:0xf bank_mask:0xf
	v_mul_f32 v35, 0xbdd53b94, v35
	v_fma_f32 v34, v34, s78, v35
	v_fma_f32 v32, v32, s78, v35
	v_fma_f32 v33, v33, s78, v35
	v_exp_f32 v34, v34
	v_exp_f32 v32, v32
	v_exp_f32 v33, v33
	s_nop 0
	v_add_f32 v35, v32, v33
	v_fmac_f32 v35, 0.5, v34
	v_mov_b32_dpp v36, v34 quad_perm:[1,0,3,2] row_mask:0xf bank_mask:0xf
	s_nop 0
	v_add_f32_dpp v35, v35, v35 row_ror:8 row_mask:0xf bank_mask:0xf
	s_nop 1
	v_add_f32_dpp v35, v35, v35 row_ror:4 row_mask:0xf bank_mask:0xf
	s_nop 1
	v_add_f32_dpp v35, v35, v35 row_ror:2 row_mask:0xf bank_mask:0xf
	s_nop 1
	v_add_f32_dpp v35, v35, v35 row_ror:1 row_mask:0xf bank_mask:0xf
	v_rcp_f32 v35, v35
	s_nop 0
	v_mul_f32 v32, v32, v35
	v_mul_f32 v33, v33, v35
	v_mul_f32 v34, v34, v35
	v_cvt_pk_bf16_f32 v40, v32, v33
	v_mul_f32 v36, v36, v35
	v_cvt_pk_bf16_f32 v44, v34, v36
	v_mov_b32_dpp v41, v40 row_half_mirror row_mask:0xf bank_mask:0xf
	v_mov_b32_dpp v42, v40 row_ror:8 row_mask:0xf bank_mask:0xf
	v_mov_b32_dpp v43, v40 row_mirror row_mask:0xf bank_mask:0xf
	buffer_store_dwordx4 v[40:43], v10, s[20:23], s59 offen sc1
	v_mov_b32_dpp v45, v44 row_half_mirror row_mask:0xf bank_mask:0xf
	v_mov_b32_dpp v46, v44 row_ror:8 row_mask:0xf bank_mask:0xf
	v_mov_b32_dpp v47, v44 row_mirror row_mask:0xf bank_mask:0xf
	buffer_store_dwordx4 v[44:47], v11, s[20:23], s59 offen sc1
	s_memrealtime s[98:99]
	ds_read_b32 v37, v3
	s_waitcnt lgkmcnt(0)
	v_readfirstlane_b32 s70, v37
	s_sub_u32 s18, s60, s35
	s_max_u32 s70, s70, s18
	s_add_u32 s70, s70, s35
	s_cmp_lg_u32 s37, 0
	s_cbranch_scc1 .Lqk_l56
	s_sub_u32 s76, s98, s72
	s_min_u32 s76, s76, 0x3000
	s_pack_ll_b32_b16 s77, s58, s76
	s_and_b32 s18, s19, 255
	s_min_u32 s18, s18, 1
	s_lshl_b32 s18, s18, 31
	s_or_b32 s77, s77, s18
	v_mov_b32 v52, s77
	s_cmp_eq_u32 s60, s70
	s_cselect_b64 exec, 1, 0
	s_mov_b32 s19, 0
	buffer_store_dword v52, off, s[20:23], s38 sc1
	s_mov_b64 exec, -1
.Lqk_l56:
	s_add_u32 s76, s58, 3
	s_and_b32 s76, s76, 7
	s_lshl_b32 s76, s76, 22
	s_add_u32 s76, s76, s46
	v_mov_b64 v[48:49], -1
	v_mov_b64 v[50:51], -1
	s_waitcnt vmcnt(0)
	buffer_store_dwordx4 v[48:51], v10, s[20:23], s76 offen sc1
	buffer_store_dwordx4 v[48:51], v11, s[20:23], s76 offen sc1
	v_readfirstlane_b32 s76, v1
	s_add_u32 s60, s70, s63
	s_and_b32 s77, s76, 1023
	s_add_u32 s60, s60, s77
	s_bfe_u32 s63, s76, 0xa000a
	s_xor_b32 s48, s66, s48
	s_xor_b32 s49, s66, s49
	s_xor_b32 s50, s66, s50
	s_mov_b32 s66, 0
	s_sub_u32 s74, 73728, s74
	s_add_u32 s58, s58, 1
	s_cmp_lt_u32 s58, 64
	s_cbranch_scc1 .Lqk_step50
	s_branch .Lqk_end48
.Lqk_f051:
	s_cmp_eq_u32 s69, 0
	s_cbranch_scc1 .Lqk_f051c
	s_sub_u32 s69, s69, 1
	s_memrealtime s[98:99]
	s_add_u32 s19, s19, 1
	s_mov_b32 s82, 1
	v_mov_b64 v[16:17], 0
	v_mov_b64 v[18:19], 0
	v_mov_b64 v[20:21], 0
	v_mov_b64 v[22:23], 0
	v_mov_b64 v[24:25], 0
	v_mov_b64 v[26:27], 0
	v_mov_b64 v[28:29], 0
	v_mov_b64 v[30:31], 0
	v_mov_b64 v[32:33], 0
	v_mov_b64 v[34:35], 0
	v_mov_b64 v[36:37], 0
	v_mov_b64 v[38:39], 0
	s_waitcnt vmcnt(0) lgkmcnt(0)
.Lqk_c57:
	s_add_u32 s60, s60, 80
	s_sub_u32 s18, s60, s98
	s_cmp_lt_i32 s18, -8
	s_cbranch_scc1 .Lqk_c57
	s_sub_u32 s18, s60, s35
	v_mov_b32 v126, s41
	v_mov_b32 v127, s18
	ds_max_u32 v126, v127
	s_branch .Lqk_b052
.Lqk_b052c:
	s_sub_u32 s18, s98, s60
	s_cmp_gt_i32 s18, 24
	s_cbranch_scc1 .Lqk_b052l
	s_cmp_eq_u32 s82, 2
	s_cbranch_scc1 .Lqk_b052r
.Lqk_b052d:
	s_sub_u32 s18, s60, s98
	s_sub_u32 s18, s18, 3
	s_cmp_lt_u32 s18, 0xfa0
	s_cbranch_scc1 .Lqk_b052e
	s_cmp_lg_u32 s82, 0
	s_branch .Lqk_b052x
.Lqk_b052e:
	s_memrealtime s[98:99]
	s_waitcnt lgkmcnt(0)
	s_branch .Lqk_b052d
.Lqk_b052l:
	s_mov_b32 s72, s98
	s_add_u32 s19, s19, 1
.Lqk_c58:
	s_add_u32 s60, s60, 80
	s_sub_u32 s18, s60, s98
	s_cmp_lt_i32 s18, -8
	s_cbranch_scc1 .Lqk_c58
	s_barrier
	s_sub_u32 s18, s60, s35
	v_mov_b32 v126, s41
	v_mov_b32 v127, s18
	ds_max_u32 v126, v127
	s_branch .Lqk_b052g
.Lqk_tk47:
	s_cmp_lg_u32 s30, 0
	s_cbranch_scc1 .Lqk_l59
	s_waitcnt vmcnt(12)
	v_readfirstlane_b32 s73, v119
	s_mov_b32 s78, 1
.Lqk_f60:
	s_cmp_ge_u32 s73, s78
	s_cbranch_scc1 .Lqk_f61
	s_cmp_eq_u32 s69, 0
	s_cbranch_scc1 .Lqk_f61
	s_sub_u32 s69, s69, 1
	buffer_load_dword v2, off, s[20:23], s39 sc1
	s_waitcnt vmcnt(0)
	v_readfirstlane_b32 s73, v2
	s_sleep 2
	s_branch .Lqk_f60
.Lqk_f61:
	s_add_u32 s80, s40, 0
	buffer_load_dwordx4 v[64:67], v13, s[20:23], s80 offen offset:0 sc1
	buffer_load_dwordx4 v[68:71], v13, s[20:23], s80 offen offset:1024 sc1
	buffer_load_dwordx4 v[72:75], v13, s[20:23], s80 offen offset:2048 sc1
	buffer_load_dwordx4 v[76:79], v13, s[20:23], s80 offen offset:3072 sc1
	s_add_u32 s80, s40, s36
	buffer_load_dwordx4 v[80:83], v13, s[20:23], s80 offen offset:0 sc1
	buffer_load_dwordx4 v[84:87], v13, s[20:23], s80 offen offset:1024 sc1
	buffer_load_dwordx4 v[88:91], v13, s[20:23], s80 offen offset:2048 sc1
	buffer_load_dwordx4 v[92:95], v13, s[20:23], s80 offen offset:3072 sc1
	s_add_u32 s80, s40, 16384
	buffer_load_dwordx4 v[96:99], v13, s[20:23], s80 offen offset:0 sc1
	buffer_load_dwordx4 v[100:103], v13, s[20:23], s80 offen offset:1024 sc1
	buffer_load_dwordx4 v[104:107], v13, s[20:23], s80 offen offset:2048 sc1
	buffer_load_dwordx4 v[108:111], v13, s[20:23], s80 offen offset:3072 sc1
.Lqk_l59:
	v_add_u32 v3, s42, v14
	s_waitcnt vmcnt(11)
	ds_write_b128 v3, v[64:67] offset:0
	s_waitcnt vmcnt(10)
	ds_write_b128 v3, v[68:71] offset:1024
	s_waitcnt vmcnt(9)
	ds_write_b128 v3, v[72:75] offset:2048
	s_waitcnt vmcnt(8)
	ds_write_b128 v3, v[76:79] offset:3072
	s_waitcnt vmcnt(7)
	ds_write_b128 v3, v[80:83] offset:4096
	s_waitcnt vmcnt(6)
	ds_write_b128 v3, v[84:87] offset:5120
	s_waitcnt vmcnt(5)
	ds_write_b128 v3, v[88:91] offset:6144
	s_waitcnt vmcnt(4)
	ds_write_b128 v3, v[92:95] offset:7168
	s_waitcnt vmcnt(3)
	ds_write_b128 v3, v[96:99] offset:8192
	s_waitcnt vmcnt(2)
	ds_write_b128 v3, v[100:103] offset:9216
	s_waitcnt vmcnt(1)
	ds_write_b128 v3, v[104:107] offset:10240
	s_waitcnt vmcnt(0)
	ds_write_b128 v3, v[108:111] offset:11264
	s_waitcnt vmcnt(0)
	s_waitcnt lgkmcnt(0)
	s_barrier
	s_memrealtime s[98:99]
	s_waitcnt lgkmcnt(0)
	s_mov_b32 s58, 0
	s_mov_b32 s74, 0
	s_mov_b32 s75, 0
	s_mov_b32 s19, 0
	s_mov_b32 s63, 128
	s_mov_b32 s65, 72
	s_mov_b32 s69, 0x20000
	s_sub_u32 s76, s98, s35
	s_cmp_gt_u32 s76, 0x10000
	s_cselect_b32 s60, s98, s35
	s_sub_u32 s60, s60, s63
.Lqk_c62:
	s_add_u32 s60, s60, 80
	s_sub_u32 s18, s60, s98
	s_cmp_lt_i32 s18, 0
	s_cbranch_scc1 .Lqk_c62
	s_sub_u32 s18, s60, s35
	v_mov_b32 v126, s41
	v_mov_b32 v127, s18
	ds_max_u32 v126, v127
.Lqk_step63:
	s_add_u32 s76, s58, 0
	s_and_b32 s76, s76, 7
	s_lshl_b32 s76, s76, 22
	s_add_u32 s76, s76, s46
	s_add_u32 s71, s76, 67584
	s_sub_u32 s77, s76, s59
	s_mov_b32 s59, s76
	s_add_u32 s48, s48, s77
	s_add_u32 s49, s49, s77
	s_add_u32 s50, s50, s77
	v_add_u32 v3, s74, v14
	s_add_u32 s76, s58, 2
	s_cmp_ge_u32 s73, s76
	s_cselect_b32 s30, s22, 0
	s_and_b32 s27, s40, 0xffe02000
	s_add_u32 s40, s40, 0x200000
	s_xor_b32 s40, s40, 8192
	s_mov_b32 s24, s40
	s_add_u32 s25, s24, s36
	s_add_u32 s26, s24, 16384
	ds_read_b128 v[40:43], v6 offset:24576
	ds_read_b128 v[44:47], v6 offset:25600
	ds_read_b128 v[48:51], v6 offset:12288
	ds_read_b128 v[52:55], v6 offset:13312
	ds_read_b128 v[56:59], v6 offset:26624
	ds_read_b128 v[60:63], v6 offset:27648
	v_add_u32 v124, s51, v13
	v_add_u32 v125, s52, v13
	v_add_u32 v80, s53, v13
	v_add_u32 v88, s54, v13
	v_add_u32 v96, s55, v13
	v_add_u32 v100, s56, v13
	v_mov_b64 v[16:17], 0
	v_mov_b64 v[18:19], 0
	v_mov_b64 v[20:21], 0
	v_mov_b64 v[22:23], 0
	v_mov_b64 v[24:25], 0
	v_mov_b64 v[26:27], 0
	v_mov_b64 v[28:29], 0
	v_mov_b64 v[30:31], 0
	v_mov_b64 v[32:33], 0
	v_mov_b64 v[34:35], 0
	v_mov_b64 v[36:37], 0
	v_mov_b64 v[38:39], 0
	s_cmp_lg_u32 s37, 1
	s_cselect_b32 s82, 2, 0
	s_memrealtime s[98:99]
	s_waitcnt lgkmcnt(0)
.Lqk_b065:
	s_mov_b32 s72, s60
	s_sub_u32 s18, s60, s98
	s_sub_u32 s18, s18, 8
	s_cmp_gt_u32 s18, 0xfa0
	s_cbranch_scc1 .Lqk_b065c
	s_cmp_eq_u32 s82, 2
	s_cbranch_scc1 .Lqk_b065r
	s_cmp_le_u32 s18, 6
	s_cbranch_scc1 .Lqk_w66b
.Lqk_w66:
	s_memrealtime s[98:99]
	s_waitcnt lgkmcnt(0)
	s_sub_u32 s18, s60, s98
	s_cmp_gt_i32 s18, 14
	s_cbranch_scc1 .Lqk_w66
.Lqk_w66b:
	s_memrealtime s[0:1]
	s_nop 1
	s_memrealtime s[2:3]
	s_nop 1
	s_memrealtime s[4:5]
	s_nop 0
	s_memrealtime s[6:7]
	s_nop 1
	s_memrealtime s[76:77]
	s_nop 1
	s_memrealtime s[78:79]
	s_nop 0
	s_memrealtime s[80:81]
	s_nop 1
	s_memrealtime s[98:99]
	s_lshl_b32 s18, s60, 3
	s_waitcnt lgkmcnt(0)
	s_add_u32 s0, s0, s2
	s_add_u32 s4, s4, s6
	s_add_u32 s76, s76, s78
	s_add_u32 s80, s80, s98
	s_add_u32 s0, s0, s4
	s_add_u32 s76, s76, s80
	s_getpc_b64 s[2:3]
.Lqk_w67:
	s_sub_u32 s18, s0, s18
	s_add_u32 s18, s18, s76
	s_mul_i32 s18, s18, 88
	s_ashr_i32 s18, s18, 8
	s_max_i32 s18, s18, -40
	s_min_i32 s18, s18, -8
	s_lshl2_add_u32 s18, s18, .Lqk_w68-.Lqk_w67+160
	s_add_u32 s2, s2, s18
	s_addc_u32 s3, s3, 0
	s_cmp_lg_u32 s82, 0
	s_setpc_b64 s[2:3]
	.p2align 6
.Lqk_w68:
	s_nop 1
	s_nop 1
	s_nop 1
	s_nop 1
	s_nop 1
	s_nop 1
	s_nop 1
	s_nop 1
	s_nop 1
	s_nop 1
	s_nop 1
	s_nop 1
	s_nop 1
	s_nop 1
	s_nop 1
	s_nop 1
	s_nop 1
	s_nop 1
	s_nop 1
	s_nop 1
	s_nop 1
	s_nop 1
	s_nop 1
	s_nop 1
	s_nop 1
	s_nop 1
	s_nop 1
	s_nop 1
	s_nop 1
	s_nop 1
	s_nop 1
	s_nop 1
.Lqk_b065x:
	s_cbranch_scc1 .Lqk_b065g
.Lqk_b065r:
	s_barrier
.Lqk_b065g:
	buffer_load_dwordx4 v[64:67], v5, s[20:23], s48 offen sc1
	s_nop 3
	buffer_load_dwordx4 v[68:71], v5, s[20:23], s49 offen sc1
	s_nop 3
	buffer_load_dwordx4 v[72:75], v5, s[20:23], s50 offen sc1
	s_waitcnt vmcnt(2)
	v_smfmac_f32_16x16x64_bf16 v[16:19], v[64:67], a[0:7], v4
	ds_write_b128 v124, v[64:67] offset:0
	v_smfmac_f32_16x16x64_bf16 v[20:23], v[64:67], a[8:15], v4
	v_smfmac_f32_16x16x64_bf16 v[24:27], v[64:67], a[16:23], v4
	v_smfmac_f32_16x16x64_bf16 v[28:31], v[64:67], a[24:31], v4
	v_smfmac_f32_16x16x64_bf16 v[32:35], v[64:67], a[32:39], v4
	s_waitcnt vmcnt(1)
	ds_write_b128 v124, v[68:71] offset:1024
	v_smfmac_f32_16x16x64_bf16 v[36:39], v[64:67], a[40:47], v4
	v_smfmac_f32_16x16x64_bf16 v[16:19], v[68:71], a[48:55], v4
	v_smfmac_f32_16x16x64_bf16 v[20:23], v[68:71], a[56:63], v4
	v_smfmac_f32_16x16x64_bf16 v[24:27], v[68:71], a[64:71], v4
	s_waitcnt vmcnt(0)
	ds_write_b128 v125, v[72:75] offset:0
	v_max3_u32 v126, v64, v68, v72
	v_smfmac_f32_16x16x64_bf16 v[28:31], v[68:71], a[72:79], v4
	v_cmp_eq_u32 vcc, -1, v126
	v_smfmac_f32_16x16x64_bf16 v[32:35], v[68:71], a[80:87], v4
	v_smfmac_f32_16x16x64_bf16 v[36:39], v[68:71], a[88:95], v4
	s_cbranch_vccnz .Lqk_f064
.Lqk_f064c:
	s_waitcnt lgkmcnt(0)
	s_barrier
	buffer_load_dwordx4 v[104:107], v13, s[28:31], s24 offen offset:0 sc1
	v_smfmac_f32_16x16x64_bf16 v[16:19], v[72:75], a[96:103], v4
	ds_read_b128 v[76:79], v80 offset:0
	ds_read_b128 v[80:83], v80 offset:1024
	buffer_load_dwordx4 v[108:111], v13, s[28:31], s24 offen offset:1024 sc1
	v_smfmac_f32_16x16x64_bf16 v[20:23], v[72:75], a[104:111], v4
	ds_read_b128 v[84:87], v88 offset:0
	ds_read_b128 v[88:91], v88 offset:1024
	buffer_load_dwordx4 v[112:115], v13, s[28:31], s24 offen offset:2048 sc1
	v_smfmac_f32_16x16x64_bf16 v[24:27], v[72:75], a[112:119], v4
	ds_read_b128 v[92:95], v96 offset:0
	ds_read_b128 v[96:99], v96 offset:1024
	buffer_load_dword v0, v15, s[28:31], s26 offen sc1
	v_smfmac_f32_16x16x64_bf16 v[28:31], v[72:75], a[120:127], v4
	ds_read_b128 v[100:103], v100 offset:0
	buffer_load_dwordx4 v[116:119], v13, s[28:31], s24 offen offset:3072 sc1
	v_smfmac_f32_16x16x64_bf16 v[32:35], v[72:75], a[128:135], v4
	buffer_load_dwordx4 v[120:123], v13, s[28:31], s25 offen offset:0 sc1
	v_smfmac_f32_16x16x64_bf16 v[36:39], v[72:75], a[136:143], v4
	buffer_load_dwordx4 v[124:127], v13, s[28:31], s25 offen offset:1024 sc1
	s_waitcnt lgkmcnt(6)
	v_smfmac_f32_16x16x64_bf16 v[16:19], v[76:79], v[40:47], v4
	ds_read_b128 v[40:43], v6 offset:14336
	ds_read_b128 v[44:47], v6 offset:15360
	buffer_load_dwordx4 v[64:67], v13, s[28:31], s25 offen offset:2048 sc1
	s_waitcnt lgkmcnt(7)
	v_smfmac_f32_16x16x64_bf16 v[16:19], v[80:83], a[144:151], v4
	s_waitcnt lgkmcnt(2)
	buffer_load_dwordx4 v[68:71], v13, s[28:31], s25 offen offset:3072 sc1
	v_smfmac_f32_16x16x64_bf16 v[16:19], v[84:87], a[192:199], v4
	buffer_load_dwordx4 v[72:75], v13, s[28:31], s26 offen offset:0 sc1
	v_smfmac_f32_16x16x64_bf16 v[16:19], v[88:91], a[240:247], v4
	v_smfmac_f32_16x16x64_bf16 v[16:19], v[92:95], v[48:55], v4
	ds_read_b128 v[48:51], v6 offset:28672
	ds_read_b128 v[52:55], v6 offset:29696
	v_smfmac_f32_16x16x64_bf16 v[16:19], v[96:99], v[160:167], v4
	v_smfmac_f32_16x16x64_bf16 v[16:19], v[100:103], v[208:215], v4
	s_waitcnt lgkmcnt(2)
	v_smfmac_f32_16x16x64_bf16 v[20:23], v[76:79], v[56:63], v4
	ds_read_b128 v[56:59], v6 offset:16384
	ds_read_b128 v[60:63], v6 offset:17408
	v_smfmac_f32_16x16x64_bf16 v[20:23], v[92:95], v[40:47], v4
	ds_read_b128 v[40:43], v6 offset:30720
	ds_read_b128 v[44:47], v6 offset:31744
	v_smfmac_f32_16x16x64_bf16 v[20:23], v[80:83], a[152:159], v4
	v_add_f32 v16, v16, v18
	v_smfmac_f32_16x16x64_bf16 v[20:23], v[84:87], a[200:207], v4
	v_add_f32 v17, v17, v19
	v_smfmac_f32_16x16x64_bf16 v[20:23], v[88:91], a[248:255], v4
	s_barrier
	v_smfmac_f32_16x16x64_bf16 v[20:23], v[96:99], v[168:175], v4
	v_smfmac_f32_16x16x64_bf16 v[20:23], v[100:103], v[216:223], v4
	s_waitcnt lgkmcnt(2)
	v_smfmac_f32_16x16x64_bf16 v[24:27], v[76:79], v[48:55], v4
	ds_read_b128 v[48:51], v6 offset:18432
	ds_read_b128 v[52:55], v6 offset:19456
	v_smfmac_f32_16x16x64_bf16 v[24:27], v[92:95], v[56:63], v4
	ds_read_b128 v[56:59], v6 offset:32768
	ds_read_b128 v[60:63], v6 offset:33792
	v_smfmac_f32_16x16x64_bf16 v[24:27], v[80:83], a[160:167], v4
	v_add_f32 v20, v20, v22
	v_smfmac_f32_16x16x64_bf16 v[24:27], v[84:87], a[208:215], v4
	v_add_f32 v21, v21, v23
	v_smfmac_f32_16x16x64_bf16 v[24:27], v[88:91], v[128:135], v4
	v_cvt_pk_bf16_f32 v18, v16, v20
	v_smfmac_f32_16x16x64_bf16 v[24:27], v[96:99], v[176:183], v4
	v_cvt_pk_bf16_f32 v19, v17, v21
	ds_write2_b32 v7, v18, v19 offset0:0 offset1:104
	v_smfmac_f32_16x16x64_bf16 v[24:27], v[100:103], v[224:231], v4
	s_waitcnt lgkmcnt(3)
	v_smfmac_f32_16x16x64_bf16 v[28:31], v[76:79], v[40:47], v4
	ds_read_b128 v[40:43], v6 offset:20480
	ds_read_b128 v[44:47], v6 offset:21504
	v_smfmac_f32_16x16x64_bf16 v[28:31], v[92:95], v[48:55], v4
	ds_read_b128 v[48:51], v6 offset:34816
	ds_read_b128 v[52:55], v6 offset:35840
	buffer_load_dword v1, off, s[20:23], s71 sc1
	v_smfmac_f32_16x16x64_bf16 v[28:31], v[80:83], a[168:175], v4
	v_add_f32 v24, v24, v26
	v_smfmac_f32_16x16x64_bf16 v[28:31], v[84:87], a[216:223], v4
	buffer_load_dword v2, off, s[20:23], s39 sc1
	v_add_f32 v25, v25, v27
	v_smfmac_f32_16x16x64_bf16 v[28:31], v[88:91], v[136:143], v4
	v_smfmac_f32_16x16x64_bf16 v[28:31], v[96:99], v[184:191], v4
	v_smfmac_f32_16x16x64_bf16 v[28:31], v[100:103], v[232:239], v4
	s_waitcnt lgkmcnt(2)
	v_smfmac_f32_16x16x64_bf16 v[32:35], v[76:79], v[56:63], v4
	ds_read_b128 v[56:59], v6 offset:22528
	ds_read_b128 v[60:63], v6 offset:23552
	v_smfmac_f32_16x16x64_bf16 v[32:35], v[92:95], v[40:47], v4
	v_smfmac_f32_16x16x64_bf16 v[32:35], v[80:83], a[176:183], v4
	v_add_f32 v28, v28, v30
	v_smfmac_f32_16x16x64_bf16 v[32:35], v[84:87], a[224:231], v4
	v_add_f32 v29, v29, v31
	v_smfmac_f32_16x16x64_bf16 v[32:35], v[88:91], v[144:151], v4
	v_cvt_pk_bf16_f32 v26, v24, v28
	v_smfmac_f32_16x16x64_bf16 v[32:35], v[96:99], v[192:199], v4
	v_cvt_pk_bf16_f32 v27, v25, v29
	ds_write2_b32 v7, v26, v27 offset0:32 offset1:136
	v_smfmac_f32_16x16x64_bf16 v[32:35], v[100:103], v[240:247], v4
	s_waitcnt lgkmcnt(1)
	v_smfmac_f32_16x16x64_bf16 v[36:39], v[76:79], v[48:55], v4
	buffer_load_dwordx4 v[76:79], v13, s[28:31], s26 offen offset:1024 sc1
	v_smfmac_f32_16x16x64_bf16 v[36:39], v[92:95], v[56:63], v4
	v_smfmac_f32_16x16x64_bf16 v[36:39], v[80:83], a[184:191], v4
	v_add_f32 v32, v32, v34
	buffer_load_dwordx4 v[80:83], v13, s[28:31], s26 offen offset:2048 sc1
	v_smfmac_f32_16x16x64_bf16 v[36:39], v[84:87], a[232:239], v4
	v_add_f32 v33, v33, v35
	buffer_load_dwordx4 v[84:87], v13, s[28:31], s26 offen offset:3072 sc1
	v_smfmac_f32_16x16x64_bf16 v[36:39], v[88:91], v[152:159], v4
	v_smfmac_f32_16x16x64_bf16 v[36:39], v[96:99], v[200:207], v4
	s_waitcnt lgkmcnt(0)
	s_barrier
	v_smfmac_f32_16x16x64_bf16 v[36:39], v[100:103], v[248:255], v4
	s_nop 7
	v_pk_add_f32 v[36:37], v[36:37], v[38:39]
	v_cvt_pk_bf16_f32 v34, v32, v36
	v_cvt_pk_bf16_f32 v35, v33, v37
	ds_write2_b32 v7, v34, v35 offset0:64 offset1:168
	s_waitcnt lgkmcnt(0)
	s_barrier
	v_add_u32 v90, s43, v3
	s_sleep 1
	ds_read_b128 v[40:43], v90 offset:0
	ds_read_b128 v[16:19], v8 offset:0
	ds_read_b128 v[52:55], v90 offset:3072
	ds_read_b128 v[20:23], v8 offset:64
	ds_read_b128 v[44:47], v90 offset:1024
	ds_read_b128 v[24:27], v8 offset:128
	ds_read_b128 v[56:59], v90 offset:4096
	ds_read_b128 v[28:31], v8 offset:192
	ds_read_b128 v[48:51], v90 offset:2048
	ds_read_b128 v[32:35], v8 offset:256
	ds_read_b128 v[60:63], v90 offset:5120
	ds_read_b128 v[36:39], v8 offset:320
	s_waitcnt lgkmcnt(10)
	v_mfma_f32_16x16x32_bf16 v[92:95], v[16:19], v[40:43], 0
	ds_read_b128 v[16:19], v3 offset:0
	s_waitcnt lgkmcnt(9)
	v_mfma_f32_16x16x32_bf16 v[92:95], v[20:23], v[52:55], v[92:95]
	ds_read_b128 v[20:23], v3 offset:1024
	s_waitcnt lgkmcnt(8)
	v_mfma_f32_16x16x32_bf16 v[92:95], v[24:27], v[44:47], v[92:95]
	ds_read_b128 v[24:27], v3 offset:2048
	s_waitcnt lgkmcnt(7)
	v_mfma_f32_16x16x32_bf16 v[92:95], v[28:31], v[56:59], v[92:95]
	ds_read_b128 v[28:31], v3 offset:3072
	s_waitcnt lgkmcnt(6)
	v_mfma_f32_16x16x32_bf16 v[92:95], v[32:35], v[48:51], v[92:95]
	ds_read_b128 v[32:35], v3 offset:4096
	s_waitcnt lgkmcnt(5)
	v_mfma_f32_16x16x32_bf16 v[92:95], v[36:39], v[60:63], v[92:95]
	ds_read_b128 v[36:39], v3 offset:5120
	s_waitcnt lgkmcnt(5)
	v_mfma_f32_16x16x32_bf16 v[96:99], v[16:19], v[40:43], 0
	ds_read_b128 v[16:19], v3 offset:6144
	s_waitcnt lgkmcnt(5)
	v_mfma_f32_16x16x32_bf16 v[96:99], v[20:23], v[44:47], v[96:99]
	ds_read_b128 v[20:23], v3 offset:7168
	s_waitcnt lgkmcnt(5)
	v_mfma_f32_16x16x32_bf16 v[96:99], v[24:27], v[48:51], v[96:99]
	ds_read_b128 v[24:27], v3 offset:8192
	s_waitcnt lgkmcnt(5)
	v_mfma_f32_16x16x32_bf16 v[96:99], v[28:31], v[52:55], v[96:99]
	ds_read_b128 v[28:31], v3 offset:9216
	s_waitcnt lgkmcnt(5)
	v_mfma_f32_16x16x32_bf16 v[96:99], v[32:35], v[56:59], v[96:99]
	ds_read_b128 v[32:35], v3 offset:10240
	s_waitcnt lgkmcnt(5)
	v_mfma_f32_16x16x32_bf16 v[96:99], v[36:39], v[60:63], v[96:99]
	ds_read_b128 v[36:39], v3 offset:11264
	s_waitcnt lgkmcnt(5)
	v_mfma_f32_16x16x32_bf16 v[100:103], v[16:19], v[40:43], 0
	s_waitcnt lgkmcnt(4)
	v_mfma_f32_16x16x32_bf16 v[100:103], v[20:23], v[44:47], v[100:103]
	s_waitcnt lgkmcnt(3)
	v_mfma_f32_16x16x32_bf16 v[100:103], v[24:27], v[48:51], v[100:103]
	s_waitcnt lgkmcnt(2)
	v_mfma_f32_16x16x32_bf16 v[100:103], v[28:31], v[52:55], v[100:103]
	s_waitcnt lgkmcnt(1)
	v_mfma_f32_16x16x32_bf16 v[100:103], v[32:35], v[56:59], v[100:103]
	s_waitcnt lgkmcnt(0)
	v_mfma_f32_16x16x32_bf16 v[100:103], v[36:39], v[60:63], v[100:103]
	s_mov_b32 s78, 0x3dd53b94
	v_mov_b32 v91, 0xceabfb8f
	v_cmp_le_i32 vcc, 0, v9
	v_cmp_le_i32 s[96:97], 1, v9
	v_cmp_le_i32 s[80:81], 2, v9
	v_cmp_le_i32 s[82:83], 3, v9
	v_mul_f32 v16, s78, v96
	s_nop 0
	v_mul_f32 v17, s78, v100
	v_mul_f32 v18, s78, v97
	v_mul_f32 v19, s78, v101
	v_cndmask_b32 v16, v91, v16, vcc
	v_cndmask_b32 v17, v91, v17, s[96:97]
	v_cndmask_b32 v18, v91, v18, s[80:81]
	v_cndmask_b32 v19, v91, v19, s[82:83]
	v_cmp_le_i32 vcc, 4, v9
	v_cmp_le_i32 s[96:97], 5, v9
	v_cmp_le_i32 s[80:81], 6, v9
	v_cmp_le_i32 s[82:83], 7, v9
	v_mul_f32 v20, s78, v98
	v_mul_f32 v21, s78, v102
	v_mul_f32 v22, s78, v99
	v_mul_f32 v23, s78, v103
	v_cndmask_b32 v20, v91, v20, vcc
	v_cndmask_b32 v21, v91, v21, s[96:97]
	v_cndmask_b32 v22, v91, v22, s[80:81]
	v_cndmask_b32 v23, v91, v23, s[82:83]
	v_mul_f32 v24, s78, v92
	v_mul_f32 v25, s78, v93
	v_max3_f32 v26, v16, v17, v18
	v_max3_f32 v27, v19, v20, v21
	v_max3_f32 v28, v22, v23, v24
	v_max3_f32 v26, v26, v27, v25
	v_max_f32 v26, v26, v28
	v_mov_b32 v27, v26
	s_nop 1
	v_permlane16_swap_b32 v26, v27
	v_max_f32 v26, v26, v27
	v_mov_b32 v27, v26
	s_nop 1
	v_permlane32_swap_b32 v26, v27
	v_max_f32 v26, v26, v27
	v_sub_f32 v16, v16, v26
	v_sub_f32 v17, v17, v26
	v_sub_f32 v18, v18, v26
	v_sub_f32 v19, v19, v26
	v_sub_f32 v20, v20, v26
	v_sub_f32 v21, v21, v26
	v_sub_f32 v22, v22, v26
	v_sub_f32 v23, v23, v26
	v_sub_f32 v24, v24, v26
	v_sub_f32 v25, v25, v26
	v_exp_f32 v16, v16
	v_exp_f32 v17, v17
	v_exp_f32 v18, v18
	v_exp_f32 v19, v19
	v_exp_f32 v20, v20
	v_exp_f32 v21, v21
	v_exp_f32 v22, v22
	v_exp_f32 v23, v23
	v_exp_f32 v24, v24
	v_exp_f32 v25, v25
	v_add_f32 v26, v16, v17
	v_add_f32 v27, v18, v19
	v_add_f32 v28, v20, v21
	v_add_f32 v29, v22, v23
	v_add_f32 v30, v24, v25
	v_add_f32 v26, v26, v27
	v_add_f32 v28, v28, v29
	v_add_f32 v26, v26, v30
	v_add_f32 v26, v26, v28
	v_mov_b32 v27, v26
	s_nop 1
	v_permlane16_swap_b32 v26, v27
	v_add_f32 v26, v26, v27
	v_mov_b32 v27, v26
	s_nop 1
	v_permlane32_swap_b32 v26, v27
	v_add_f32 v26, v26, v27
	v_rcp_f32 v26, v26
	s_nop 0
	v_mul_f32 v16, v16, v26
	v_mul_f32 v17, v17, v26
	v_mul_f32 v18, v18, v26
	v_mul_f32 v19, v19, v26
	v_mul_f32 v20, v20, v26
	v_mul_f32 v21, v21, v26
	v_mul_f32 v22, v22, v26
	v_mul_f32 v23, v23, v26
	v_mul_f32 v24, v24, v26
	v_mul_f32 v25, v25, v26
	v_cvt_pk_bf16_f32 v28, v16, v17
	v_cvt_pk_bf16_f32 v29, v18, v19
	v_cvt_pk_bf16_f32 v30, v20, v21
	v_cvt_pk_bf16_f32 v31, v22, v23
	v_cvt_pk_bf16_f32 v32, v24, v25
	buffer_store_dwordx4 v[28:31], v10, s[20:23], s27 offen sc1
	buffer_store_dword v32, v11, s[20:23], s27 offen sc1
	s_waitcnt vmcnt(2)
	s_cmp_lg_u32 s30, 0
	s_cbranch_scc1 .Lqk_l69
	s_cmp_eq_u32 s58, 63
	s_cbranch_scc1 .Lqk_l69
	s_add_u32 s78, s58, 2
.Lqk_f70:
	s_cmp_ge_u32 s73, s78
	s_cbranch_scc1 .Lqk_f71
	s_cmp_eq_u32 s69, 0
	s_cbranch_scc1 .Lqk_f71
	s_sub_u32 s69, s69, 1
	buffer_load_dword v16, off, s[20:23], s39 sc1
	s_waitcnt vmcnt(0)
	v_readfirstlane_b32 s73, v16
	s_sleep 2
	s_branch .Lqk_f70
.Lqk_f71:
	buffer_load_dwordx4 v[104:107], v13, s[20:23], s24 offen offset:0 sc1
	buffer_load_dwordx4 v[108:111], v13, s[20:23], s24 offen offset:1024 sc1
	buffer_load_dwordx4 v[112:115], v13, s[20:23], s24 offen offset:2048 sc1
	buffer_load_dwordx4 v[116:119], v13, s[20:23], s24 offen offset:3072 sc1
	buffer_load_dwordx4 v[120:123], v13, s[20:23], s25 offen offset:0 sc1
	buffer_load_dwordx4 v[124:127], v13, s[20:23], s25 offen offset:1024 sc1
	buffer_load_dwordx4 v[64:67], v13, s[20:23], s25 offen offset:2048 sc1
	buffer_load_dwordx4 v[68:71], v13, s[20:23], s25 offen offset:3072 sc1
	buffer_load_dwordx4 v[72:75], v13, s[20:23], s26 offen offset:0 sc1
	buffer_load_dwordx4 v[76:79], v13, s[20:23], s26 offen offset:1024 sc1
	buffer_load_dwordx4 v[80:83], v13, s[20:23], s26 offen offset:2048 sc1
	buffer_load_dwordx4 v[84:87], v13, s[20:23], s26 offen offset:3072 sc1
	s_waitcnt vmcnt(0)
.Lqk_l69:
	s_sub_u32 s78, 73728, s74
	s_add_u32 s78, s78, s42
	v_add_u32 v90, s78, v14
	v_readfirstlane_b32 s78, v2
	s_max_u32 s73, s73, s78
	v_mov_b32 v35, s41
	ds_read_b32 v34, v35
	ds_write_b128 v90, v[104:107] offset:0
	ds_write_b128 v90, v[108:111] offset:1024
	ds_write_b128 v90, v[112:115] offset:2048
	ds_write_b128 v90, v[116:119] offset:3072
	ds_write_b128 v90, v[120:123] offset:4096
	ds_write_b128 v90, v[124:127] offset:5120
	ds_write_b128 v90, v[64:67] offset:6144
	ds_write_b128 v90, v[68:71] offset:7168
	ds_write_b128 v90, v[72:75] offset:8192
	ds_write_b128 v90, v[76:79] offset:9216
	ds_write_b128 v90, v[80:83] offset:10240
	ds_write_b128 v90, v[84:87] offset:11264
	s_waitcnt lgkmcnt(12)
	v_readfirstlane_b32 s70, v34
	s_sub_u32 s18, s60, s35
	s_max_u32 s70, s70, s18
	s_add_u32 s70, s70, s35
	v_readfirstlane_b32 s76, v1
	s_add_u32 s60, s70, s63
	s_and_b32 s77, s76, 1023
	s_add_u32 s60, s60, s77
	s_bfe_u32 s63, s76, 0xa000a
	s_xor_b32 s48, s66, s48
	s_xor_b32 s49, s66, s49
	s_xor_b32 s50, s66, s50
	s_mov_b32 s66, 0
	s_sub_u32 s74, 73728, s74
	s_add_u32 s58, s58, 1
	s_cmp_lt_u32 s58, 64
	s_cbranch_scc1 .Lqk_step63
	s_branch .Lqk_end48
.Lqk_f064:
	s_cmp_eq_u32 s69, 0
	s_cbranch_scc1 .Lqk_f064c
	s_sub_u32 s69, s69, 1
	s_memrealtime s[98:99]
	s_add_u32 s19, s19, 1
	s_mov_b32 s82, 1
	v_mov_b64 v[16:17], 0
	v_mov_b64 v[18:19], 0
	v_mov_b64 v[20:21], 0
	v_mov_b64 v[22:23], 0
	v_mov_b64 v[24:25], 0
	v_mov_b64 v[26:27], 0
	v_mov_b64 v[28:29], 0
	v_mov_b64 v[30:31], 0
	v_mov_b64 v[32:33], 0
	v_mov_b64 v[34:35], 0
	v_mov_b64 v[36:37], 0
	v_mov_b64 v[38:39], 0
	s_waitcnt vmcnt(0) lgkmcnt(0)
.Lqk_c72:
	s_add_u32 s60, s60, 80
	s_sub_u32 s18, s60, s98
	s_cmp_lt_i32 s18, -8
	s_cbranch_scc1 .Lqk_c72
	s_sub_u32 s18, s60, s35
	v_mov_b32 v126, s41
	v_mov_b32 v127, s18
	ds_max_u32 v126, v127
	s_branch .Lqk_b065
.Lqk_b065c:
	s_sub_u32 s18, s98, s60
	s_cmp_gt_i32 s18, 24
	s_cbranch_scc1 .Lqk_b065l
	s_cmp_eq_u32 s82, 2
	s_cbranch_scc1 .Lqk_b065r
.Lqk_b065d:
	s_sub_u32 s18, s60, s98
	s_sub_u32 s18, s18, 3
	s_cmp_lt_u32 s18, 0xfa0
	s_cbranch_scc1 .Lqk_b065e
	s_cmp_lg_u32 s82, 0
	s_branch .Lqk_b065x
.Lqk_b065e:
	s_memrealtime s[98:99]
	s_waitcnt lgkmcnt(0)
	s_branch .Lqk_b065d
.Lqk_b065l:
	s_mov_b32 s72, s98
	s_add_u32 s19, s19, 1
.Lqk_c73:
	s_add_u32 s60, s60, 80
	s_sub_u32 s18, s60, s98
	s_cmp_lt_i32 s18, -8
	s_cbranch_scc1 .Lqk_c73
	s_barrier
	s_sub_u32 s18, s60, s35
	v_mov_b32 v126, s41
	v_mov_b32 v127, s18
	ds_max_u32 v126, v127
	s_branch .Lqk_b065g
.Lqk_end48:
	s_endpgm
.Lqk_x1:
	s_waitcnt vmcnt(0) lgkmcnt(0)
	s_mov_b32 s47, -1
	s_mov_b32 m0, s19
)ASM" : "+{v[0:15]}"(vi), "+{s[32:47]}"(si), "+{s[24:27]}"(rw) : "{s[20:23]}"(rs) : "a0", "a1", "a2", "a3", "a4", "a5", "a6", "a7", "a8", "a9", "a10", "a11", "a12", "a13", "a14", "a15", "a16", "a17", "a18", "a19", "a20", "a21", "a22", "a23", "a24", "a25", "a26", "a27", "a28", "a29", "a30", "a31", "a32", "a33", "a34", "a35", "a36", "a37", "a38", "a39", "a40", "a41", "a42", "a43", "a44", "a45", "a46", "a47", "a48", "a49", "a50", "a51", "a52", "a53", "a54", "a55", "a56", "a57", "a58", "a59", "a60", "a61", "a62", "a63", "a64", "a65", "a66", "a67", "a68", "a69", "a70", "a71", "a72", "a73", "a74", "a75", "a76", "a77", "a78", "a79", "a80", "a81", "a82", "a83", "a84", "a85", "a86", "a87", "a88", "a89", "a90", "a91", "a92", "a93", "a94", "a95", "a96", "a97", "a98", "a99", "a100", "a101", "a102", "a103", "a104", "a105", "a106", "a107", "a108", "a109", "a110", "a111", "a112", "a113", "a114", "a115", "a116", "a117", "a118", "a119", "a120", "a121", "a122", "a123", "a124", "a125", "a126", "a127", "a128", "a129", "a130", "a131", "a132", "a133", "a134", "a135", "a136", "a137", "a138", "a139", "a140", "a141", "a142", "a143", "a144", "a145", "a146", "a147", "a148", "a149", "a150", "a151", "a152", "a153", "a154", "a155", "a156", "a157", "a158", "a159", "a160", "a161", "a162", "a163", "a164", "a165", "a166", "a167", "a168", "a169", "a170", "a171", "a172", "a173", "a174", "a175", "a176", "a177", "a178", "a179", "a180", "a181", "a182", "a183", "a184", "a185", "a186", "a187", "a188", "a189", "a190", "a191", "a192", "a193", "a194", "a195", "a196", "a197", "a198", "a199", "a200", "a201", "a202", "a203", "a204", "a205", "a206", "a207", "a208", "a209", "a210", "a211", "a212", "a213", "a214", "a215", "a216", "a217", "a218", "a219", "a220", "a221", "a222", "a223", "a224", "a225", "a226", "a227", "a228", "a229", "a230", "a231", "a232", "a233", "a234", "a235", "a236", "a237", "a238", "a239", "a240", "a241", "a242", "a243", "a244", "a245", "a246", "a247", "a248", "a249", "a250", "a251", "a252", "a253", "a254", "a255", "s18", "s19", "s28", "s29", "s30", "s31", "s48", "s49", "s50", "s51", "s52", "s53", "s54", "s55", "s56", "s58", "s59", "s60", "s63", "s64", "s65", "s66", "s67", "s69", "s70", "s71", "s72", "s73", "s74", "s75", "s76", "s77", "s78", "s79", "s80", "s81", "s82", "s83", "s96", "s97", "s98", "s99", "v16", "v17", "v18", "v19", "v20", "v21", "v22", "v23", "v24", "v25", "v26", "v27", "v28", "v29", "v30", "v31", "v32", "v33", "v34", "v35", "v36", "v37", "v38", "v39", "v40", "v41", "v42", "v43", "v44", "v45", "v46", "v47", "v48", "v49", "v50", "v51", "v52", "v53", "v54", "v55", "v56", "v57", "v58", "v59", "v60", "v61", "v62", "v63", "v64", "v65", "v66", "v67", "v68", "v69", "v70", "v71", "v72", "v73", "v74", "v75", "v76", "v77", "v78", "v79", "v80", "v81", "v82", "v83", "v84", "v85", "v86", "v87", "v88", "v89", "v90", "v91", "v92", "v93", "v94", "v95", "v96", "v97", "v98", "v99", "v100", "v101", "v102", "v103", "v104", "v105", "v106", "v107", "v108", "v109", "v110", "v111", "v112", "v113", "v114", "v115", "v116", "v117", "v118", "v119", "v120", "v121", "v122", "v123", "v124", "v125", "v126", "v127", "v128", "v129", "v130", "v131", "v132", "v133", "v134", "v135", "v136", "v137", "v138", "v139", "v140", "v141", "v142", "v143", "v144", "v145", "v146", "v147", "v148", "v149", "v150", "v151", "v152", "v153", "v154", "v155", "v156", "v157", "v158", "v159", "v160", "v161", "v162", "v163", "v164", "v165", "v166", "v167", "v168", "v169", "v170", "v171", "v172", "v173", "v174", "v175", "v176", "v177", "v178", "v179", "v180", "v181", "v182", "v183", "v184", "v185", "v186", "v187", "v188", "v189", "v190", "v191", "v192", "v193", "v194", "v195", "v196", "v197", "v198", "v199", "v200", "v201", "v202", "v203", "v204", "v205", "v206", "v207", "v208", "v209", "v210", "v211", "v212", "v213", "v214", "v215", "v216", "v217", "v218", "v219", "v220", "v221", "v222", "v223", "v224", "v225", "v226", "v227", "v228", "v229", "v230", "v231", "v232", "v233", "v234", "v235", "v236", "v237", "v238", "v239", "v240", "v241", "v242", "v243", "v244", "v245", "v246", "v247", "v248", "v249", "v250", "v251", "v252", "v253", "v254", "v255", "vcc", "scc", "memory");
}

DI void role_vo(KArgs& a, const int h, const unsigned hp, ldsp lds, const int lane, const int wv) {
  const int g = lane >> 4, c = lane & 15, q = c >> 2, p = c & 3;
  const int half = (c >> 1) & 1, arow = 2 * (c >> 2) + (c & 1);
  const unsigned sd = 0;
  const u32x2 rg = rings(sd);
  const unsigned reg = la(lds) + wv * WREG, vimg = la(lds) + OFF_IMG;
  const unsigned yh = (h >> 4) * 16384 + (h & 15) * 512 + (g & 1) * 256 + c * 16;
  u32x16 vi = (u32x16)0u, vj = (u32x16)0u, si = (u32x16)0u;
  vi[4] = half ? 0xEEEE : 0x4444;
  vi[5] = g * 256 + arow * 32 + half * 16;
  vi[6] = reg + lane * 16;
  vi[7] = vimg + 2 * g * VRS + wv * 64 + c * 4;
  vi[8] = vimg + 2 * g * VRS + 256 + wv * 32 + c * 2;
  vi[9] = vimg + (arow ^ (wv & 1) * 4) * VRS + g * 16;
  vi[10] = (5 * wv + (g >> 1)) * 32768 + yh;
  vi[11] = g < 2 ? (5 * wv + 4) * 32768 + yh : OOB;
  vi[12] = lane * 128;
  vi[13] = U_RM + (lane < 40 ? lane : 0) * 128;
  vj[0] = wsrc(lane, 16 * wv, h >> 3); vj[1] = wsrc(lane, 16 * wv + 1, h >> 3);
  vj[2] = la(lds) + wblk(lane, 3 * wv); vj[13] = la(lds) + wblk(lane, 3 * wv + 1); vj[14] = la(lds) + wblk(lane, 3 * wv + 2);
#pragma unroll
  for (int r = 0; r < 5; ++r) {
    const unsigned L = r * 64 + lane, ps = L / 80, o = (L % 80 ^ ps * 4) * 16;
    vj[3 + r] = (8 * (wv & 1) + 4 * (wv >> 1) + 16 * (ps & 1) + (ps & 2)) * 1280 + o; vj[8 + r] = (48 * (wv & 1) + 8 * (wv >> 1) + (ps & 1) + 2 * (ps & 2)) * 1280 + o;
  }
#pragma unroll
  for (int t = 0; t < 4; ++t) vi[t] = la(lds) + 2 * (g >> 1) * WREG + (2 * (g & 1) + (q & 1)) * 1280 + ((5 * wv + t) ^ (2 * (g & 1) + (q & 1))) * 64 + (q >> 1) * 32 + p * 8;
  si[0] = h * 384; si[1] = reg; si[2] = opq(wv); si[3] = opq(hp); si[4] = rg.y; si[5] = opq(wv);
  si[6] = rg.x + U_QM + (32 + h) * 128; si[7] = wv * 320; si[9] = vimg + 16 * VRS; si[12] = la(lds) + 24576; si[13] = qb(h >> 3); si[14] = rg.x; si[15] = dbgo(32 + h, wv);
#pragma unroll
  for (int i = 0; i < 16; ++i) si[i] = rfl(si[i]);
  const u32x4 rs = rsw(a.ws, WS_RANGE), rv = rsw(a.Wv, 640u * 12288u), ro = rsw(a.Wo + (size_t)h * 192 * 640, 192u * 1280u);
  asm volatile(R"ASM(
	s_movk_i32 s78, 1024
	s_mov_b32 s79, 196608
	s_mov_b32 s19, m0
	s_mov_b32 s18, 12566528
	s_mov_b32 s69, 0x20000
	buffer_load_dword v109, off, s[20:23], s18 offset:3968 sc1
	v_lshrrev_b32 v112, 3, v12
	s_add_u32 m0, s33, 0
	s_nop 0
	buffer_load_dwordx4 v16, s[24:27], s32 offen offset:0 nt lds
	s_add_u32 m0, s33, 1920
	s_nop 0
	buffer_load_dwordx4 v16, s[24:27], s32 offen offset:128 nt lds
	s_add_u32 m0, s33, 3840
	s_nop 0
	buffer_load_dwordx4 v16, s[24:27], s32 offen offset:256 nt lds
	s_add_u32 m0, s33, 1024
	s_nop 0
	buffer_load_dwordx4 v17, s[24:27], s32 offen offset:0 nt lds
	s_add_u32 m0, s33, 2944
	s_nop 0
	buffer_load_dwordx4 v17, s[24:27], s32 offen offset:128 nt lds
	s_add_u32 m0, s33, 4864
	s_nop 0
	buffer_load_dwordx4 v17, s[24:27], s32 offen offset:256 nt lds
	s_add_u32 s32, s32, s79
	s_add_u32 m0, s33, 6144
	s_nop 0
	buffer_load_dwordx4 v16, s[24:27], s32 offen offset:0 nt lds
	s_add_u32 m0, s33, 8064
	s_nop 0
	buffer_load_dwordx4 v16, s[24:27], s32 offen offset:128 nt lds
	s_add_u32 m0, s33, 9984
	s_nop 0
	buffer_load_dwordx4 v16, s[24:27], s32 offen offset:256 nt lds
	s_add_u32 m0, s33, 7168
	s_nop 0
	buffer_load_dwordx4 v17, s[24:27], s32 offen offset:0 nt lds
	s_add_u32 m0, s33, 9088
	s_nop 0
	buffer_load_dwordx4 v17, s[24:27], s32 offen offset:128 nt lds
	s_add_u32 m0, s33, 11008
	s_nop 0
	buffer_load_dwordx4 v17, s[24:27], s32 offen offset:256 nt lds
	s_add_u32 s32, s32, 1376256
	s_waitcnt vmcnt(12)
	v_readfirstlane_b32 s70, v109
	s_and_b32 s18, s35, 255
	s_add_u32 s70, s70, 0x9e377900
	s_andn2_b32 s70, s70, 255
	s_or_b32 s70, s70, s18
	s_lshr_b32 s64, s35, 8
	s_and_b32 s71, s64, 1024
	s_add_u32 s71, s71, 12566528
	s_add_u32 s64, s64, 12566528
	v_mov_b32 v109, 16
	v_xor_b32 v109, s70, v109
	s_bitcmp0_b32 s64, 11
	s_cselect_b64 exec, 1, 0
	buffer_store_dword v109, off, s[20:23], s64 sc1
	s_mov_b64 exec, -1
	s_add_u32 m0, s33, 12288
	s_nop 0
	buffer_load_dwordx4 v16, s[24:27], s32 offen offset:0 nt lds
	s_add_u32 m0, s33, 14208
	s_nop 0
	buffer_load_dwordx4 v16, s[24:27], s32 offen offset:128 nt lds
	s_add_u32 m0, s33, 16128
	s_nop 0
	buffer_load_dwordx4 v16, s[24:27], s32 offen offset:256 nt lds
	s_add_u32 m0, s33, 13312
	s_nop 0
	buffer_load_dwordx4 v17, s[24:27], s32 offen offset:0 nt lds
	s_add_u32 m0, s33, 15232
	s_nop 0
	buffer_load_dwordx4 v17, s[24:27], s32 offen offset:128 nt lds
	s_add_u32 m0, s33, 17152
	s_nop 0
	buffer_load_dwordx4 v17, s[24:27], s32 offen offset:256 nt lds
	s_add_u32 s32, s32, s79
	s_add_u32 m0, s33, 18432
	s_nop 0
	buffer_load_dwordx4 v16, s[24:27], s32 offen offset:0 nt lds
	s_add_u32 m0, s33, 20352
	s_nop 0
	buffer_load_dwordx4 v16, s[24:27], s32 offen offset:128 nt lds
	s_add_u32 m0, s33, 22272
	s_nop 0
	buffer_load_dwordx4 v16, s[24:27], s32 offen offset:256 nt lds
	s_add_u32 m0, s33, 19456
	s_nop 0
	buffer_load_dwordx4 v17, s[24:27], s32 offen offset:0 nt lds
	s_add_u32 m0, s33, 21376
	s_nop 0
	buffer_load_dwordx4 v17, s[24:27], s32 offen offset:128 nt lds
	s_add_u32 m0, s33, 23296
	s_nop 0
	buffer_load_dwordx4 v17, s[24:27], s32 offen offset:256 nt lds
	s_add_u32 s32, s32, 1376256
	s_add_u32 m0, s33, 24576
	s_nop 0
	buffer_load_dwordx4 v16, s[24:27], s32 offen offset:0 nt lds
	s_add_u32 m0, s33, 26496
	s_nop 0
	buffer_load_dwordx4 v16, s[24:27], s32 offen offset:128 nt lds
	s_add_u32 m0, s33, 28416
	s_nop 0
	buffer_load_dwordx4 v16, s[24:27], s32 offen offset:256 nt lds
	s_add_u32 m0, s33, 25600
	s_nop 0
	buffer_load_dwordx4 v17, s[24:27], s32 offen offset:0 nt lds
	s_add_u32 m0, s33, 27520
	s_nop 0
	buffer_load_dwordx4 v17, s[24:27], s32 offen offset:128 nt lds
	s_add_u32 m0, s33, 29440
	s_nop 0
	buffer_load_dwordx4 v17, s[24:27], s32 offen offset:256 nt lds
	s_add_u32 s32, s32, s79
	s_add_u32 m0, s33, 30720
	s_nop 0
	buffer_load_dwordx4 v16, s[24:27], s32 offen offset:0 nt lds
	s_add_u32 m0, s33, 32640
	s_nop 0
	buffer_load_dwordx4 v16, s[24:27], s32 offen offset:128 nt lds
	s_add_u32 m0, s33, 34560
	s_nop 0
	buffer_load_dwordx4 v16, s[24:27], s32 offen offset:256 nt lds
	s_add_u32 m0, s33, 31744
	s_nop 0
	buffer_load_dwordx4 v17, s[24:27], s32 offen offset:0 nt lds
	s_add_u32 m0, s33, 33664
	s_nop 0
	buffer_load_dwordx4 v17, s[24:27], s32 offen offset:128 nt lds
	s_add_u32 m0, s33, 35584
	s_nop 0
	buffer_load_dwordx4 v17, s[24:27], s32 offen offset:256 nt lds
	s_add_u32 s32, s32, 1376256
	v_add_u32 v32, 73728, v18
	v_add_u32 v33, 73728, v29
	v_add_u32 v34, 73728, v30
	s_waitcnt vmcnt(24)
	s_barrier
	s_cmp_ge_u32 s37, 2
	s_cbranch_scc1 .Lvo_v4
	s_cmp_eq_u32 s37, 1
	s_cbranch_scc1 .Lvo_v3
	ds_read_b64_tr_b16 a[0:1], v18 offset:0
	ds_read_b64_tr_b16 a[2:3], v18 offset:1024
	ds_read_b64_tr_b16 a[4:5], v18 offset:36864
	ds_read_b64_tr_b16 a[6:7], v18 offset:37888
	ds_read_b64_tr_b16 a[8:9], v29 offset:0
	ds_read_b64_tr_b16 a[10:11], v29 offset:1024
	ds_read_b64_tr_b16 a[12:13], v29 offset:36864
	ds_read_b64_tr_b16 a[14:15], v29 offset:37888
	ds_read_b64_tr_b16 a[16:17], v30 offset:0
	ds_read_b64_tr_b16 a[18:19], v30 offset:1024
	ds_read_b64_tr_b16 a[20:21], v30 offset:36864
	ds_read_b64_tr_b16 a[22:23], v30 offset:37888
	ds_read_b64_tr_b16 a[24:25], v32 offset:0
	ds_read_b64_tr_b16 a[26:27], v32 offset:1024
	ds_read_b64_tr_b16 a[28:29], v32 offset:36864
	ds_read_b64_tr_b16 a[30:31], v32 offset:37888
	ds_read_b64_tr_b16 a[32:33], v33 offset:0
	ds_read_b64_tr_b16 a[34:35], v33 offset:1024
	ds_read_b64_tr_b16 a[36:37], v33 offset:36864
	ds_read_b64_tr_b16 a[38:39], v33 offset:37888
	ds_read_b64_tr_b16 a[40:41], v34 offset:0
	ds_read_b64_tr_b16 a[42:43], v34 offset:1024
	ds_read_b64_tr_b16 a[44:45], v34 offset:36864
	ds_read_b64_tr_b16 a[46:47], v34 offset:37888
	s_branch .Lvo_v2
.Lvo_v3:
	ds_read_b64_tr_b16 v[64:65], v18 offset:0
	ds_read_b64_tr_b16 v[66:67], v18 offset:1024
	ds_read_b64_tr_b16 v[68:69], v18 offset:36864
	ds_read_b64_tr_b16 v[70:71], v18 offset:37888
	ds_read_b64_tr_b16 v[72:73], v29 offset:0
	ds_read_b64_tr_b16 v[74:75], v29 offset:1024
	ds_read_b64_tr_b16 v[76:77], v29 offset:36864
	ds_read_b64_tr_b16 v[78:79], v29 offset:37888
	ds_read_b64_tr_b16 v[80:81], v30 offset:0
	ds_read_b64_tr_b16 v[82:83], v30 offset:1024
	ds_read_b64_tr_b16 v[84:85], v30 offset:36864
	ds_read_b64_tr_b16 v[86:87], v30 offset:37888
	ds_read_b64_tr_b16 a[144:145], v32 offset:0
	ds_read_b64_tr_b16 a[146:147], v32 offset:1024
	ds_read_b64_tr_b16 a[148:149], v32 offset:36864
	ds_read_b64_tr_b16 a[150:151], v32 offset:37888
	ds_read_b64_tr_b16 a[152:153], v33 offset:0
	ds_read_b64_tr_b16 a[154:155], v33 offset:1024
	ds_read_b64_tr_b16 a[156:157], v33 offset:36864
	ds_read_b64_tr_b16 a[158:159], v33 offset:37888
	ds_read_b64_tr_b16 a[160:161], v34 offset:0
	ds_read_b64_tr_b16 a[162:163], v34 offset:1024
	ds_read_b64_tr_b16 a[164:165], v34 offset:36864
	ds_read_b64_tr_b16 a[166:167], v34 offset:37888
	s_branch .Lvo_v2
.Lvo_v4:
	s_cmp_eq_u32 s37, 3
	s_cbranch_scc1 .Lvo_v5
	ds_read_b64_tr_b16 a[96:97], v18 offset:0
	ds_read_b64_tr_b16 a[98:99], v18 offset:1024
	ds_read_b64_tr_b16 a[100:101], v18 offset:36864
	ds_read_b64_tr_b16 a[102:103], v18 offset:37888
	ds_read_b64_tr_b16 a[104:105], v29 offset:0
	ds_read_b64_tr_b16 a[106:107], v29 offset:1024
	ds_read_b64_tr_b16 a[108:109], v29 offset:36864
	ds_read_b64_tr_b16 a[110:111], v29 offset:37888
	ds_read_b64_tr_b16 a[112:113], v30 offset:0
	ds_read_b64_tr_b16 a[114:115], v30 offset:1024
	ds_read_b64_tr_b16 a[116:117], v30 offset:36864
	ds_read_b64_tr_b16 a[118:119], v30 offset:37888
	ds_read_b64_tr_b16 a[120:121], v32 offset:0
	ds_read_b64_tr_b16 a[122:123], v32 offset:1024
	ds_read_b64_tr_b16 a[124:125], v32 offset:36864
	ds_read_b64_tr_b16 a[126:127], v32 offset:37888
	ds_read_b64_tr_b16 a[128:129], v33 offset:0
	ds_read_b64_tr_b16 a[130:131], v33 offset:1024
	ds_read_b64_tr_b16 a[132:133], v33 offset:36864
	ds_read_b64_tr_b16 a[134:135], v33 offset:37888
	ds_read_b64_tr_b16 a[136:137], v34 offset:0
	ds_read_b64_tr_b16 a[138:139], v34 offset:1024
	ds_read_b64_tr_b16 a[140:141], v34 offset:36864
	ds_read_b64_tr_b16 a[142:143], v34 offset:37888
	s_branch .Lvo_v2
.Lvo_v5:
	ds_read_b64_tr_b16 v[40:41], v18 offset:0
	ds_read_b64_tr_b16 v[42:43], v18 offset:1024
	ds_read_b64_tr_b16 v[44:45], v18 offset:36864
	ds_read_b64_tr_b16 v[46:47], v18 offset:37888
	ds_read_b64_tr_b16 v[48:49], v29 offset:0
	ds_read_b64_tr_b16 v[50:51], v29 offset:1024
	ds_read_b64_tr_b16 v[52:53], v29 offset:36864
	ds_read_b64_tr_b16 v[54:55], v29 offset:37888
	ds_read_b64_tr_b16 v[56:57], v30 offset:0
	ds_read_b64_tr_b16 v[58:59], v30 offset:1024
	ds_read_b64_tr_b16 v[60:61], v30 offset:36864
	ds_read_b64_tr_b16 v[62:63], v30 offset:37888
	ds_read_b64_tr_b16 a[72:73], v32 offset:0
	ds_read_b64_tr_b16 a[74:75], v32 offset:1024
	ds_read_b64_tr_b16 a[76:77], v32 offset:36864
	ds_read_b64_tr_b16 a[78:79], v32 offset:37888
	ds_read_b64_tr_b16 a[80:81], v33 offset:0
	ds_read_b64_tr_b16 a[82:83], v33 offset:1024
	ds_read_b64_tr_b16 a[84:85], v33 offset:36864
	ds_read_b64_tr_b16 a[86:87], v33 offset:37888
	ds_read_b64_tr_b16 a[88:89], v34 offset:0
	ds_read_b64_tr_b16 a[90:91], v34 offset:1024
	ds_read_b64_tr_b16 a[92:93], v34 offset:36864
	ds_read_b64_tr_b16 a[94:95], v34 offset:37888
.Lvo_v2:
	s_waitcnt lgkmcnt(0)
	s_barrier
	s_add_u32 m0, s33, 0
	s_nop 0
	buffer_load_dwordx4 v16, s[24:27], s32 offen offset:0 nt lds
	s_add_u32 m0, s33, 1920
	s_nop 0
	buffer_load_dwordx4 v16, s[24:27], s32 offen offset:128 nt lds
	s_add_u32 m0, s33, 3840
	s_nop 0
	buffer_load_dwordx4 v16, s[24:27], s32 offen offset:256 nt lds
	s_add_u32 m0, s33, 1024
	s_nop 0
	buffer_load_dwordx4 v17, s[24:27], s32 offen offset:0 nt lds
	s_add_u32 m0, s33, 2944
	s_nop 0
	buffer_load_dwordx4 v17, s[24:27], s32 offen offset:128 nt lds
	s_add_u32 m0, s33, 4864
	s_nop 0
	buffer_load_dwordx4 v17, s[24:27], s32 offen offset:256 nt lds
	s_add_u32 s32, s32, s79
	s_add_u32 m0, s33, 6144
	s_nop 0
	buffer_load_dwordx4 v16, s[24:27], s32 offen offset:0 nt lds
	s_add_u32 m0, s33, 8064
	s_nop 0
	buffer_load_dwordx4 v16, s[24:27], s32 offen offset:128 nt lds
	s_add_u32 m0, s33, 9984
	s_nop 0
	buffer_load_dwordx4 v16, s[24:27], s32 offen offset:256 nt lds
	s_add_u32 m0, s33, 7168
	s_nop 0
	buffer_load_dwordx4 v17, s[24:27], s32 offen offset:0 nt lds
	s_add_u32 m0, s33, 9088
	s_nop 0
	buffer_load_dwordx4 v17, s[24:27], s32 offen offset:128 nt lds
	s_add_u32 m0, s33, 11008
	s_nop 0
	buffer_load_dwordx4 v17, s[24:27], s32 offen offset:256 nt lds
	s_add_u32 s32, s32, 1376256
	s_waitcnt vmcnt(24)
	s_barrier
	s_cmp_ge_u32 s37, 2
	s_cbranch_scc1 .Lvo_v8
	s_cmp_eq_u32 s37, 1
	s_cbranch_scc1 .Lvo_v7
	ds_read_b64_tr_b16 v[40:41], v18 offset:12288
	ds_read_b64_tr_b16 v[42:43], v18 offset:13312
	ds_read_b64_tr_b16 v[44:45], v18 offset:49152
	ds_read_b64_tr_b16 v[46:47], v18 offset:50176
	ds_read_b64_tr_b16 v[48:49], v29 offset:12288
	ds_read_b64_tr_b16 v[50:51], v29 offset:13312
	ds_read_b64_tr_b16 v[52:53], v29 offset:49152
	ds_read_b64_tr_b16 v[54:55], v29 offset:50176
	ds_read_b64_tr_b16 v[56:57], v30 offset:12288
	ds_read_b64_tr_b16 v[58:59], v30 offset:13312
	ds_read_b64_tr_b16 v[60:61], v30 offset:49152
	ds_read_b64_tr_b16 v[62:63], v30 offset:50176
	ds_read_b64_tr_b16 a[72:73], v32 offset:12288
	ds_read_b64_tr_b16 a[74:75], v32 offset:13312
	ds_read_b64_tr_b16 a[76:77], v32 offset:49152
	ds_read_b64_tr_b16 a[78:79], v32 offset:50176
	ds_read_b64_tr_b16 a[80:81], v33 offset:12288
	ds_read_b64_tr_b16 a[82:83], v33 offset:13312
	ds_read_b64_tr_b16 a[84:85], v33 offset:49152
	ds_read_b64_tr_b16 a[86:87], v33 offset:50176
	ds_read_b64_tr_b16 a[88:89], v34 offset:12288
	ds_read_b64_tr_b16 a[90:91], v34 offset:13312
	ds_read_b64_tr_b16 a[92:93], v34 offset:49152
	ds_read_b64_tr_b16 a[94:95], v34 offset:50176
	s_branch .Lvo_v6
.Lvo_v7:
	ds_read_b64_tr_b16 a[0:1], v18 offset:12288
	ds_read_b64_tr_b16 a[2:3], v18 offset:13312
	ds_read_b64_tr_b16 a[4:5], v18 offset:49152
	ds_read_b64_tr_b16 a[6:7], v18 offset:50176
	ds_read_b64_tr_b16 a[8:9], v29 offset:12288
	ds_read_b64_tr_b16 a[10:11], v29 offset:13312
	ds_read_b64_tr_b16 a[12:13], v29 offset:49152
	ds_read_b64_tr_b16 a[14:15], v29 offset:50176
	ds_read_b64_tr_b16 a[16:17], v30 offset:12288
	ds_read_b64_tr_b16 a[18:19], v30 offset:13312
	ds_read_b64_tr_b16 a[20:21], v30 offset:49152
	ds_read_b64_tr_b16 a[22:23], v30 offset:50176
	ds_read_b64_tr_b16 a[24:25], v32 offset:12288
	ds_read_b64_tr_b16 a[26:27], v32 offset:13312
	ds_read_b64_tr_b16 a[28:29], v32 offset:49152
	ds_read_b64_tr_b16 a[30:31], v32 offset:50176
	ds_read_b64_tr_b16 a[32:33], v33 offset:12288
	ds_read_b64_tr_b16 a[34:35], v33 offset:13312
	ds_read_b64_tr_b16 a[36:37], v33 offset:49152
	ds_read_b64_tr_b16 a[38:39], v33 offset:50176
	ds_read_b64_tr_b16 a[40:41], v34 offset:12288
	ds_read_b64_tr_b16 a[42:43], v34 offset:13312
	ds_read_b64_tr_b16 a[44:45], v34 offset:49152
	ds_read_b64_tr_b16 a[46:47], v34 offset:50176
	s_branch .Lvo_v6
.Lvo_v8:
	s_cmp_eq_u32 s37, 3
	s_cbranch_scc1 .Lvo_v9
	ds_read_b64_tr_b16 v[64:65], v18 offset:12288
	ds_read_b64_tr_b16 v[66:67], v18 offset:13312
	ds_read_b64_tr_b16 v[68:69], v18 offset:49152
	ds_read_b64_tr_b16 v[70:71], v18 offset:50176
	ds_read_b64_tr_b16 v[72:73], v29 offset:12288
	ds_read_b64_tr_b16 v[74:75], v29 offset:13312
	ds_read_b64_tr_b16 v[76:77], v29 offset:49152
	ds_read_b64_tr_b16 v[78:79], v29 offset:50176
	ds_read_b64_tr_b16 v[80:81], v30 offset:12288
	ds_read_b64_tr_b16 v[82:83], v30 offset:13312
	ds_read_b64_tr_b16 v[84:85], v30 offset:49152
	ds_read_b64_tr_b16 v[86:87], v30 offset:50176
	ds_read_b64_tr_b16 a[144:145], v32 offset:12288
	ds_read_b64_tr_b16 a[146:147], v32 offset:13312
	ds_read_b64_tr_b16 a[148:149], v32 offset:49152
	ds_read_b64_tr_b16 a[150:151], v32 offset:50176
	ds_read_b64_tr_b16 a[152:153], v33 offset:12288
	ds_read_b64_tr_b16 a[154:155], v33 offset:13312
	ds_read_b64_tr_b16 a[156:157], v33 offset:49152
	ds_read_b64_tr_b16 a[158:159], v33 offset:50176
	ds_read_b64_tr_b16 a[160:161], v34 offset:12288
	ds_read_b64_tr_b16 a[162:163], v34 offset:13312
	ds_read_b64_tr_b16 a[164:165], v34 offset:49152
	ds_read_b64_tr_b16 a[166:167], v34 offset:50176
	s_branch .Lvo_v6
.Lvo_v9:
	ds_read_b64_tr_b16 a[96:97], v18 offset:12288
	ds_read_b64_tr_b16 a[98:99], v18 offset:13312
	ds_read_b64_tr_b16 a[100:101], v18 offset:49152
	ds_read_b64_tr_b16 a[102:103], v18 offset:50176
	ds_read_b64_tr_b16 a[104:105], v29 offset:12288
	ds_read_b64_tr_b16 a[106:107], v29 offset:13312
	ds_read_b64_tr_b16 a[108:109], v29 offset:49152
	ds_read_b64_tr_b16 a[110:111], v29 offset:50176
	ds_read_b64_tr_b16 a[112:113], v30 offset:12288
	ds_read_b64_tr_b16 a[114:115], v30 offset:13312
	ds_read_b64_tr_b16 a[116:117], v30 offset:49152
	ds_read_b64_tr_b16 a[118:119], v30 offset:50176
	ds_read_b64_tr_b16 a[120:121], v32 offset:12288
	ds_read_b64_tr_b16 a[122:123], v32 offset:13312
	ds_read_b64_tr_b16 a[124:125], v32 offset:49152
	ds_read_b64_tr_b16 a[126:127], v32 offset:50176
	ds_read_b64_tr_b16 a[128:129], v33 offset:12288
	ds_read_b64_tr_b16 a[130:131], v33 offset:13312
	ds_read_b64_tr_b16 a[132:133], v33 offset:49152
	ds_read_b64_tr_b16 a[134:135], v33 offset:50176
	ds_read_b64_tr_b16 a[136:137], v34 offset:12288
	ds_read_b64_tr_b16 a[138:139], v34 offset:13312
	ds_read_b64_tr_b16 a[140:141], v34 offset:49152
	ds_read_b64_tr_b16 a[142:143], v34 offset:50176
.Lvo_v6:
	s_waitcnt lgkmcnt(0)
	s_barrier
	s_add_u32 m0, s33, 12288
	s_nop 0
	buffer_load_dwordx4 v16, s[24:27], s32 offen offset:0 nt lds
	s_add_u32 m0, s33, 14208
	s_nop 0
	buffer_load_dwordx4 v16, s[24:27], s32 offen offset:128 nt lds
	s_add_u32 m0, s33, 16128
	s_nop 0
	buffer_load_dwordx4 v16, s[24:27], s32 offen offset:256 nt lds
	s_add_u32 m0, s33, 13312
	s_nop 0
	buffer_load_dwordx4 v17, s[24:27], s32 offen offset:0 nt lds
	s_add_u32 m0, s33, 15232
	s_nop 0
	buffer_load_dwordx4 v17, s[24:27], s32 offen offset:128 nt lds
	s_add_u32 m0, s33, 17152
	s_nop 0
	buffer_load_dwordx4 v17, s[24:27], s32 offen offset:256 nt lds
	s_add_u32 s32, s32, s79
	s_add_u32 m0, s33, 18432
	s_nop 0
	buffer_load_dwordx4 v16, s[24:27], s32 offen offset:0 nt lds
	s_add_u32 m0, s33, 20352
	s_nop 0
	buffer_load_dwordx4 v16, s[24:27], s32 offen offset:128 nt lds
	s_add_u32 m0, s33, 22272
	s_nop 0
	buffer_load_dwordx4 v16, s[24:27], s32 offen offset:256 nt lds
	s_add_u32 m0, s33, 19456
	s_nop 0
	buffer_load_dwordx4 v17, s[24:27], s32 offen offset:0 nt lds
	s_add_u32 m0, s33, 21376
	s_nop 0
	buffer_load_dwordx4 v17, s[24:27], s32 offen offset:128 nt lds
	s_add_u32 m0, s33, 23296
	s_nop 0
	buffer_load_dwordx4 v17, s[24:27], s32 offen offset:256 nt lds
	s_add_u32 s32, s32, 1376256
	s_waitcnt vmcnt(24)
	s_barrier
	s_cmp_ge_u32 s37, 2
	s_cbranch_scc1 .Lvo_v12
	s_cmp_eq_u32 s37, 1
	s_cbranch_scc1 .Lvo_v11
	ds_read_b64_tr_b16 a[96:97], v18 offset:24576
	ds_read_b64_tr_b16 a[98:99], v18 offset:25600
	ds_read_b64_tr_b16 a[100:101], v18 offset:61440
	ds_read_b64_tr_b16 a[102:103], v18 offset:62464
	ds_read_b64_tr_b16 a[104:105], v29 offset:24576
	ds_read_b64_tr_b16 a[106:107], v29 offset:25600
	ds_read_b64_tr_b16 a[108:109], v29 offset:61440
	ds_read_b64_tr_b16 a[110:111], v29 offset:62464
	ds_read_b64_tr_b16 a[112:113], v30 offset:24576
	ds_read_b64_tr_b16 a[114:115], v30 offset:25600
	ds_read_b64_tr_b16 a[116:117], v30 offset:61440
	ds_read_b64_tr_b16 a[118:119], v30 offset:62464
	ds_read_b64_tr_b16 a[120:121], v32 offset:24576
	ds_read_b64_tr_b16 a[122:123], v32 offset:25600
	ds_read_b64_tr_b16 a[124:125], v32 offset:61440
	ds_read_b64_tr_b16 a[126:127], v32 offset:62464
	ds_read_b64_tr_b16 a[128:129], v33 offset:24576
	ds_read_b64_tr_b16 a[130:131], v33 offset:25600
	ds_read_b64_tr_b16 a[132:133], v33 offset:61440
	ds_read_b64_tr_b16 a[134:135], v33 offset:62464
	ds_read_b64_tr_b16 a[136:137], v34 offset:24576
	ds_read_b64_tr_b16 a[138:139], v34 offset:25600
	ds_read_b64_tr_b16 a[140:141], v34 offset:61440
	ds_read_b64_tr_b16 a[142:143], v34 offset:62464
	s_branch .Lvo_v10
.Lvo_v11:
	ds_read_b64_tr_b16 v[40:41], v18 offset:24576
	ds_read_b64_tr_b16 v[42:43], v18 offset:25600
	ds_read_b64_tr_b16 v[44:45], v18 offset:61440
	ds_read_b64_tr_b16 v[46:47], v18 offset:62464
	ds_read_b64_tr_b16 v[48:49], v29 offset:24576
	ds_read_b64_tr_b16 v[50:51], v29 offset:25600
	ds_read_b64_tr_b16 v[52:53], v29 offset:61440
	ds_read_b64_tr_b16 v[54:55], v29 offset:62464
	ds_read_b64_tr_b16 v[56:57], v30 offset:24576
	ds_read_b64_tr_b16 v[58:59], v30 offset:25600
	ds_read_b64_tr_b16 v[60:61], v30 offset:61440
	ds_read_b64_tr_b16 v[62:63], v30 offset:62464
	ds_read_b64_tr_b16 a[72:73], v32 offset:24576
	ds_read_b64_tr_b16 a[74:75], v32 offset:25600
	ds_read_b64_tr_b16 a[76:77], v32 offset:61440
	ds_read_b64_tr_b16 a[78:79], v32 offset:62464
	ds_read_b64_tr_b16 a[80:81], v33 offset:24576
	ds_read_b64_tr_b16 a[82:83], v33 offset:25600
	ds_read_b64_tr_b16 a[84:85], v33 offset:61440
	ds_read_b64_tr_b16 a[86:87], v33 offset:62464
	ds_read_b64_tr_b16 a[88:89], v34 offset:24576
	ds_read_b64_tr_b16 a[90:91], v34 offset:25600
	ds_read_b64_tr_b16 a[92:93], v34 offset:61440
	ds_read_b64_tr_b16 a[94:95], v34 offset:62464
	s_branch .Lvo_v10
.Lvo_v12:
	s_cmp_eq_u32 s37, 3
	s_cbranch_scc1 .Lvo_v13
	ds_read_b64_tr_b16 a[0:1], v18 offset:24576
	ds_read_b64_tr_b16 a[2:3], v18 offset:25600
	ds_read_b64_tr_b16 a[4:5], v18 offset:61440
	ds_read_b64_tr_b16 a[6:7], v18 offset:62464
	ds_read_b64_tr_b16 a[8:9], v29 offset:24576
	ds_read_b64_tr_b16 a[10:11], v29 offset:25600
	ds_read_b64_tr_b16 a[12:13], v29 offset:61440
	ds_read_b64_tr_b16 a[14:15], v29 offset:62464
	ds_read_b64_tr_b16 a[16:17], v30 offset:24576
	ds_read_b64_tr_b16 a[18:19], v30 offset:25600
	ds_read_b64_tr_b16 a[20:21], v30 offset:61440
	ds_read_b64_tr_b16 a[22:23], v30 offset:62464
	ds_read_b64_tr_b16 a[24:25], v32 offset:24576
	ds_read_b64_tr_b16 a[26:27], v32 offset:25600
	ds_read_b64_tr_b16 a[28:29], v32 offset:61440
	ds_read_b64_tr_b16 a[30:31], v32 offset:62464
	ds_read_b64_tr_b16 a[32:33], v33 offset:24576
	ds_read_b64_tr_b16 a[34:35], v33 offset:25600
	ds_read_b64_tr_b16 a[36:37], v33 offset:61440
	ds_read_b64_tr_b16 a[38:39], v33 offset:62464
	ds_read_b64_tr_b16 a[40:41], v34 offset:24576
	ds_read_b64_tr_b16 a[42:43], v34 offset:25600
	ds_read_b64_tr_b16 a[44:45], v34 offset:61440
	ds_read_b64_tr_b16 a[46:47], v34 offset:62464
	s_branch .Lvo_v10
.Lvo_v13:
	ds_read_b64_tr_b16 v[64:65], v18 offset:24576
	ds_read_b64_tr_b16 v[66:67], v18 offset:25600
	ds_read_b64_tr_b16 v[68:69], v18 offset:61440
	ds_read_b64_tr_b16 v[70:71], v18 offset:62464
	ds_read_b64_tr_b16 v[72:73], v29 offset:24576
	ds_read_b64_tr_b16 v[74:75], v29 offset:25600
	ds_read_b64_tr_b16 v[76:77], v29 offset:61440
	ds_read_b64_tr_b16 v[78:79], v29 offset:62464
	ds_read_b64_tr_b16 v[80:81], v30 offset:24576
	ds_read_b64_tr_b16 v[82:83], v30 offset:25600
	ds_read_b64_tr_b16 v[84:85], v30 offset:61440
	ds_read_b64_tr_b16 v[86:87], v30 offset:62464
	ds_read_b64_tr_b16 a[144:145], v32 offset:24576
	ds_read_b64_tr_b16 a[146:147], v32 offset:25600
	ds_read_b64_tr_b16 a[148:149], v32 offset:61440
	ds_read_b64_tr_b16 a[150:151], v32 offset:62464
	ds_read_b64_tr_b16 a[152:153], v33 offset:24576
	ds_read_b64_tr_b16 a[154:155], v33 offset:25600
	ds_read_b64_tr_b16 a[156:157], v33 offset:61440
	ds_read_b64_tr_b16 a[158:159], v33 offset:62464
	ds_read_b64_tr_b16 a[160:161], v34 offset:24576
	ds_read_b64_tr_b16 a[162:163], v34 offset:25600
	ds_read_b64_tr_b16 a[164:165], v34 offset:61440
	ds_read_b64_tr_b16 a[166:167], v34 offset:62464
.Lvo_v10:
	s_waitcnt lgkmcnt(0)
	s_barrier
	s_add_u32 m0, s33, 24576
	s_mov_b32 s77, 61440
	buffer_load_dwordx4 v19, s[28:31], s77 offen offset:0 nt lds
	s_add_u32 m0, m0, s78
	s_nop 0
	buffer_load_dwordx4 v20, s[28:31], s77 offen offset:0 nt lds
	s_add_u32 m0, m0, s78
	s_nop 0
	buffer_load_dwordx4 v21, s[28:31], s77 offen offset:0 nt lds
	s_add_u32 m0, m0, s78
	s_nop 0
	buffer_load_dwordx4 v22, s[28:31], s77 offen offset:0 nt lds
	s_add_u32 m0, m0, s78
	s_nop 0
	buffer_load_dwordx4 v23, s[28:31], s77 offen offset:0 nt lds
	s_add_u32 m0, m0, s78
	s_mov_b32 s77, 62720
	buffer_load_dwordx4 v19, s[28:31], s77 offen offset:0 nt lds
	s_add_u32 m0, m0, s78
	s_nop 0
	buffer_load_dwordx4 v20, s[28:31], s77 offen offset:0 nt lds
	s_add_u32 m0, m0, s78
	s_nop 0
	buffer_load_dwordx4 v21, s[28:31], s77 offen offset:0 nt lds
	s_add_u32 m0, m0, s78
	s_nop 0
	buffer_load_dwordx4 v22, s[28:31], s77 offen offset:0 nt lds
	s_add_u32 m0, m0, s78
	s_nop 0
	buffer_load_dwordx4 v23, s[28:31], s77 offen offset:0 nt lds
	buffer_load_dwordx4 v[104:107], v112, s[20:23], s71 offen offset:0 sc1
	buffer_load_dword v108, off, s[20:23], s71 offset:2048 sc1
	s_waitcnt vmcnt(24)
	s_barrier
	s_cmp_ge_u32 s37, 2
	s_cbranch_scc1 .Lvo_v16
	s_cmp_eq_u32 s37, 1
	s_cbranch_scc1 .Lvo_v15
	ds_read_b64_tr_b16 v[64:65], v18 offset:0
	ds_read_b64_tr_b16 v[66:67], v18 offset:1024
	ds_read_b64_tr_b16 v[68:69], v18 offset:36864
	ds_read_b64_tr_b16 v[70:71], v18 offset:37888
	ds_read_b64_tr_b16 v[72:73], v29 offset:0
	ds_read_b64_tr_b16 v[74:75], v29 offset:1024
	ds_read_b64_tr_b16 v[76:77], v29 offset:36864
	ds_read_b64_tr_b16 v[78:79], v29 offset:37888
	ds_read_b64_tr_b16 v[80:81], v30 offset:0
	ds_read_b64_tr_b16 v[82:83], v30 offset:1024
	ds_read_b64_tr_b16 v[84:85], v30 offset:36864
	ds_read_b64_tr_b16 v[86:87], v30 offset:37888
	ds_read_b64_tr_b16 a[144:145], v32 offset:0
	ds_read_b64_tr_b16 a[146:147], v32 offset:1024
	ds_read_b64_tr_b16 a[148:149], v32 offset:36864
	ds_read_b64_tr_b16 a[150:151], v32 offset:37888
	ds_read_b64_tr_b16 a[152:153], v33 offset:0
	ds_read_b64_tr_b16 a[154:155], v33 offset:1024
	ds_read_b64_tr_b16 a[156:157], v33 offset:36864
	ds_read_b64_tr_b16 a[158:159], v33 offset:37888
	ds_read_b64_tr_b16 a[160:161], v34 offset:0
	ds_read_b64_tr_b16 a[162:163], v34 offset:1024
	ds_read_b64_tr_b16 a[164:165], v34 offset:36864
	ds_read_b64_tr_b16 a[166:167], v34 offset:37888
	s_branch .Lvo_v14
.Lvo_v15:
	ds_read_b64_tr_b16 a[96:97], v18 offset:0
	ds_read_b64_tr_b16 a[98:99], v18 offset:1024
	ds_read_b64_tr_b16 a[100:101], v18 offset:36864
	ds_read_b64_tr_b16 a[102:103], v18 offset:37888
	ds_read_b64_tr_b16 a[104:105], v29 offset:0
	ds_read_b64_tr_b16 a[106:107], v29 offset:1024
	ds_read_b64_tr_b16 a[108:109], v29 offset:36864
	ds_read_b64_tr_b16 a[110:111], v29 offset:37888
	ds_read_b64_tr_b16 a[112:113], v30 offset:0
	ds_read_b64_tr_b16 a[114:115], v30 offset:1024
	ds_read_b64_tr_b16 a[116:117], v30 offset:36864
	ds_read_b64_tr_b16 a[118:119], v30 offset:37888
	ds_read_b64_tr_b16 a[120:121], v32 offset:0
	ds_read_b64_tr_b16 a[122:123], v32 offset:1024
	ds_read_b64_tr_b16 a[124:125], v32 offset:36864
	ds_read_b64_tr_b16 a[126:127], v32 offset:37888
	ds_read_b64_tr_b16 a[128:129], v33 offset:0
	ds_read_b64_tr_b16 a[130:131], v33 offset:1024
	ds_read_b64_tr_b16 a[132:133], v33 offset:36864
	ds_read_b64_tr_b16 a[134:135], v33 offset:37888
	ds_read_b64_tr_b16 a[136:137], v34 offset:0
	ds_read_b64_tr_b16 a[138:139], v34 offset:1024
	ds_read_b64_tr_b16 a[140:141], v34 offset:36864
	ds_read_b64_tr_b16 a[142:143], v34 offset:37888
	s_branch .Lvo_v14
.Lvo_v16:
	s_cmp_eq_u32 s37, 3
	s_cbranch_scc1 .Lvo_v17
	ds_read_b64_tr_b16 v[40:41], v18 offset:0
	ds_read_b64_tr_b16 v[42:43], v18 offset:1024
	ds_read_b64_tr_b16 v[44:45], v18 offset:36864
	ds_read_b64_tr_b16 v[46:47], v18 offset:37888
	ds_read_b64_tr_b16 v[48:49], v29 offset:0
	ds_read_b64_tr_b16 v[50:51], v29 offset:1024
	ds_read_b64_tr_b16 v[52:53], v29 offset:36864
	ds_read_b64_tr_b16 v[54:55], v29 offset:37888
	ds_read_b64_tr_b16 v[56:57], v30 offset:0
	ds_read_b64_tr_b16 v[58:59], v30 offset:1024
	ds_read_b64_tr_b16 v[60:61], v30 offset:36864
	ds_read_b64_tr_b16 v[62:63], v30 offset:37888
	ds_read_b64_tr_b16 a[72:73], v32 offset:0
	ds_read_b64_tr_b16 a[74:75], v32 offset:1024
	ds_read_b64_tr_b16 a[76:77], v32 offset:36864
	ds_read_b64_tr_b16 a[78:79], v32 offset:37888
	ds_read_b64_tr_b16 a[80:81], v33 offset:0
	ds_read_b64_tr_b16 a[82:83], v33 offset:1024
	ds_read_b64_tr_b16 a[84:85], v33 offset:36864
	ds_read_b64_tr_b16 a[86:87], v33 offset:37888
	ds_read_b64_tr_b16 a[88:89], v34 offset:0
	ds_read_b64_tr_b16 a[90:91], v34 offset:1024
	ds_read_b64_tr_b16 a[92:93], v34 offset:36864
	ds_read_b64_tr_b16 a[94:95], v34 offset:37888
	s_branch .Lvo_v14
.Lvo_v17:
	ds_read_b64_tr_b16 a[0:1], v18 offset:0
	ds_read_b64_tr_b16 a[2:3], v18 offset:1024
	ds_read_b64_tr_b16 a[4:5], v18 offset:36864
	ds_read_b64_tr_b16 a[6:7], v18 offset:37888
	ds_read_b64_tr_b16 a[8:9], v29 offset:0
	ds_read_b64_tr_b16 a[10:11], v29 offset:1024
	ds_read_b64_tr_b16 a[12:13], v29 offset:36864
	ds_read_b64_tr_b16 a[14:15], v29 offset:37888
	ds_read_b64_tr_b16 a[16:17], v30 offset:0
	ds_read_b64_tr_b16 a[18:19], v30 offset:1024
	ds_read_b64_tr_b16 a[20:21], v30 offset:36864
	ds_read_b64_tr_b16 a[22:23], v30 offset:37888
	ds_read_b64_tr_b16 a[24:25], v32 offset:0
	ds_read_b64_tr_b16 a[26:27], v32 offset:1024
	ds_read_b64_tr_b16 a[28:29], v32 offset:36864
	ds_read_b64_tr_b16 a[30:31], v32 offset:37888
	ds_read_b64_tr_b16 a[32:33], v33 offset:0
	ds_read_b64_tr_b16 a[34:35], v33 offset:1024
	ds_read_b64_tr_b16 a[36:37], v33 offset:36864
	ds_read_b64_tr_b16 a[38:39], v33 offset:37888
	ds_read_b64_tr_b16 a[40:41], v34 offset:0
	ds_read_b64_tr_b16 a[42:43], v34 offset:1024
	ds_read_b64_tr_b16 a[44:45], v34 offset:36864
	ds_read_b64_tr_b16 a[46:47], v34 offset:37888
.Lvo_v14:
	s_waitcnt lgkmcnt(0)
	s_barrier
	s_add_u32 m0, s33, 0
	s_mov_b32 s77, 122880
	buffer_load_dwordx4 v19, s[28:31], s77 offen offset:0 nt lds
	s_add_u32 m0, m0, s78
	s_nop 0
	buffer_load_dwordx4 v20, s[28:31], s77 offen offset:0 nt lds
	s_add_u32 m0, m0, s78
	s_nop 0
	buffer_load_dwordx4 v21, s[28:31], s77 offen offset:0 nt lds
	s_add_u32 m0, m0, s78
	s_nop 0
	buffer_load_dwordx4 v22, s[28:31], s77 offen offset:0 nt lds
	s_add_u32 m0, m0, s78
	s_nop 0
	buffer_load_dwordx4 v23, s[28:31], s77 offen offset:0 nt lds
	s_add_u32 m0, m0, s78
	s_mov_b32 s77, 124160
	buffer_load_dwordx4 v19, s[28:31], s77 offen offset:0 nt lds
	s_add_u32 m0, m0, s78
	s_nop 0
	buffer_load_dwordx4 v20, s[28:31], s77 offen offset:0 nt lds
	s_add_u32 m0, m0, s78
	s_nop 0
	buffer_load_dwordx4 v21, s[28:31], s77 offen offset:0 nt lds
	s_add_u32 m0, m0, s78
	s_nop 0
	buffer_load_dwordx4 v22, s[28:31], s77 offen offset:0 nt lds
	s_add_u32 m0, m0, s78
	s_nop 0
	buffer_load_dwordx4 v23, s[28:31], s77 offen offset:0 nt lds
	s_waitcnt vmcnt(22)
	s_barrier
	s_cmp_ge_u32 s37, 2
	s_cbranch_scc1 .Lvo_v20
	s_cmp_eq_u32 s37, 1
	s_cbranch_scc1 .Lvo_v19
	ds_read_b64_tr_b16 a[48:49], v18 offset:12288
	ds_read_b64_tr_b16 a[50:51], v18 offset:13312
	ds_read_b64_tr_b16 a[52:53], v18 offset:49152
	ds_read_b64_tr_b16 a[54:55], v18 offset:50176
	ds_read_b64_tr_b16 a[56:57], v29 offset:12288
	ds_read_b64_tr_b16 a[58:59], v29 offset:13312
	ds_read_b64_tr_b16 a[60:61], v29 offset:49152
	ds_read_b64_tr_b16 a[62:63], v29 offset:50176
	ds_read_b64_tr_b16 a[64:65], v30 offset:12288
	ds_read_b64_tr_b16 a[66:67], v30 offset:13312
	ds_read_b64_tr_b16 a[68:69], v30 offset:49152
	ds_read_b64_tr_b16 a[70:71], v30 offset:50176
	ds_read_b64_tr_b16 a[168:169], v32 offset:12288
	ds_read_b64_tr_b16 a[170:171], v32 offset:13312
	ds_read_b64_tr_b16 a[172:173], v32 offset:49152
	ds_read_b64_tr_b16 a[174:175], v32 offset:50176
	ds_read_b64_tr_b16 a[176:177], v33 offset:12288
	ds_read_b64_tr_b16 a[178:179], v33 offset:13312
	ds_read_b64_tr_b16 a[180:181], v33 offset:49152
	ds_read_b64_tr_b16 a[182:183], v33 offset:50176
	ds_read_b64_tr_b16 a[184:185], v34 offset:12288
	ds_read_b64_tr_b16 a[186:187], v34 offset:13312
	ds_read_b64_tr_b16 a[188:189], v34 offset:49152
	ds_read_b64_tr_b16 a[190:191], v34 offset:50176
	s_branch .Lvo_v18
.Lvo_v19:
	ds_read_b64_tr_b16 a[48:49], v18 offset:12288
	ds_read_b64_tr_b16 a[50:51], v18 offset:13312
	ds_read_b64_tr_b16 a[52:53], v18 offset:49152
	ds_read_b64_tr_b16 a[54:55], v18 offset:50176
	ds_read_b64_tr_b16 a[56:57], v29 offset:12288
	ds_read_b64_tr_b16 a[58:59], v29 offset:13312
	ds_read_b64_tr_b16 a[60:61], v29 offset:49152
	ds_read_b64_tr_b16 a[62:63], v29 offset:50176
	ds_read_b64_tr_b16 a[64:65], v30 offset:12288
	ds_read_b64_tr_b16 a[66:67], v30 offset:13312
	ds_read_b64_tr_b16 a[68:69], v30 offset:49152
	ds_read_b64_tr_b16 a[70:71], v30 offset:50176
	ds_read_b64_tr_b16 a[168:169], v32 offset:12288
	ds_read_b64_tr_b16 a[170:171], v32 offset:13312
	ds_read_b64_tr_b16 a[172:173], v32 offset:49152
	ds_read_b64_tr_b16 a[174:175], v32 offset:50176
	ds_read_b64_tr_b16 a[176:177], v33 offset:12288
	ds_read_b64_tr_b16 a[178:179], v33 offset:13312
	ds_read_b64_tr_b16 a[180:181], v33 offset:49152
	ds_read_b64_tr_b16 a[182:183], v33 offset:50176
	ds_read_b64_tr_b16 a[184:185], v34 offset:12288
	ds_read_b64_tr_b16 a[186:187], v34 offset:13312
	ds_read_b64_tr_b16 a[188:189], v34 offset:49152
	ds_read_b64_tr_b16 a[190:191], v34 offset:50176
	s_branch .Lvo_v18
.Lvo_v20:
	s_cmp_eq_u32 s37, 3
	s_cbranch_scc1 .Lvo_v21
	ds_read_b64_tr_b16 a[168:169], v18 offset:12288
	ds_read_b64_tr_b16 a[170:171], v18 offset:13312
	ds_read_b64_tr_b16 a[172:173], v18 offset:49152
	ds_read_b64_tr_b16 a[174:175], v18 offset:50176
	ds_read_b64_tr_b16 a[176:177], v29 offset:12288
	ds_read_b64_tr_b16 a[178:179], v29 offset:13312
	ds_read_b64_tr_b16 a[180:181], v29 offset:49152
	ds_read_b64_tr_b16 a[182:183], v29 offset:50176
	ds_read_b64_tr_b16 a[184:185], v30 offset:12288
	ds_read_b64_tr_b16 a[186:187], v30 offset:13312
	ds_read_b64_tr_b16 a[188:189], v30 offset:49152
	ds_read_b64_tr_b16 a[190:191], v30 offset:50176
	ds_read_b64_tr_b16 a[48:49], v32 offset:12288
	ds_read_b64_tr_b16 a[50:51], v32 offset:13312
	ds_read_b64_tr_b16 a[52:53], v32 offset:49152
	ds_read_b64_tr_b16 a[54:55], v32 offset:50176
	ds_read_b64_tr_b16 a[56:57], v33 offset:12288
	ds_read_b64_tr_b16 a[58:59], v33 offset:13312
	ds_read_b64_tr_b16 a[60:61], v33 offset:49152
	ds_read_b64_tr_b16 a[62:63], v33 offset:50176
	ds_read_b64_tr_b16 a[64:65], v34 offset:12288
	ds_read_b64_tr_b16 a[66:67], v34 offset:13312
	ds_read_b64_tr_b16 a[68:69], v34 offset:49152
	ds_read_b64_tr_b16 a[70:71], v34 offset:50176
	s_branch .Lvo_v18
.Lvo_v21:
	ds_read_b64_tr_b16 a[168:169], v18 offset:12288
	ds_read_b64_tr_b16 a[170:171], v18 offset:13312
	ds_read_b64_tr_b16 a[172:173], v18 offset:49152
	ds_read_b64_tr_b16 a[174:175], v18 offset:50176
	ds_read_b64_tr_b16 a[176:177], v29 offset:12288
	ds_read_b64_tr_b16 a[178:179], v29 offset:13312
	ds_read_b64_tr_b16 a[180:181], v29 offset:49152
	ds_read_b64_tr_b16 a[182:183], v29 offset:50176
	ds_read_b64_tr_b16 a[184:185], v30 offset:12288
	ds_read_b64_tr_b16 a[186:187], v30 offset:13312
	ds_read_b64_tr_b16 a[188:189], v30 offset:49152
	ds_read_b64_tr_b16 a[190:191], v30 offset:50176
	ds_read_b64_tr_b16 a[48:49], v32 offset:12288
	ds_read_b64_tr_b16 a[50:51], v32 offset:13312
	ds_read_b64_tr_b16 a[52:53], v32 offset:49152
	ds_read_b64_tr_b16 a[54:55], v32 offset:50176
	ds_read_b64_tr_b16 a[56:57], v33 offset:12288
	ds_read_b64_tr_b16 a[58:59], v33 offset:13312
	ds_read_b64_tr_b16 a[60:61], v33 offset:49152
	ds_read_b64_tr_b16 a[62:63], v33 offset:50176
	ds_read_b64_tr_b16 a[64:65], v34 offset:12288
	ds_read_b64_tr_b16 a[66:67], v34 offset:13312
	ds_read_b64_tr_b16 a[68:69], v34 offset:49152
	ds_read_b64_tr_b16 a[70:71], v34 offset:50176
.Lvo_v18:
	s_waitcnt lgkmcnt(0)
	s_barrier
	s_add_u32 m0, s33, 12288
	s_mov_b32 s77, 184320
	buffer_load_dwordx4 v19, s[28:31], s77 offen offset:0 nt lds
	s_add_u32 m0, m0, s78
	s_nop 0
	buffer_load_dwordx4 v20, s[28:31], s77 offen offset:0 nt lds
	s_add_u32 m0, m0, s78
	s_nop 0
	buffer_load_dwordx4 v21, s[28:31], s77 offen offset:0 nt lds
	s_add_u32 m0, m0, s78
	s_nop 0
	buffer_load_dwordx4 v22, s[28:31], s77 offen offset:0 nt lds
	s_add_u32 m0, m0, s78
	s_nop 0
	buffer_load_dwordx4 v23, s[28:31], s77 offen offset:0 nt lds
	s_add_u32 m0, m0, s78
	s_mov_b32 s77, 185600
	buffer_load_dwordx4 v19, s[28:31], s77 offen offset:0 nt lds
	s_add_u32 m0, m0, s78
	s_nop 0
	buffer_load_dwordx4 v20, s[28:31], s77 offen offset:0 nt lds
	s_add_u32 m0, m0, s78
	s_nop 0
	buffer_load_dwordx4 v21, s[28:31], s77 offen offset:0 nt lds
	s_add_u32 m0, m0, s78
	s_nop 0
	buffer_load_dwordx4 v22, s[28:31], s77 offen offset:0 nt lds
	s_add_u32 m0, m0, s78
	s_nop 0
	buffer_load_dwordx4 v23, s[28:31], s77 offen offset:0 nt lds
	s_waitcnt vmcnt(22)
	s_barrier
	v_add_u32 v32, 24576, v0
	v_add_u32 v33, 24576, v1
	v_add_u32 v34, 24576, v2
	v_add_u32 v35, 24576, v3
	ds_read_b64_tr_b16 a[192:193], v32 offset:0
	ds_read_b64_tr_b16 a[194:195], v32 offset:5120
	ds_read_b64_tr_b16 a[196:197], v32 offset:36864
	ds_read_b64_tr_b16 a[198:199], v32 offset:41984
	ds_read_b64_tr_b16 a[200:201], v33 offset:0
	ds_read_b64_tr_b16 a[202:203], v33 offset:5120
	ds_read_b64_tr_b16 a[204:205], v33 offset:36864
	ds_read_b64_tr_b16 a[206:207], v33 offset:41984
	ds_read_b64_tr_b16 a[208:209], v34 offset:0
	ds_read_b64_tr_b16 a[210:211], v34 offset:5120
	ds_read_b64_tr_b16 a[212:213], v34 offset:36864
	ds_read_b64_tr_b16 a[214:215], v34 offset:41984
	ds_read_b64_tr_b16 a[216:217], v35 offset:0
	ds_read_b64_tr_b16 a[218:219], v35 offset:5120
	ds_read_b64_tr_b16 a[220:221], v35 offset:36864
	ds_read_b64_tr_b16 a[222:223], v35 offset:41984
	ds_read_b64_tr_b16 a[224:225], v32 offset:256
	ds_read_b64_tr_b16 a[226:227], v32 offset:5376
	ds_read_b64_tr_b16 a[228:229], v32 offset:37120
	ds_read_b64_tr_b16 a[230:231], v32 offset:42240
	s_waitcnt lgkmcnt(0)
	s_barrier
	s_add_u32 m0, s33, 24576
	s_mov_b32 s77, 40960
	buffer_load_dwordx4 v24, s[28:31], s77 offen offset:0 nt lds
	s_add_u32 m0, m0, s78
	s_nop 0
	buffer_load_dwordx4 v25, s[28:31], s77 offen offset:0 nt lds
	s_add_u32 m0, m0, s78
	s_nop 0
	buffer_load_dwordx4 v26, s[28:31], s77 offen offset:0 nt lds
	s_add_u32 m0, m0, s78
	s_nop 0
	buffer_load_dwordx4 v27, s[28:31], s77 offen offset:0 nt lds
	s_add_u32 m0, m0, s78
	s_nop 0
	buffer_load_dwordx4 v28, s[28:31], s77 offen offset:0 nt lds
	s_add_u32 m0, m0, s78
	s_mov_b32 s77, 43520
	buffer_load_dwordx4 v24, s[28:31], s77 offen offset:0 nt lds
	s_add_u32 m0, m0, s78
	s_nop 0
	buffer_load_dwordx4 v25, s[28:31], s77 offen offset:0 nt lds
	s_add_u32 m0, m0, s78
	s_nop 0
	buffer_load_dwordx4 v26, s[28:31], s77 offen offset:0 nt lds
	s_add_u32 m0, m0, s78
	s_nop 0
	buffer_load_dwordx4 v27, s[28:31], s77 offen offset:0 nt lds
	s_add_u32 m0, m0, s78
	s_nop 0
	buffer_load_dwordx4 v28, s[28:31], s77 offen offset:0 nt lds
.Lvo_h22:
	s_waitcnt vmcnt(31)
	v_xor_b32 v104, s70, v104
	v_xor_b32 v105, s70, v105
	v_xor_b32 v106, s70, v106
	v_xor_b32 v107, s70, v107
	v_or3_b32 v109, v104, v105, v106
	v_or_b32 v109, v109, v107
	v_and_b32 v110, 0xffffff07, v109
	v_cmp_ne_u32 vcc, 0, v110
	s_cbranch_vccz .Lvo_h23
	v_cmp_lt_u32 vcc, 0xff, v109
	s_cbranch_vccz .Lvo_x1
	s_cmp_eq_u32 s69, 0
	s_cbranch_scc1 .Lvo_h23
	s_sub_u32 s69, s69, 1
	s_sleep 2
	buffer_load_dwordx4 v[104:107], v112, s[20:23], s71 offen offset:0 sc1
	buffer_load_dword v108, off, s[20:23], s71 offset:2048 sc1
	s_waitcnt vmcnt(0)
	s_branch .Lvo_h22
.Lvo_h23:
	v_and_b32 v110, 64, v104
	v_cmp_ne_u32 vcc, 0, v110
	s_bcnt1_i32_b64 s72, vcc
	v_and_b32 v110, 64, v105
	v_cmp_ne_u32 vcc, 0, v110
	s_bcnt1_i32_b64 s18, vcc
	s_add_u32 s72, s72, s18
	v_and_b32 v110, 64, v106
	v_cmp_ne_u32 vcc, 0, v110
	s_bcnt1_i32_b64 s18, vcc
	s_add_u32 s72, s72, s18
	v_and_b32 v110, 64, v107
	v_cmp_ne_u32 vcc, 0, v110
	s_bcnt1_i32_b64 s18, vcc
	s_add_u32 s72, s72, s18
	s_cmp_ge_u32 s72, 20
	s_cselect_b32 s72, 8192, 0
	v_and_b32 v110, 32, v104
	v_cmp_ne_u32 vcc, 0, v110
	s_bcnt1_i32_b64 s66, vcc
	v_and_b32 v110, 32, v105
	v_cmp_ne_u32 vcc, 0, v110
	s_bcnt1_i32_b64 s18, vcc
	s_add_u32 s66, s66, s18
	v_and_b32 v110, 32, v106
	v_cmp_ne_u32 vcc, 0, v110
	s_bcnt1_i32_b64 s18, vcc
	s_add_u32 s66, s66, s18
	v_and_b32 v110, 32, v107
	v_cmp_ne_u32 vcc, 0, v110
	s_bcnt1_i32_b64 s18, vcc
	s_add_u32 s66, s66, s18
	s_cmp_ge_u32 s66, 20
	s_cselect_b32 s66, 4096, 0
	v_and_b32 v110, 128, v104
	v_cmp_ne_u32 vcc, 0, v110
	s_bcnt1_i32_b64 s67, vcc
	v_and_b32 v110, 128, v105
	v_cmp_ne_u32 vcc, 0, v110
	s_bcnt1_i32_b64 s18, vcc
	s_add_u32 s67, s67, s18
	v_and_b32 v110, 128, v106
	v_cmp_ne_u32 vcc, 0, v110
	s_bcnt1_i32_b64 s18, vcc
	s_add_u32 s67, s67, s18
	v_and_b32 v110, 128, v107
	v_cmp_ne_u32 vcc, 0, v110
	s_bcnt1_i32_b64 s18, vcc
	s_add_u32 s67, s67, s18
	s_cmp_ge_u32 s67, 20
	s_cselect_b32 s67, 16384, 0
	s_or_b32 s66, s66, s67
	s_xor_b32 s36, s72, s36
	s_xor_b32 s38, s72, s38
	s_xor_b32 s46, s72, s46
	s_waitcnt vmcnt(30)
	v_readfirstlane_b32 s35, v108
	s_lshl_b32 s48, s34, 1
	s_add_u32 s48, s48, 0
	s_and_b32 s48, s48, 7
	s_lshr_b32 s76, s48, 2
	s_mul_i32 s76, s76, 28
	s_add_u32 s48, s48, s76
	s_lshl_b32 s48, s48, 10
	s_add_u32 s48, s48, s45
	s_lshl_b32 s49, s34, 1
	s_add_u32 s49, s49, 1
	s_and_b32 s49, s49, 7
	s_lshr_b32 s76, s49, 2
	s_mul_i32 s76, s76, 28
	s_add_u32 s49, s49, s76
	s_lshl_b32 s49, s49, 10
	s_add_u32 s49, s49, s45
	s_lshr_b32 s50, s34, 1
	s_add_u32 s50, s50, 8
	s_lshr_b32 s76, s50, 2
	s_mul_i32 s76, s76, 28
	s_add_u32 s50, s50, s76
	s_lshl_b32 s50, s50, 10
	s_add_u32 s50, s50, s45
	s_lshl_b32 s51, s34, 1
	s_add_u32 s51, s51, 0
	s_and_b32 s51, s51, 7
	s_lshl_b32 s51, s51, 10
	s_add_u32 s51, s51, s44
	s_lshr_b32 s52, s34, 1
	s_add_u32 s52, s52, 8
	s_lshl_b32 s52, s52, 10
	s_add_u32 s52, s52, s44
	s_lshl_b32 s53, s34, 1
	s_add_u32 s53, s53, 2
	s_and_b32 s53, s53, 7
	s_lshl_b32 s53, s53, 10
	s_add_u32 s53, s53, s44
	s_lshl_b32 s54, s34, 1
	s_add_u32 s54, s54, 4
	s_and_b32 s54, s54, 7
	s_lshl_b32 s54, s54, 10
	s_add_u32 s54, s54, s44
	s_lshl_b32 s55, s34, 1
	s_add_u32 s55, s55, 6
	s_and_b32 s55, s55, 7
	s_lshl_b32 s55, s55, 10
	s_add_u32 s55, s55, s44
	s_lshr_b32 s56, s34, 1
	s_sub_u32 s56, 9, s56
	s_lshl_b32 s56, s56, 10
	s_add_u32 s56, s56, s44
	s_mov_b32 s59, 0
	v_mov_b64 v[96:97], -1
	v_mov_b64 v[98:99], -1
	s_mov_b32 s42, s36
	s_add_u32 s80, s36, 0
	s_add_u32 s81, s80, 65536
	buffer_store_dwordx4 v[96:99], v10, s[20:23], s80 offen sc1
	buffer_store_dwordx4 v[96:99], v10, s[20:23], s81 offen sc1
	buffer_store_dwordx4 v[96:99], v11, s[20:23], s80 offen sc1
	s_add_u32 s80, s36, 4194304
	s_add_u32 s81, s80, 65536
	buffer_store_dwordx4 v[96:99], v10, s[20:23], s80 offen sc1
	buffer_store_dwordx4 v[96:99], v10, s[20:23], s81 offen sc1
	buffer_store_dwordx4 v[96:99], v11, s[20:23], s80 offen sc1
	s_add_u32 s80, s36, 8388608
	s_add_u32 s81, s80, 65536
	buffer_store_dwordx4 v[96:99], v10, s[20:23], s80 offen sc1
	buffer_store_dwordx4 v[96:99], v10, s[20:23], s81 offen sc1
	buffer_store_dwordx4 v[96:99], v11, s[20:23], s80 offen sc1
	s_waitcnt vmcnt(29)
	s_barrier
	v_add_u32 v32, 0, v0
	v_add_u32 v33, 0, v1
	v_add_u32 v34, 0, v2
	v_add_u32 v35, 0, v3
	ds_read_b64_tr_b16 a[232:233], v32 offset:0
	ds_read_b64_tr_b16 a[234:235], v32 offset:5120
	ds_read_b64_tr_b16 a[236:237], v32 offset:36864
	ds_read_b64_tr_b16 a[238:239], v32 offset:41984
	ds_read_b64_tr_b16 a[240:241], v33 offset:0
	ds_read_b64_tr_b16 a[242:243], v33 offset:5120
	ds_read_b64_tr_b16 a[244:245], v33 offset:36864
	ds_read_b64_tr_b16 a[246:247], v33 offset:41984
	ds_read_b64_tr_b16 a[248:249], v34 offset:0
	ds_read_b64_tr_b16 a[250:251], v34 offset:5120
	ds_read_b64_tr_b16 a[252:253], v34 offset:36864
	ds_read_b64_tr_b16 a[254:255], v34 offset:41984
	ds_read_b64_tr_b16 v[120:121], v35 offset:0
	ds_read_b64_tr_b16 v[122:123], v35 offset:5120
	ds_read_b64_tr_b16 v[124:125], v35 offset:36864
	ds_read_b64_tr_b16 v[126:127], v35 offset:41984
	ds_read_b64_tr_b16 v[128:129], v32 offset:256
	ds_read_b64_tr_b16 v[130:131], v32 offset:5376
	ds_read_b64_tr_b16 v[132:133], v32 offset:37120
	ds_read_b64_tr_b16 v[134:135], v32 offset:42240
	s_waitcnt lgkmcnt(0)
	s_barrier
	s_add_u32 m0, s33, 0
	s_mov_b32 s77, 163840
	buffer_load_dwordx4 v24, s[28:31], s77 offen offset:0 nt lds
	s_add_u32 m0, m0, s78
	s_nop 0
	buffer_load_dwordx4 v25, s[28:31], s77 offen offset:0 nt lds
	s_add_u32 m0, m0, s78
	s_nop 0
	buffer_load_dwordx4 v26, s[28:31], s77 offen offset:0 nt lds
	s_add_u32 m0, m0, s78
	s_nop 0
	buffer_load_dwordx4 v27, s[28:31], s77 offen offset:0 nt lds
	s_add_u32 m0, m0, s78
	s_nop 0
	buffer_load_dwordx4 v28, s[28:31], s77 offen offset:0 nt lds
	s_add_u32 m0, m0, s78
	s_mov_b32 s77, 166400
	buffer_load_dwordx4 v24, s[28:31], s77 offen offset:0 nt lds
	s_add_u32 m0, m0, s78
	s_nop 0
	buffer_load_dwordx4 v25, s[28:31], s77 offen offset:0 nt lds
	s_add_u32 m0, m0, s78
	s_nop 0
	buffer_load_dwordx4 v26, s[28:31], s77 offen offset:0 nt lds
	s_add_u32 m0, m0, s78
	s_nop 0
	buffer_load_dwordx4 v27, s[28:31], s77 offen offset:0 nt lds
	s_add_u32 m0, m0, s78
	s_nop 0
	buffer_load_dwordx4 v28, s[28:31], s77 offen offset:0 nt lds
	s_waitcnt vmcnt(29)
	s_barrier
	v_add_u32 v32, 12288, v0
	v_add_u32 v33, 12288, v1
	v_add_u32 v34, 12288, v2
	v_add_u32 v35, 12288, v3
	ds_read_b64_tr_b16 v[136:137], v32 offset:0
	ds_read_b64_tr_b16 v[138:139], v32 offset:5120
	ds_read_b64_tr_b16 v[140:141], v32 offset:36864
	ds_read_b64_tr_b16 v[142:143], v32 offset:41984
	ds_read_b64_tr_b16 v[144:145], v33 offset:0
	ds_read_b64_tr_b16 v[146:147], v33 offset:5120
	ds_read_b64_tr_b16 v[148:149], v33 offset:36864
	ds_read_b64_tr_b16 v[150:151], v33 offset:41984
	ds_read_b64_tr_b16 v[152:153], v34 offset:0
	ds_read_b64_tr_b16 v[154:155], v34 offset:5120
	ds_read_b64_tr_b16 v[156:157], v34 offset:36864
	ds_read_b64_tr_b16 v[158:159], v34 offset:41984
	ds_read_b64_tr_b16 v[160:161], v35 offset:0
	ds_read_b64_tr_b16 v[162:163], v35 offset:5120
	ds_read_b64_tr_b16 v[164:165], v35 offset:36864
	ds_read_b64_tr_b16 v[166:167], v35 offset:41984
	ds_read_b64_tr_b16 v[168:169], v32 offset:256
	ds_read_b64_tr_b16 v[170:171], v32 offset:5376
	ds_read_b64_tr_b16 v[172:173], v32 offset:37120
	ds_read_b64_tr_b16 v[174:175], v32 offset:42240
	s_waitcnt lgkmcnt(0)
	s_barrier
	s_add_u32 m0, s33, 12288
	s_mov_b32 s77, 0
	buffer_load_dwordx4 v19, s[28:31], s77 offen offset:0 nt lds
	s_add_u32 m0, m0, s78
	s_nop 0
	buffer_load_dwordx4 v20, s[28:31], s77 offen offset:0 nt lds
	s_add_u32 m0, m0, s78
	s_nop 0
	buffer_load_dwordx4 v21, s[28:31], s77 offen offset:0 nt lds
	s_add_u32 m0, m0, s78
	s_nop 0
	buffer_load_dwordx4 v22, s[28:31], s77 offen offset:0 nt lds
	s_add_u32 m0, m0, s78
	s_nop 0
	buffer_load_dwordx4 v23, s[28:31], s77 offen offset:0 nt lds
	s_add_u32 m0, m0, s78
	s_mov_b32 s77, 1280
	buffer_load_dwordx4 v19, s[28:31], s77 offen offset:0 nt lds
	s_add_u32 m0, m0, s78
	s_nop 0
	buffer_load_dwordx4 v20, s[28:31], s77 offen offset:0 nt lds
	s_add_u32 m0, m0, s78
	s_nop 0
	buffer_load_dwordx4 v21, s[28:31], s77 offen offset:0 nt lds
	s_add_u32 m0, m0, s78
	s_nop 0
	buffer_load_dwordx4 v22, s[28:31], s77 offen offset:0 nt lds
	s_add_u32 m0, m0, s78
	s_nop 0
	buffer_load_dwordx4 v23, s[28:31], s77 offen offset:0 nt lds
	s_waitcnt vmcnt(29)
	s_barrier
	v_add_u32 v32, 24576, v0
	v_add_u32 v33, 24576, v1
	v_add_u32 v34, 24576, v2
	v_add_u32 v35, 24576, v3
	ds_read_b64_tr_b16 v[176:177], v32 offset:0
	ds_read_b64_tr_b16 v[178:179], v32 offset:5120
	ds_read_b64_tr_b16 v[180:181], v32 offset:36864
	ds_read_b64_tr_b16 v[182:183], v32 offset:41984
	ds_read_b64_tr_b16 v[184:185], v33 offset:0
	ds_read_b64_tr_b16 v[186:187], v33 offset:5120
	ds_read_b64_tr_b16 v[188:189], v33 offset:36864
	ds_read_b64_tr_b16 v[190:191], v33 offset:41984
	ds_read_b64_tr_b16 v[192:193], v34 offset:0
	ds_read_b64_tr_b16 v[194:195], v34 offset:5120
	ds_read_b64_tr_b16 v[196:197], v34 offset:36864
	ds_read_b64_tr_b16 v[198:199], v34 offset:41984
	ds_read_b64_tr_b16 v[200:201], v35 offset:0
	ds_read_b64_tr_b16 v[202:203], v35 offset:5120
	ds_read_b64_tr_b16 v[204:205], v35 offset:36864
	ds_read_b64_tr_b16 v[206:207], v35 offset:41984
	ds_read_b64_tr_b16 v[208:209], v32 offset:256
	ds_read_b64_tr_b16 v[210:211], v32 offset:5376
	ds_read_b64_tr_b16 v[212:213], v32 offset:37120
	ds_read_b64_tr_b16 v[214:215], v32 offset:42240
	s_waitcnt lgkmcnt(0)
	s_barrier
	s_waitcnt vmcnt(10)
	s_barrier
	v_add_u32 v32, 0, v0
	v_add_u32 v33, 0, v1
	v_add_u32 v34, 0, v2
	v_add_u32 v35, 0, v3
	ds_read_b64_tr_b16 v[216:217], v32 offset:0
	ds_read_b64_tr_b16 v[218:219], v32 offset:5120
	ds_read_b64_tr_b16 v[220:221], v32 offset:36864
	ds_read_b64_tr_b16 v[222:223], v32 offset:41984
	ds_read_b64_tr_b16 v[224:225], v33 offset:0
	ds_read_b64_tr_b16 v[226:227], v33 offset:5120
	ds_read_b64_tr_b16 v[228:229], v33 offset:36864
	ds_read_b64_tr_b16 v[230:231], v33 offset:41984
	ds_read_b64_tr_b16 v[232:233], v34 offset:0
	ds_read_b64_tr_b16 v[234:235], v34 offset:5120
	ds_read_b64_tr_b16 v[236:237], v34 offset:36864
	ds_read_b64_tr_b16 v[238:239], v34 offset:41984
	ds_read_b64_tr_b16 v[240:241], v35 offset:0
	ds_read_b64_tr_b16 v[242:243], v35 offset:5120
	ds_read_b64_tr_b16 v[244:245], v35 offset:36864
	ds_read_b64_tr_b16 v[246:247], v35 offset:41984
	ds_read_b64_tr_b16 v[248:249], v32 offset:256
	ds_read_b64_tr_b16 v[250:251], v32 offset:5376
	ds_read_b64_tr_b16 v[252:253], v32 offset:37120
	ds_read_b64_tr_b16 v[254:255], v32 offset:42240
	s_waitcnt lgkmcnt(0)
	s_barrier
	v_mov_b32 v109, s70
	s_bitcmp0_b32 s64, 11
	s_cselect_b64 exec, 1, 0
	buffer_store_dword v109, off, s[20:23], s64 sc1
	s_mov_b64 exec, -1
	ds_write_b128 v6, v[40:43] offset:0
	ds_write_b128 v6, v[44:47] offset:1024
	ds_write_b128 v6, v[48:51] offset:2048
	ds_write_b128 v6, v[52:55] offset:3072
	ds_write_b128 v6, v[56:59] offset:4096
	ds_write_b128 v6, v[60:63] offset:5120
	ds_write_b128 v6, v[64:67] offset:6144
	ds_write_b128 v6, v[68:71] offset:7168
	ds_write_b128 v6, v[72:75] offset:8192
	ds_write_b128 v6, v[76:79] offset:9216
	ds_write_b128 v6, v[80:83] offset:10240
	ds_write_b128 v6, v[84:87] offset:11264
	s_waitcnt vmcnt(0)
	s_barrier
	v_add_u32 v32, 12288, v0
	v_add_u32 v33, 12288, v1
	v_add_u32 v34, 12288, v2
	v_add_u32 v35, 12288, v3
	ds_read_b64_tr_b16 v[40:41], v32 offset:0
	ds_read_b64_tr_b16 v[42:43], v32 offset:5120
	ds_read_b64_tr_b16 v[44:45], v32 offset:36864
	ds_read_b64_tr_b16 v[46:47], v32 offset:41984
	ds_read_b64_tr_b16 v[48:49], v33 offset:0
	ds_read_b64_tr_b16 v[50:51], v33 offset:5120
	ds_read_b64_tr_b16 v[52:53], v33 offset:36864
	ds_read_b64_tr_b16 v[54:55], v33 offset:41984
	ds_read_b64_tr_b16 v[56:57], v34 offset:0
	ds_read_b64_tr_b16 v[58:59], v34 offset:5120
	ds_read_b64_tr_b16 v[60:61], v34 offset:36864
	ds_read_b64_tr_b16 v[62:63], v34 offset:41984
	ds_read_b64_tr_b16 v[64:65], v35 offset:0
	ds_read_b64_tr_b16 v[66:67], v35 offset:5120
	ds_read_b64_tr_b16 v[68:69], v35 offset:36864
	ds_read_b64_tr_b16 v[70:71], v35 offset:41984
	ds_read_b64_tr_b16 v[72:73], v32 offset:256
	ds_read_b64_tr_b16 v[74:75], v32 offset:5376
	ds_read_b64_tr_b16 v[76:77], v32 offset:37120
	ds_read_b64_tr_b16 v[78:79], v32 offset:42240
	s_waitcnt lgkmcnt(0)
	s_barrier
	ds_write_b128 v6, v[40:43] offset:12288
	ds_write_b128 v6, v[44:47] offset:13312
	ds_write_b128 v6, v[48:51] offset:14336
	ds_write_b128 v6, v[52:55] offset:15360
	ds_write_b128 v6, v[56:59] offset:16384
	ds_write_b128 v6, v[60:63] offset:17408
	ds_write_b128 v6, v[64:67] offset:18432
	ds_write_b128 v6, v[68:71] offset:19456
	ds_write_b128 v6, v[72:75] offset:20480
	ds_write_b128 v6, v[76:79] offset:21504
	v_mov_b32 v15, s41
	v_mov_b32 v1, 0
	ds_write_b32 v15, v1
	s_waitcnt lgkmcnt(0)
	s_barrier
	s_memrealtime s[98:99]
	s_waitcnt lgkmcnt(0)
	s_mov_b32 s58, 0
	s_mov_b32 s74, 0
	s_mov_b32 s75, 0
	s_mov_b32 s19, 0
	s_mov_b32 s63, 128
	s_mov_b32 s65, 72
	s_mov_b32 s69, 0x20000
	s_sub_u32 s76, s98, s35
	s_cmp_gt_u32 s76, 0x10000
	s_cselect_b32 s60, s98, s35
	s_sub_u32 s60, s60, s63
.Lvo_c25:
	s_add_u32 s60, s60, 80
	s_sub_u32 s18, s60, s98
	s_cmp_lt_i32 s18, 0
	s_cbranch_scc1 .Lvo_c25
	s_add_u32 s60, s60, 7
	s_sub_u32 s18, s60, s35
	v_mov_b32 v115, s41
	v_mov_b32 v116, s18
	ds_max_u32 v115, v116
.Lvo_step26:
	s_add_u32 s76, s58, 0
	s_and_b32 s76, s76, 7
	s_lshl_b32 s76, s76, 22
	s_add_u32 s76, s76, s46
	s_add_u32 s71, s76, 67584
	s_sub_u32 s77, s76, s59
	s_mov_b32 s59, s76
	s_add_u32 s48, s48, s77
	s_add_u32 s49, s49, s77
	s_add_u32 s50, s50, s77
	s_add_u32 s43, s42, 65536
	v_add_u32 v2, s74, v7
	v_add_u32 v3, s74, v8
	v_add_u32 v14, s74, v9
	ds_read_b128 v[28:31], v6 offset:0
	ds_read_b128 v[32:35], v6 offset:1024
	ds_read_b128 v[36:39], v6 offset:6144
	ds_read_b128 v[40:43], v6 offset:7168
	ds_read_b128 v[44:47], v6 offset:2048
	ds_read_b128 v[48:51], v6 offset:3072
	v_mov_b64 v[92:93], 0
	v_mov_b64 v[94:95], 0
	v_mov_b64 v[96:97], 0
	v_mov_b64 v[98:99], 0
	v_mov_b64 v[100:101], 0
	v_mov_b64 v[102:103], 0
	v_mov_b64 v[104:105], 0
	v_mov_b64 v[106:107], 0
	v_mov_b64 v[108:109], 0
	v_mov_b64 v[110:111], 0
	v_add_u32 v113, s51, v112
	v_add_u32 v114, s52, v112
	v_add_u32 v68, s53, v112
	v_add_u32 v76, s54, v112
	v_add_u32 v84, s55, v112
	v_add_u32 v88, s56, v112
	v_mov_b64 v[16:17], 0
	v_mov_b64 v[18:19], 0
	v_mov_b64 v[20:21], 0
	v_mov_b64 v[22:23], 0
	v_mov_b64 v[24:25], 0
	v_mov_b64 v[26:27], 0
	s_cmp_lg_u32 s37, 1
	s_cselect_b32 s82, 2, 0
	s_memrealtime s[98:99]
	s_waitcnt lgkmcnt(0)
.Lvo_b028:
	s_mov_b32 s72, s60
	s_sub_u32 s18, s60, s98
	s_sub_u32 s18, s18, 8
	s_cmp_gt_u32 s18, 0xfa0
	s_cbranch_scc1 .Lvo_b028c
	s_cmp_eq_u32 s82, 2
	s_cbranch_scc1 .Lvo_b028r
	s_cmp_le_u32 s18, 6
	s_cbranch_scc1 .Lvo_w29b
.Lvo_w29:
	s_memrealtime s[98:99]
	s_waitcnt lgkmcnt(0)
	s_sub_u32 s18, s60, s98
	s_cmp_gt_i32 s18, 14
	s_cbranch_scc1 .Lvo_w29
.Lvo_w29b:
	s_memrealtime s[0:1]
	s_nop 1
	s_memrealtime s[2:3]
	s_nop 1
	s_memrealtime s[4:5]
	s_nop 0
	s_memrealtime s[6:7]
	s_nop 1
	s_memrealtime s[76:77]
	s_nop 1
	s_memrealtime s[78:79]
	s_nop 0
	s_memrealtime s[80:81]
	s_nop 1
	s_memrealtime s[98:99]
	s_lshl_b32 s18, s60, 3
	s_waitcnt lgkmcnt(0)
	s_add_u32 s0, s0, s2
	s_add_u32 s4, s4, s6
	s_add_u32 s76, s76, s78
	s_add_u32 s80, s80, s98
	s_add_u32 s0, s0, s4
	s_add_u32 s76, s76, s80
	s_getpc_b64 s[2:3]
.Lvo_w30:
	s_sub_u32 s18, s0, s18
	s_add_u32 s18, s18, s76
	s_mul_i32 s18, s18, 88
	s_ashr_i32 s18, s18, 8
	s_max_i32 s18, s18, -40
	s_min_i32 s18, s18, -8
	s_lshl2_add_u32 s18, s18, .Lvo_w31-.Lvo_w30+160
	s_add_u32 s2, s2, s18
	s_addc_u32 s3, s3, 0
	s_cmp_lg_u32 s82, 0
	s_setpc_b64 s[2:3]
	.p2align 6
.Lvo_w31:
	s_nop 1
	s_nop 1
	s_nop 1
	s_nop 1
	s_nop 1
	s_nop 1
	s_nop 1
	s_nop 1
	s_nop 1
	s_nop 1
	s_nop 1
	s_nop 1
	s_nop 1
	s_nop 1
	s_nop 1
	s_nop 1
	s_nop 1
	s_nop 1
	s_nop 1
	s_nop 1
	s_nop 1
	s_nop 1
	s_nop 1
	s_nop 1
	s_nop 1
	s_nop 1
	s_nop 1
	s_nop 1
	s_nop 1
	s_nop 1
	s_nop 1
	s_nop 1
.Lvo_b028x:
	s_cbranch_scc1 .Lvo_b028g
.Lvo_b028r:
	s_barrier
.Lvo_b028g:
	buffer_load_dwordx4 v[52:55], v5, s[20:23], s48 offen sc1
	s_nop 3
	buffer_load_dwordx4 v[56:59], v5, s[20:23], s49 offen sc1
	s_nop 3
	buffer_load_dwordx4 v[60:63], v5, s[20:23], s50 offen sc1
	s_sub_u32 s72, s72, 7
	s_waitcnt vmcnt(2)
	v_smfmac_f32_16x16x64_bf16 v[16:19], v[52:55], a[0:7], v4
	ds_write_b128 v113, v[52:55] offset:0
	v_smfmac_f32_16x16x64_bf16 v[20:23], v[52:55], a[8:15], v4
	v_smfmac_f32_16x16x64_bf16 v[24:27], v[52:55], a[16:23], v4
	s_waitcnt vmcnt(1)
	v_smfmac_f32_16x16x64_bf16 v[16:19], v[56:59], a[24:31], v4
	ds_write_b128 v113, v[56:59] offset:1024
	v_smfmac_f32_16x16x64_bf16 v[20:23], v[56:59], a[32:39], v4
	v_smfmac_f32_16x16x64_bf16 v[24:27], v[56:59], a[40:47], v4
	s_waitcnt vmcnt(0)
	v_smfmac_f32_16x16x64_bf16 v[16:19], v[60:63], a[48:55], v4
	ds_write_b128 v114, v[60:63] offset:0
	v_max3_u32 v115, v52, v56, v60
	v_cmp_eq_u32 vcc, -1, v115
	v_smfmac_f32_16x16x64_bf16 v[20:23], v[60:63], a[56:63], v4
	s_cbranch_vccnz .Lvo_f027
.Lvo_f027c:
	s_waitcnt lgkmcnt(0)
	s_barrier
	ds_read_b128 v[64:67], v68 offset:0
	ds_read_b128 v[68:71], v68 offset:1024
	ds_read_b128 v[72:75], v76 offset:0
	ds_read_b128 v[76:79], v76 offset:1024
	v_smfmac_f32_16x16x64_bf16 v[24:27], v[60:63], a[64:71], v4
	ds_read_b128 v[80:83], v84 offset:0
	ds_read_b128 v[84:87], v84 offset:1024
	s_waitcnt lgkmcnt(5)
	v_smfmac_f32_16x16x64_bf16 v[16:19], v[64:67], v[28:35], v4
	ds_read_b128 v[88:91], v88 offset:0
	ds_read_b128 v[28:31], v6 offset:8192
	ds_read_b128 v[32:35], v6 offset:9216
	s_waitcnt lgkmcnt(7)
	v_smfmac_f32_16x16x64_bf16 v[16:19], v[68:71], a[72:79], v4
	s_waitcnt lgkmcnt(6)
	v_smfmac_f32_16x16x64_bf16 v[16:19], v[72:75], a[96:103], v4
	s_waitcnt lgkmcnt(5)
	v_smfmac_f32_16x16x64_bf16 v[16:19], v[76:79], a[120:127], v4
	s_waitcnt lgkmcnt(4)
	v_smfmac_f32_16x16x64_bf16 v[16:19], v[80:83], v[36:43], v4
	ds_read_b128 v[36:39], v6 offset:4096
	ds_read_b128 v[40:43], v6 offset:5120
	s_waitcnt lgkmcnt(5)
	v_smfmac_f32_16x16x64_bf16 v[16:19], v[84:87], a[144:151], v4
	s_waitcnt lgkmcnt(4)
	v_smfmac_f32_16x16x64_bf16 v[16:19], v[88:91], a[168:175], v4
	v_smfmac_f32_16x16x64_bf16 v[20:23], v[64:67], v[44:51], v4
	ds_read_b128 v[44:47], v6 offset:10240
	ds_read_b128 v[48:51], v6 offset:11264
	v_smfmac_f32_16x16x64_bf16 v[20:23], v[68:71], a[80:87], v4
	v_smfmac_f32_16x16x64_bf16 v[20:23], v[72:75], a[104:111], v4
	v_add_f32 v16, v16, v18
	v_smfmac_f32_16x16x64_bf16 v[20:23], v[76:79], a[128:135], v4
	v_add_f32 v17, v17, v19
	s_waitcnt lgkmcnt(4)
	v_smfmac_f32_16x16x64_bf16 v[20:23], v[80:83], v[28:35], v4
	ds_read_b128 v[28:31], v6 offset:12288
	ds_read_b128 v[32:35], v6 offset:13312
	v_smfmac_f32_16x16x64_bf16 v[20:23], v[84:87], a[152:159], v4
	buffer_load_dword v1, off, s[20:23], s71 sc1
	v_smfmac_f32_16x16x64_bf16 v[20:23], v[88:91], a[176:183], v4
	s_waitcnt lgkmcnt(4)
	v_smfmac_f32_16x16x64_bf16 v[24:27], v[64:67], v[36:43], v4
	ds_read_b128 v[36:39], v6 offset:14336
	ds_read_b128 v[40:43], v6 offset:15360
	v_smfmac_f32_16x16x64_bf16 v[24:27], v[68:71], a[88:95], v4
	v_smfmac_f32_16x16x64_bf16 v[24:27], v[72:75], a[112:119], v4
	v_add_f32 v20, v20, v22
	v_add_f32 v21, v21, v23
	v_smfmac_f32_16x16x64_bf16 v[24:27], v[76:79], a[136:143], v4
	v_cvt_pk_bf16_f32 v18, v16, v20
	v_cvt_pk_bf16_f32 v19, v17, v21
	ds_write2_b32 v2, v18, v19 offset1:100
	s_waitcnt lgkmcnt(5)
	v_smfmac_f32_16x16x64_bf16 v[24:27], v[80:83], v[44:51], v4
	ds_read_b128 v[44:47], v6 offset:16384
	ds_read_b128 v[48:51], v6 offset:17408
	v_smfmac_f32_16x16x64_bf16 v[24:27], v[84:87], a[160:167], v4
	v_smfmac_f32_16x16x64_bf16 v[24:27], v[88:91], a[184:191], v4
	s_waitcnt lgkmcnt(2)
	s_barrier
	ds_read_b128 v[52:55], v14 offset:0
	ds_read_b128 v[56:59], v14 offset:64
	ds_read_b128 v[60:63], v14 offset:128
	ds_read_b128 v[64:67], v14 offset:192
	s_nop 1
	v_pk_add_f32 v[24:25], v[24:25], v[26:27]
	v_cvt_pk_bf16_f32 v22, v24, v25
	ds_write_b16 v3, v22
	ds_write_b16_d16_hi v3, v22 offset:400
	s_waitcnt lgkmcnt(0)
	s_barrier
	ds_read_b128 v[68:71], v14 offset:256
	ds_read_b128 v[72:75], v14 offset:320
	v_smfmac_f32_16x16x64_bf16 v[92:95], v[52:55], v[28:35], v4
	ds_read_b128 v[28:31], v6 offset:18432
	ds_read_b128 v[32:35], v6 offset:19456
	v_smfmac_f32_16x16x64_bf16 v[92:95], v[56:59], a[192:199], v4
	v_smfmac_f32_16x16x64_bf16 v[92:95], v[60:63], a[232:239], v4
	v_smfmac_f32_16x16x64_bf16 v[92:95], v[64:67], v[136:143], v4
	v_smfmac_f32_16x16x64_bf16 v[96:99], v[52:55], v[36:43], v4
	ds_read_b128 v[36:39], v6 offset:20480
	ds_read_b128 v[40:43], v6 offset:21504
	v_smfmac_f32_16x16x64_bf16 v[96:99], v[56:59], a[200:207], v4
	v_smfmac_f32_16x16x64_bf16 v[96:99], v[60:63], a[240:247], v4
	ds_read_b32 v23, v15
	v_smfmac_f32_16x16x64_bf16 v[96:99], v[64:67], v[144:151], v4
	s_waitcnt lgkmcnt(6)
	v_smfmac_f32_16x16x64_bf16 v[92:95], v[68:71], v[176:183], v4
	s_waitcnt lgkmcnt(5)
	v_smfmac_f32_16x16x64_bf16 v[92:95], v[72:75], v[216:223], v4
	v_smfmac_f32_16x16x64_bf16 v[96:99], v[68:71], v[184:191], v4
	v_smfmac_f32_16x16x64_bf16 v[96:99], v[72:75], v[224:231], v4
	v_smfmac_f32_16x16x64_bf16 v[100:103], v[52:55], v[44:51], v4
	v_cvt_pk_bf16_f32 v80, v92, v93
	v_smfmac_f32_16x16x64_bf16 v[100:103], v[56:59], a[208:215], v4
	v_cvt_pk_bf16_f32 v81, v94, v95
	v_smfmac_f32_16x16x64_bf16 v[100:103], v[60:63], a[248:255], v4
	v_cvt_pk_bf16_f32 v82, v96, v97
	v_smfmac_f32_16x16x64_bf16 v[100:103], v[64:67], v[152:159], v4
	v_cvt_pk_bf16_f32 v83, v98, v99
	v_smfmac_f32_16x16x64_bf16 v[100:103], v[68:71], v[192:199], v4
	v_permlane32_swap_b32 v80, v82
	v_smfmac_f32_16x16x64_bf16 v[100:103], v[72:75], v[232:239], v4
	v_permlane32_swap_b32 v81, v83
	s_waitcnt lgkmcnt(3)
	v_smfmac_f32_16x16x64_bf16 v[104:107], v[52:55], v[28:35], v4
	v_permlane16_swap_b32 v80, v81
	v_smfmac_f32_16x16x64_bf16 v[104:107], v[56:59], a[216:223], v4
	v_permlane16_swap_b32 v82, v83
	v_smfmac_f32_16x16x64_bf16 v[104:107], v[60:63], v[120:127], v4
	buffer_store_dwordx4 v[80:83], v10, s[20:23], s42 offen sc1
	v_cvt_pk_bf16_f32 v84, v100, v101
	v_smfmac_f32_16x16x64_bf16 v[104:107], v[64:67], v[160:167], v4
	v_cvt_pk_bf16_f32 v85, v102, v103
	v_smfmac_f32_16x16x64_bf16 v[104:107], v[68:71], v[200:207], v4
	v_smfmac_f32_16x16x64_bf16 v[104:107], v[72:75], v[240:247], v4
	s_waitcnt lgkmcnt(1)
	v_smfmac_f32_16x16x64_bf16 v[108:111], v[52:55], v[36:43], v4
	v_smfmac_f32_16x16x64_bf16 v[108:111], v[56:59], a[224:231], v4
	v_smfmac_f32_16x16x64_bf16 v[108:111], v[60:63], v[128:135], v4
	v_cvt_pk_bf16_f32 v86, v104, v105
	v_smfmac_f32_16x16x64_bf16 v[108:111], v[64:67], v[168:175], v4
	v_cvt_pk_bf16_f32 v87, v106, v107
	v_smfmac_f32_16x16x64_bf16 v[108:111], v[68:71], v[208:215], v4
	v_permlane32_swap_b32 v84, v86
	v_permlane32_swap_b32 v85, v87
	v_smfmac_f32_16x16x64_bf16 v[108:111], v[72:75], v[248:255], v4
	s_nop 0
	v_permlane16_swap_b32 v84, v85
	v_permlane16_swap_b32 v86, v87
	buffer_store_dwordx4 v[84:87], v10, s[20:23], s43 offen sc1
	s_nop 3
	v_cvt_pk_bf16_f32 v88, v108, v109
	v_cvt_pk_bf16_f32 v90, v108, v109
	v_cvt_pk_bf16_f32 v89, v110, v111
	v_cvt_pk_bf16_f32 v91, v110, v111
	v_permlane32_swap_b32 v88, v90
	s_nop 0
	v_permlane32_swap_b32 v89, v91
	s_nop 1
	v_permlane16_swap_b32 v88, v89
	v_permlane16_swap_b32 v90, v91
	buffer_store_dwordx4 v[88:91], v11, s[20:23], s42 offen sc1
	s_memrealtime s[98:99]
	s_waitcnt lgkmcnt(0)
	v_readfirstlane_b32 s70, v23
	s_sub_u32 s18, s60, s35
	s_max_u32 s70, s70, s18
	s_add_u32 s70, s70, s35
	s_cmp_lg_u32 s37, 0
	s_cbranch_scc1 .Lvo_l32
	s_sub_u32 s76, s98, s72
	s_min_u32 s76, s76, 0x3000
	s_pack_ll_b32_b16 s77, s58, s76
	s_and_b32 s18, s19, 255
	s_min_u32 s18, s18, 1
	s_lshl_b32 s18, s18, 31
	s_or_b32 s77, s77, s18
	v_mov_b32 v16, s77
	s_cmp_eq_u32 s60, s70
	s_cselect_b64 exec, 1, 0
	s_mov_b32 s19, 0
	buffer_store_dword v16, off, s[20:23], s38 sc1
	s_mov_b64 exec, -1
.Lvo_l32:
	s_add_u32 s42, s42, 4194304
	s_sub_u32 s18, s42, s36
	s_cmp_eq_u32 s18, 12582912
	s_cselect_b32 s42, s36, s42
	s_waitcnt vmcnt(3)
	v_readfirstlane_b32 s76, v1
	s_add_u32 s60, s70, s63
	s_and_b32 s77, s76, 1023
	s_add_u32 s60, s60, s77
	s_bfe_u32 s63, s76, 0xa000a
	s_xor_b32 s48, s66, s48
	s_xor_b32 s49, s66, s49
	s_xor_b32 s50, s66, s50
	s_mov_b32 s66, 0
	s_sub_u32 s74, 3200, s74
	s_add_u32 s58, s58, 1
	s_cmp_lt_u32 s58, 64
	s_cbranch_scc1 .Lvo_step26
	s_branch .Lvo_end33
.Lvo_f027:
	s_cmp_eq_u32 s69, 0
	s_cbranch_scc1 .Lvo_f027c
	s_sub_u32 s69, s69, 1
	s_memrealtime s[98:99]
	s_add_u32 s19, s19, 1
	s_mov_b32 s82, 1
	v_mov_b64 v[16:17], 0
	v_mov_b64 v[18:19], 0
	v_mov_b64 v[20:21], 0
	v_mov_b64 v[22:23], 0
	v_mov_b64 v[24:25], 0
	v_mov_b64 v[26:27], 0
	s_waitcnt vmcnt(0) lgkmcnt(0)
.Lvo_c34:
	s_add_u32 s60, s60, 80
	s_sub_u32 s18, s60, s98
	s_cmp_lt_i32 s18, -8
	s_cbranch_scc1 .Lvo_c34
	s_sub_u32 s18, s60, s35
	v_mov_b32 v115, s41
	v_mov_b32 v116, s18
	ds_max_u32 v115, v116
	s_branch .Lvo_b028
.Lvo_b028c:
	s_sub_u32 s18, s98, s60
	s_cmp_gt_i32 s18, 24
	s_cbranch_scc1 .Lvo_b028l
	s_cmp_eq_u32 s82, 2
	s_cbranch_scc1 .Lvo_b028r
.Lvo_b028d:
	s_sub_u32 s18, s60, s98
	s_sub_u32 s18, s18, 3
	s_cmp_lt_u32 s18, 0xfa0
	s_cbranch_scc1 .Lvo_b028e
	s_cmp_lg_u32 s82, 0
	s_branch .Lvo_b028x
.Lvo_b028e:
	s_memrealtime s[98:99]
	s_waitcnt lgkmcnt(0)
	s_branch .Lvo_b028d
.Lvo_b028l:
	s_mov_b32 s72, s98
	s_add_u32 s19, s19, 1
.Lvo_c35:
	s_add_u32 s60, s60, 80
	s_sub_u32 s18, s60, s98
	s_cmp_lt_i32 s18, -8
	s_cbranch_scc1 .Lvo_c35
	s_barrier
	s_sub_u32 s18, s60, s35
	v_mov_b32 v115, s41
	v_mov_b32 v116, s18
	ds_max_u32 v115, v116
	s_branch .Lvo_b028g
.Lvo_end33:
	s_endpgm
.Lvo_x1:
	s_waitcnt vmcnt(0) lgkmcnt(0)
	s_mov_b32 s47, -1
	s_mov_b32 m0, s19
)ASM" : "+{v[0:15]}"(vi), "+{v[16:31]}"(vj), "+{s[32:47]}"(si) : "{s[20:23]}"(rs), "{s[24:27]}"(rv), "{s[28:31]}"(ro) : "a0", "a1", "a2", "a3", "a4", "a5", "a6", "a7", "a8", "a9", "a10", "a11", "a12", "a13", "a14", "a15", "a16", "a17", "a18", "a19", "a20", "a21", "a22", "a23", "a24", "a25", "a26", "a27", "a28", "a29", "a30", "a31", "a32", "a33", "a34", "a35", "a36", "a37", "a38", "a39", "a40", "a41", "a42", "a43", "a44", "a45", "a46", "a47", "a48", "a49", "a50", "a51", "a52", "a53", "a54", "a55", "a56", "a57", "a58", "a59", "a60", "a61", "a62", "a63", "a64", "a65", "a66", "a67", "a68", "a69", "a70", "a71", "a72", "a73", "a74", "a75", "a76", "a77", "a78", "a79", "a80", "a81", "a82", "a83", "a84", "a85", "a86", "a87", "a88", "a89", "a90", "a91", "a92", "a93", "a94", "a95", "a96", "a97", "a98", "a99", "a100", "a101", "a102", "a103", "a104", "a105", "a106", "a107", "a108", "a109", "a110", "a111", "a112", "a113", "a114", "a115", "a116", "a117", "a118", "a119", "a120", "a121", "a122", "a123", "a124", "a125", "a126", "a127", "a128", "a129", "a130", "a131", "a132", "a133", "a134", "a135", "a136", "a137", "a138", "a139", "a140", "a141", "a142", "a143", "a144", "a145", "a146", "a147", "a148", "a149", "a150", "a151", "a152", "a153", "a154", "a155", "a156", "a157", "a158", "a159", "a160", "a161", "a162", "a163", "a164", "a165", "a166", "a167", "a168", "a169", "a170", "a171", "a172", "a173", "a174", "a175", "a176", "a177", "a178", "a179", "a180", "a181", "a182", "a183", "a184", "a185", "a186", "a187", "a188", "a189", "a190", "a191", "a192", "a193", "a194", "a195", "a196", "a197", "a198", "a199", "a200", "a201", "a202", "a203", "a204", "a205", "a206", "a207", "a208", "a209", "a210", "a211", "a212", "a213", "a214", "a215", "a216", "a217", "a218", "a219", "a220", "a221", "a222", "a223", "a224", "a225", "a226", "a227", "a228", "a229", "a230", "a231", "a232", "a233", "a234", "a235", "a236", "a237", "a238", "a239", "a240", "a241", "a242", "a243", "a244", "a245", "a246", "a247", "a248", "a249", "a250", "a251", "a252", "a253", "a254", "a255", "s18", "s19", "s48", "s49", "s50", "s51", "s52", "s53", "s54", "s55", "s56", "s58", "s59", "s60", "s63", "s64", "s65", "s66", "s67", "s69", "s70", "s71", "s72", "s74", "s75", "s76", "s77", "s78", "s79", "s80", "s81", "s82", "s98", "s99", "v32", "v33", "v34", "v35", "v36", "v37", "v38", "v39", "v40", "v41", "v42", "v43", "v44", "v45", "v46", "v47", "v48", "v49", "v50", "v51", "v52", "v53", "v54", "v55", "v56", "v57", "v58", "v59", "v60", "v61", "v62", "v63", "v64", "v65", "v66", "v67", "v68", "v69", "v70", "v71", "v72", "v73", "v74", "v75", "v76", "v77", "v78", "v79", "v80", "v81", "v82", "v83", "v84", "v85", "v86", "v87", "v88", "v89", "v90", "v91", "v92", "v93", "v94", "v95", "v96", "v97", "v98", "v99", "v100", "v101", "v102", "v103", "v104", "v105", "v106", "v107", "v108", "v109", "v110", "v111", "v112", "v113", "v114", "v115", "v116", "v120", "v121", "v122", "v123", "v124", "v125", "v126", "v127", "v128", "v129", "v130", "v131", "v132", "v133", "v134", "v135", "v136", "v137", "v138", "v139", "v140", "v141", "v142", "v143", "v144", "v145", "v146", "v147", "v148", "v149", "v150", "v151", "v152", "v153", "v154", "v155", "v156", "v157", "v158", "v159", "v160", "v161", "v162", "v163", "v164", "v165", "v166", "v167", "v168", "v169", "v170", "v171", "v172", "v173", "v174", "v175", "v176", "v177", "v178", "v179", "v180", "v181", "v182", "v183", "v184", "v185", "v186", "v187", "v188", "v189", "v190", "v191", "v192", "v193", "v194", "v195", "v196", "v197", "v198", "v199", "v200", "v201", "v202", "v203", "v204", "v205", "v206", "v207", "v208", "v209", "v210", "v211", "v212", "v213", "v214", "v215", "v216", "v217", "v218", "v219", "v220", "v221", "v222", "v223", "v224", "v225", "v226", "v227", "v228", "v229", "v230", "v231", "v232", "v233", "v234", "v235", "v236", "v237", "v238", "v239", "v240", "v241", "v242", "v243", "v244", "v245", "v246", "v247", "v248", "v249", "v250", "v251", "v252", "v253", "v254", "v255", "vcc", "scc", "memory");
}
template <int NH> DI void tok_core(KArgs& a, rsrc_t rs, ldsp lds, const u32x4 (&U)[16], const int hb, const int s, const int cg, const int h0, const int x,
                                   const unsigned sd, const unsigned need, unsigned& prog, const int lane, const int wv, bool& dead) {
  const int g = lane >> 4, c = lane & 15, tid = wv * 64 + lane;
  const unsigned mlo = s * BLK + uoff(sd, cg, 32 + 2 * wv + (g >> 1)) + (2 * h0 + (g & 1)) * 256 + c * 16;
  u32x4 ml[NH][2];
  unsigned xv[NH];
  const unsigned oe = (s * 32 + 16 * h0 + (tid >> 4)) * 640 + cg * 32 + 2 * (tid & 15);
#pragma unroll
  for (int i = 0; i < NH; ++i) {
    ml[i][0] = ld4(rs, mlo + i * 512, x * 1024); ml[i][1] = ld4(rs, mlo + i * 512, 1024 - x * 1024);
    xv[i] = TX == 1 ? 0u : __builtin_amdgcn_raw_buffer_load_b32(mkrs(PW ? a.out : a.x, 2048u * 1280u), (oe + i * 16 * 640) * 2, 0, PW ? SC1 : TX);
  }
  const unsigned bv = *(const unsigned*)(a.b2 + cg * 32 + 2 * (tid & 15));
  u32x4 ident;
#pragma unroll
  for (int j = 0; j < 4; ++j) ident[j] = (8 * (g & 1) + 2 * j == c ? 0x3F80u : 0u) | (8 * (g & 1) + 2 * j + 1 == c ? 0x3F800000u : 0u);
  if (NH == 1) wait_t(need); else wait_flag(rs, progo(sd), need, prog, dead);
  const bool ar = s < NSEG - 3;
  const unsigned yo = ar ? nb(sd, s) + t1p(sd, (hb + g) & 31, cg * 512 + c * 16) : (NB + s % 3) * BLK + na(sd, 208 + 2 * cg + (hb >> 4)) + ((hb & 15) + g) * 512 + c * 16, y4 = ar ? 6 * UN : 2048;
  const unsigned txo = rfl(nb(sd, s) + pxoff(h0, hb)), tmo = nb(sd, s) + pmoff(h0, hb + g) + c * 16;
  u32x4 T[4 + 10 * NH];
  poll4<4 + 10 * NH>(T, [&](int i) {
    if (i < 4) return ld4(rs, yo, (i >> 1) * y4 + ((i & 1) ^ x) * 256);
    const int hh = (i - 4) / 10, k = (i - 4) % 10;
    return k < 8 ? ld4(rs, (lane ^ cg / 10 * 16) * 16 + (k & 3) * 1024, txo + hh * 4 * UN + (k >> 2) * 4096) : ld4(rs, tmo + (k - 8) * 1024, hh * UN);
  }, dead);
  if (cg / 5 & 1) {
#pragma unroll
    for (int i = 4; i < 4 + 10 * NH; ++i) T[i] = __builtin_shufflevector(T[i], T[i], 2, 3, 0, 1);
  }
  LAS float* red2 = (LAS float*)(lds + 16384);
#pragma unroll
  for (int i = 0; i < NH; ++i) {
    f32x4 o0 = mf16(ident, ml[i][0], (f32x4){0.f, 0.f, 0.f, 0.f}), o1 = mf16(ident, ml[i][1], (f32x4){0.f, 0.f, 0.f, 0.f});
#pragma unroll
    for (int k = 0; k < 8; ++k) { o0 = mf16(T[4 + 10 * i + k], U[2 * k], o0); o1 = mf16(T[4 + 10 * i + k], U[2 * k + 1], o1); }
#pragma unroll
    for (int u = 0; u < 2; ++u) {
      o0 = mf16(T[12 + 10 * i + u], T[2 * u], o0);
      o1 = mf16(T[12 + 10 * i + u], T[2 * u + 1], o1);
    }
    LAS float* rp = red2 + i * 2112 + wv * 528 + 4 * g * 33 + c;
#pragma unroll
    for (int j = 0; j < 4; ++j) { rp[j * 33 + 16 * x] = o0[j]; rp[j * 33 + 16 - 16 * x] = o1[j]; }
  }
  bar();
#pragma unroll
  for (int i = 0; i < NH; ++i) {
    LAS float* rp = red2 + i * 2112 + (tid >> 4) * 33 + 2 * (tid & 15);
    float v0 = (rp[0] + rp[528]) + (rp[1056] + rp[1584]), v1 = (rp[1] + rp[529]) + (rp[1057] + rp[1585]);
    v0 += __uint_as_float(bv << 16) + __uint_as_float(xv[i] << 16); v1 += __uint_as_float(bv & 0xFFFF0000u) + __uint_as_float(xv[i] & 0xFFFF0000u);
    __builtin_amdgcn_raw_buffer_store_b32(cvtpk(v0, v1), mkrs(a.out, 2048u * 1280u), TO == 1 ? OOB : (oe + i * 16 * 640) * 2, 0, TO);
  }
}

DI void tok_wide(KArgs& a, rsrc_t rs, ldsp lds, const int s, const int w, const unsigned sd, const int lane, const int wv) {
  const int g = lane >> 4, c = lane & 15, tid = wv * 64 + lane, hb = 8 * wv;
  rsrc_t ro = mkrs(a.out, 2048u * 1280u), rb = mkrs(a.b2, 1280u);
  const unsigned us = rfl(s < ES ? (UR + s) * BLK + unoff(sd, 5 * w, hb) : s * BLK + uoff(sd, 5 * w, hb)), ud = s < ES ? 8 * UN : 10 * UN;
  const unsigned mlo = s * BLK + uoff(sd, 5 * w, 32 + 2 * wv + (g >> 1)) + (g & 1) * 256 + c * 16;
  const unsigned bo = w * 320 + 4 * (tid & 15), oe = (s * 32 + (tid >> 4)) * 1280 + bo, ns = rfl(nb(sd, s));
  u32x4 T[20], U[24];
  unsigned xv[2][3];
  auto nxt = [&](int k, int i) {
    if (k == 5) return;
    U[i] = i < 4 ? __builtin_amdgcn_raw_buffer_load_b128(rs, mlo + i * 512, k * 10 * UN, TK0) : i < 20 ? __builtin_amdgcn_raw_buffer_load_b128(rs, lane * 16 + (i & 3) * 1024, us + k * ud + (~i >> 2 & 1) * 4096 + ((i - 4) >> 3) * UN, TK0)
                 : ld4(rs, t1p(sd, (hb + g) & 31, (5 * w + k) * 512 + c * 16) + (i & 1) * 256, ns + (i >> 1 & 1) * 6 * UN);
    if (i < 23) return;
    xv[k & 1][0] = ld1(ro, oe + k * 64, 0); xv[k & 1][1] = ld1(ro, oe + k * 64, 20480); xv[k & 1][2] = __builtin_amdgcn_raw_buffer_load_b32(rb, bo + k * 64, 0, 0);
  };
#pragma unroll
  for (int i = 0; i < 20; ++i) {
    const int hh = i & 1, k = i >> 1;
    T[i] = k < 8 ? ld4(rs, (lane ^ (w >> 1) * 16) * 16 + (k & 3) * 1024, ns + pxoff(hh, hb) + (k >> 2) * 4096) : ld4(rs, pmoff(0, hb + g) + c * 16 + (k - 8) * 1024, ns + hh * UN);
  }
#pragma unroll
  for (int i = 0; i < 24; ++i) nxt(0, i);
  u32x4 ident;
#pragma unroll
  for (int j = 0; j < 4; ++j) ident[j] = (8 * (g & 1) + 2 * j == c ? 0x3F80u : 0u) | (8 * (g & 1) + 2 * j + 1 == c ? 0x3F800000u : 0u);
  if (w & 1) {
#pragma unroll
    for (int i = 0; i < 20; ++i) T[i] = __builtin_shufflevector(T[i], T[i], 2, 3, 0, 1);
  }
#pragma unroll
  for (int k = 0; k < 5; ++k) {
    f32x4 o[4];
    SB();
#pragma unroll
    for (int i = 0; i < 4; ++i) o[i] = mf16(ident, U[i], (f32x4){0.f, 0.f, 0.f, 0.f});
#pragma unroll
    for (int i = 0; i < 4; ++i) nxt(k + 1, i);
#pragma unroll
    for (int j = 0; j < 10; ++j) {
      SB();
#pragma unroll
      for (int i = 0; i < 4; ++i) o[i] = mf16(T[2 * j + (i & 1)], U[4 + 2 * j + (i >> 1)], o[i]);
      nxt(k + 1, 4 + 2 * j); nxt(k + 1, 5 + 2 * j);
    }
    SB();
    LAS float* red2 = (LAS float*)(lds + 16384) + (k & 1) * 4224;
#pragma unroll
    for (int i = 0; i < 16; ++i) red2[(i >> 2 & 1) * 2112 + wv * 528 + (4 * g + (i & 3)) * 33 + c + (i >> 3) * 16] = o[i >> 2][i & 3];
    bar();
#pragma unroll
    for (int i = 0; i < 2; ++i) {
      LAS float* rp = red2 + i * 2112 + (tid >> 4) * 33 + 2 * (tid & 15);
      const f32x2 e = unpk(xv[k & 1][2]) + unpk(xv[k & 1][i]);
      const float v0 = (rp[0] + rp[528]) + (rp[1056] + rp[1584]) + e.x, v1 = (rp[1] + rp[529]) + (rp[1057] + rp[1585]) + e.y;
      __builtin_amdgcn_raw_buffer_store_b32(cvtpk(v0, v1), ro, oe + k * 64, i * 20480, TO);
    }
  }
}

DI void role_tok(KArgs& a, const unsigned sd, ldsp lds, const int lane, const int wv) {
  rsrc_t rs = mkrs(a.ws, WS_RANGE);
  bool dead = false;
  unsigned seen = 0, prog = 0;
  const unsigned fo = flag2(sd, lane < 8 ? 8 * wv + lane : 32 + 2 * wv + (lane & 1));
  for (int it = 0;; ++it) {
    bar();
    if (wv == 0 && lane == 0) sts1(lds, __hip_atomic_fetch_add((unsigned*)(a.ws + progo(sd) + 256), 1u, __ATOMIC_RELAXED, __HIP_MEMORY_SCOPE_AGENT));
    bar();
    const unsigned item = rfl(*(LAS unsigned*)lds), fine = item + 16 * SW;
    if (fine >= (NSEG - 1) * 20u) break;
    const int s = item < 4 * SW ? item / 4 : fine / 20, cg = fine % 20;
    wait_flag(rs, fo, max(s < ES ? s : max(s, PA), s < 8 ? 8 : 16) + 1, seen, dead);
    if (s < NSEG - 4) wait_flag(rs, progo(sd), s + 3, prog, dead);
    memfence();
    if (item < 4 * SW) {
      tok_wide(a, rs, lds, s, item & 3, sd, lane, wv);
      continue;
    }
    u32x4 U[16];
    const unsigned us = rfl(s * BLK + uoff(sd, cg, 8 * wv));
#pragma unroll
    for (int i = 0; i < 16; ++i) U[i] = ld4(rs, lane * 16 + (i & 3) * 1024, us + ((i >> 2) & 1) * 4096 + (i >> 3) * UN);
    tok_core<2>(a, rs, lds, U, 8 * wv, s, cg, 0, 0, sd, s < NSEG - 3 ? s + 3 : s + 1, prog, lane, wv, dead);
  }
}

DI unsigned role_red(KArgs& a, const int r, const unsigned hp, const unsigned exp, ldsp lds, const int lane, const int wv) {
  wait_t(rtl() + RW * !STMP);
  const unsigned nr = rfl(hs_wait(mkrs(a.ws, WS_RANGE), OFF_ARR + ((hp >> 8) & 1024u), exp, lane)), sd = nr >> 13 & 1, mm = nr & 20480u, mx = mm ^ qb(r / 10);
  if (nr == 1) return 2;
  {
    const unsigned n = r * 32 + (lane & 31), t = ld1(mkrs(a.ws, WS_RANGE), n < 1024 && wv == 0 ? rings(sd).x + (n >> 7) * BLK + U_PMM + qb(n >> 5 & 3) + (n & 31) * 128 : OOB, 0);
    asm volatile("" ::"v"(t));
  }
  SB();
  const int cg = r >> 1, x = r & 1, g = lane >> 4, c = lane & 15, q = c >> 2, p = c & 3;
  const int hb = 8 * ((wv + r) & 3), half = (c >> 1) & 1;
  const u32x2 rg = rings(sd);
  u32x16 vi = (u32x16)0u, si = (u32x16)0u;
  vi[4] = half ? 0xEEEE : 0x4444;
  vi[5] = lane * 16;
  vi[6] = cg * 32768 + g * 512 + x * 256 + c * 16;
  vi[0] = t1p(sd, g, cg * 512 + x * 256 + c * 16);
  vi[2] = flag2(sd, 32 + 2 * wv + (lane & 1));
  vi[7] = U_PMX + mx + hb * 4096 + g * 256 + q * 64 + (c & 1) * 32 + half * 16;
  vi[8] = U_PMM + mx + g * 256 + (q >> 1) * 128 + half * 64 + (q & 1) * 32 + (c & 1) * 16;
  vi[9] = (r >> 4) * 32768 + (r & 15) * 256 + g * 64 + (p & 1) * 32 + half * 16 + (mm ^ qb(q));
  vi[10] = la(lds) + wv * 512 + lane * 8;
  vi[11] = p & 1 ? 0x03020706 : 0x05040100;
  vi[12] = flag2(sd, hb + (lane & 7));
  vi[13] = lane < 40 ? U_RM + lane * 128 : OOB;
  vi[14] = OFF_DBG + ((2 * g + (p & 1)) * 640 + r * 16 + (c & 14)) * 2;
  vi[15] = U_QM + lane * 128;
  si[0] = (UR < 64 ? unoff(sd, cg, hb) : uoff(sd, cg, hb)) + x * 1024; si[4] = rg.y; si[5] = opq(wv); si[6] = rg.x + U_RM + r * 128; si[7] = r == 0; si[10] = opq(exp); si[11] = OFF_ARR + ((hp >> 8) & 1024u); si[2] = r; si[3] = nb(sd, 0); si[8] = opq(sd); si[12] = progo(sd); si[13] = r == 0 && wv == 1; si[9] = la(lds) + 10240;
  si[14] = rg.x; si[15] = dbgo(64 + r, wv);
#pragma unroll
  for (int i = 0; i < 16; ++i) si[i] = rfl(si[i]);
  const u32x4 rq = rsw(a.ws, WS_RANGE);
  u32x16 u0, u1, u2, u3;
  asm volatile(R"ASM(
	s_mov_b32 s58, 0
	s_mov_b32 s73, 0
	s_mov_b32 s69, 0x20000
	v_mov_b32 v19, s41
	v_mov_b32 v18, 0
	ds_write2_b32 v19, v18, v18 offset0:2 offset1:3
	s_add_u32 s41, s41, 12
	s_mov_b32 s64, 0
	s_add_u32 s78, s58, 0
	s_and_b32 s52, s78, 15
	s_cmp_lt_u32 s78, 8
	s_cselect_b32 s18, 16, 0
	s_add_u32 s52, s52, s18
	s_lshl_b32 s52, s52, 22
	s_add_u32 s52, s52, s32
	s_add_u32 s51, s78, 1
	s_add_u32 s53, s52, 4096
	s_add_u32 s54, s52, 16384
	s_add_u32 s55, s52, 20480
.Lrd_f1:
	s_cmp_ge_u32 s73, s51
	s_cbranch_scc1 .Lrd_f2
	s_cmp_eq_u32 s69, 0
	s_cbranch_scc1 .Lrd_f2
	s_sub_u32 s69, s69, 1
	buffer_load_dword v21, v12, s[20:23], 0 offen sc1
	s_waitcnt vmcnt(0)
	s_nop 0
	v_min_u32_dpp v21, v21, v21 row_ror:8 row_mask:0xf bank_mask:0xf
	s_nop 1
	v_min_u32_dpp v21, v21, v21 row_ror:4 row_mask:0xf bank_mask:0xf
	s_nop 1
	v_min_u32_dpp v21, v21, v21 row_ror:2 row_mask:0xf bank_mask:0xf
	s_nop 1
	v_min_u32_dpp v21, v21, v21 row_ror:1 row_mask:0xf bank_mask:0xf
	s_nop 0
	v_readfirstlane_b32 s73, v21
	s_sleep 2
	s_branch .Lrd_f1
.Lrd_f2:
	buffer_load_dwordx4 v[64:67], v5, s[20:23], s52 offen offset:0 sc1
	buffer_load_dwordx4 v[68:71], v5, s[20:23], s52 offen offset:2048 sc1
	buffer_load_dwordx4 v[72:75], v5, s[20:23], s53 offen offset:0 sc1
	buffer_load_dwordx4 v[76:79], v5, s[20:23], s53 offen offset:2048 sc1
	buffer_load_dwordx4 v[80:83], v5, s[20:23], s54 offen offset:0 sc1
	buffer_load_dwordx4 v[84:87], v5, s[20:23], s54 offen offset:2048 sc1
	buffer_load_dwordx4 v[88:91], v5, s[20:23], s55 offen offset:0 sc1
	buffer_load_dwordx4 v[92:95], v5, s[20:23], s55 offen offset:2048 sc1
	s_mov_b32 s70, s42
	s_mov_b32 s71, s43
	buffer_load_dwordx4 v[24:27], v5, s[20:23], s71 offen offset:0 sc1
	buffer_load_dword v28, off, s[20:23], s71 offset:2048 sc1
.Lrd_h3:
	s_waitcnt vmcnt(1)
	v_xor_b32 v24, s70, v24
	v_xor_b32 v25, s70, v25
	v_xor_b32 v26, s70, v26
	v_xor_b32 v27, s70, v27
	v_or3_b32 v21, v24, v25, v26
	v_or_b32 v21, v21, v27
	v_and_b32 v22, 0xffffff17, v21
	v_cmp_ne_u32 vcc, 0, v22
	s_cbranch_vccz .Lrd_h4
	s_cmp_eq_u32 s69, 0
	s_cbranch_scc1 .Lrd_h4
	s_sub_u32 s69, s69, 1
	s_sleep 2
	buffer_load_dwordx4 v[24:27], v5, s[20:23], s71 offen offset:0 sc1
	buffer_load_dword v28, off, s[20:23], s71 offset:2048 sc1
	s_waitcnt vmcnt(0)
	s_branch .Lrd_h3
.Lrd_h4:
	s_waitcnt vmcnt(0)
	v_readfirstlane_b32 s33, v28
	s_waitcnt lgkmcnt(0)
	s_barrier
	s_memrealtime s[98:99]
	s_waitcnt lgkmcnt(0)
	s_mov_b32 s58, 0
	s_mov_b32 s74, 0
	s_mov_b32 s75, 0
	s_mov_b32 s19, 0
	s_mov_b32 s63, 128
	s_mov_b32 s65, 72
	s_mov_b32 s69, 0x20000
	s_sub_u32 s76, s98, s33
	s_cmp_gt_u32 s76, 0x10000
	s_cselect_b32 s61, s98, s33
.Lrd_c6:
	s_add_u32 s61, s61, 80
	s_sub_u32 s18, s61, s98
	s_cmp_lt_i32 s18, 0
	s_cbranch_scc1 .Lrd_c6
	s_mov_b32 s62, s61
	s_mov_b32 s66, 124
	s_mov_b32 s67, 117
	s_mov_b32 s68, 64
	s_sub_u32 s18, s61, s33
	v_mov_b32 v31, s41
	v_mov_b32 v21, s18
	ds_max_u32 v31, v21
	s_mov_b32 s43, s36
	v_mov_b64 v[132:133], -1
	v_mov_b64 v[134:135], -1
	s_cmp_lg_u32 s37, 0
	s_cbranch_scc1 .Lrd_h7
	ds_write2st64_b64 v10, v[132:133], v[132:133] offset0:1 offset1:2
	ds_write_b64 v10, v[132:133] offset:1536
.Lrd_step9:
	s_add_u32 s59, s58, 0
	s_and_b32 s59, s59, 7
	s_lshl_b32 s59, s59, 22
	s_add_u32 s59, s59, s46
	s_add_u32 s71, s59, 67584
	buffer_load_dword v1, off, s[20:23], s71 sc1
	s_add_u32 s42, s58, 1
	s_and_b32 s42, s42, 7
	s_lshl_b32 s42, s42, 22
	s_add_u32 s42, s42, s46
	s_add_u32 s50, s43, 4096
	s_add_u32 s56, s43, 16384
	s_add_u32 s57, s43, 20480
.Lrd_b11z:
	v_mov_b64 v[128:129], 0
	v_mov_b64 v[130:131], 0
	s_memrealtime s[98:99]
	s_waitcnt lgkmcnt(0)
.Lrd_b11:
	s_mov_b32 s72, s61
	s_sub_u32 s18, s61, s98
	s_sub_u32 s18, s18, 8
	s_cmp_gt_u32 s18, 0xfa0
	s_cbranch_scc1 .Lrd_b11c
	s_cmp_le_u32 s18, 6
	s_cbranch_scc1 .Lrd_w20b
.Lrd_w20:
	s_memrealtime s[98:99]
	s_waitcnt lgkmcnt(0)
	s_sub_u32 s18, s61, s98
	s_cmp_gt_i32 s18, 14
	s_cbranch_scc1 .Lrd_w20
.Lrd_w20b:
	s_memrealtime s[48:49]
	s_nop 1
	s_memrealtime s[52:53]
	s_nop 1
	s_memrealtime s[54:55]
	s_nop 0
	s_memrealtime s[62:63]
	s_nop 1
	s_memrealtime s[76:77]
	s_nop 1
	s_memrealtime s[78:79]
	s_nop 0
	s_memrealtime s[70:71]
	s_nop 1
	s_memrealtime s[98:99]
	s_lshl_b32 s18, s61, 3
	s_waitcnt lgkmcnt(0)
	s_add_u32 s48, s48, s52
	s_add_u32 s54, s54, s62
	s_add_u32 s76, s76, s78
	s_add_u32 s70, s70, s98
	s_add_u32 s48, s48, s54
	s_add_u32 s76, s76, s70
	s_getpc_b64 s[52:53]
.Lrd_w21:
	s_sub_u32 s18, s48, s18
	s_add_u32 s18, s18, s76
	s_mul_i32 s18, s18, 88
	s_ashr_i32 s18, s18, 8
	s_max_i32 s18, s18, -40
	s_min_i32 s18, s18, -8
	s_lshl2_add_u32 s18, s18, .Lrd_w22-.Lrd_w21+160
	s_add_u32 s52, s52, s18
	s_addc_u32 s53, s53, 0
	s_cmp_lg_u32 s82, 0
	s_setpc_b64 s[52:53]
	.p2align 6
.Lrd_w22:
	s_nop 1
	s_nop 1
	s_nop 1
	s_nop 1
	s_nop 1
	s_nop 1
	s_nop 1
	s_nop 1
	s_nop 1
	s_nop 1
	s_nop 1
	s_nop 1
	s_nop 1
	s_nop 1
	s_nop 1
	s_nop 1
	s_nop 1
	s_nop 1
	s_nop 1
	s_nop 1
	s_nop 1
	s_nop 1
	s_nop 1
	s_nop 1
	s_nop 1
	s_nop 1
	s_nop 1
	s_nop 1
	s_nop 1
	s_nop 1
	s_nop 1
	s_nop 1
.Lrd_b11x:
	buffer_load_dwordx4 v[96:99], v6, s[20:23], s43 offen offset:0 sc1
	buffer_load_dwordx4 v[100:103], v6, s[20:23], s43 offen offset:2048 sc1
	buffer_load_dwordx4 v[104:107], v6, s[20:23], s50 offen offset:0 sc1
	buffer_load_dwordx4 v[108:111], v6, s[20:23], s50 offen offset:2048 sc1
	buffer_load_dwordx4 v[112:115], v6, s[20:23], s56 offen offset:0 sc1
	buffer_load_dwordx4 v[116:119], v6, s[20:23], s56 offen offset:2048 sc1
	buffer_load_dwordx4 v[120:123], v6, s[20:23], s57 offen offset:0 sc1
	buffer_load_dwordx4 v[124:127], v6, s[20:23], s57 offen offset:2048 sc1
	s_waitcnt lgkmcnt(0)
	s_nop 15
.Lrd_b11r:
	s_barrier
.Lrd_b11g:
	buffer_load_dwordx4 v[32:35], v7, s[20:23], s59 offen offset:0 sc1
	buffer_load_dwordx4 v[36:39], v7, s[20:23], s59 offen offset:1024 sc1
	buffer_load_dwordx4 v[40:43], v7, s[20:23], s59 offen offset:2048 sc1
	buffer_load_dwordx4 v[44:47], v7, s[20:23], s59 offen offset:3072 sc1
	buffer_load_dwordx4 v[48:51], v8, s[20:23], s59 offen offset:0 sc1
	buffer_load_dwordx4 v[52:55], v8, s[20:23], s59 offen offset:1024 sc1
	buffer_load_dwordx4 v[56:59], v8, s[20:23], s59 offen offset:2048 sc1
	buffer_load_dwordx4 v[60:63], v8, s[20:23], s59 offen offset:3072 sc1
	s_add_u32 s78, s58, 1
	s_and_b32 s52, s78, 15
	s_cmp_lt_u32 s78, 8
	s_cselect_b32 s18, 16, 0
	s_add_u32 s52, s52, s18
	s_lshl_b32 s52, s52, 22
	s_add_u32 s52, s52, s32
	s_add_u32 s51, s78, 1
	s_add_u32 s53, s52, 4096
	s_add_u32 s54, s52, 16384
	s_add_u32 s55, s52, 20480
	s_waitcnt vmcnt(14)
	v_max_u32 v31, v96, v100
	v_permlane16_swap_b32 v96, v97
	v_permlane16_swap_b32 v98, v99
	v_permlane16_swap_b32 v100, v101
	v_permlane16_swap_b32 v102, v103
	s_waitcnt vmcnt(12)
	v_max3_u32 v31, v104, v108, v31
	v_permlane16_swap_b32 v104, v105
	v_permlane16_swap_b32 v106, v107
	v_permlane16_swap_b32 v108, v109
	v_permlane16_swap_b32 v110, v111
	s_waitcnt vmcnt(10)
	v_max3_u32 v31, v112, v116, v31
	v_permlane16_swap_b32 v112, v113
	v_permlane16_swap_b32 v114, v115
	v_permlane16_swap_b32 v116, v117
	v_permlane16_swap_b32 v118, v119
	s_waitcnt vmcnt(8)
	v_max3_u32 v31, v120, v124, v31
	v_cmp_eq_u32 vcc, -1, v31
	v_permlane16_swap_b32 v120, v121
	v_permlane16_swap_b32 v122, v123
	v_permlane16_swap_b32 v124, v125
	v_permlane16_swap_b32 v126, v127
	s_cbranch_vccnz .Lrd_f10y
.Lrd_c12y:
	s_waitcnt vmcnt(7)
	v_smfmac_f32_16x16x64_bf16 v[128:131], v[32:35], v[64:71], v4
	s_waitcnt vmcnt(6)
	v_smfmac_f32_16x16x64_bf16 v[128:131], v[36:39], v[72:79], v4
	v_max3_u32 v31, v32, v36, v31
	s_waitcnt vmcnt(5)
	v_smfmac_f32_16x16x64_bf16 v[128:131], v[40:43], v[80:87], v4
	s_waitcnt vmcnt(4)
	v_smfmac_f32_16x16x64_bf16 v[128:131], v[44:47], v[88:95], v4
	v_max3_u32 v31, v40, v44, v31
	s_waitcnt vmcnt(3)
	v_smfmac_f32_16x16x64_bf16 v[128:131], v[48:51], v[96:103], v4
	ds_read2st64_b64 v[24:27], v10 offset0:1 offset1:2
	ds_read_b64 v[28:29], v10 offset:1536
	s_waitcnt vmcnt(2)
	v_smfmac_f32_16x16x64_bf16 v[128:131], v[52:55], v[104:111], v4
	v_max3_u32 v31, v48, v52, v31
	s_waitcnt vmcnt(1)
	v_smfmac_f32_16x16x64_bf16 v[128:131], v[56:59], v[112:119], v4
	s_waitcnt vmcnt(0)
	v_smfmac_f32_16x16x64_bf16 v[128:131], v[60:63], v[120:127], v4
	v_max3_u32 v31, v56, v60, v31
	s_waitcnt lgkmcnt(0)
	v_max3_u32 v30, v24, v26, v28
	v_max_u32 v30, v30, v31
	v_cmp_le_u32 vcc, -2, v30
	v_pk_add_f32 v[20:21], v[24:25], v[26:27]
	v_pk_add_f32 v[20:21], v[20:21], v[28:29]
.Lrd_c12:
	s_nop 0
	v_pk_add_f32 v[22:23], v[128:129], v[130:131]
	v_pk_add_f32 v[22:23], v[22:23], v[20:21]
	v_cvt_pk_bf16_f32 v22, v22, v23
	s_nop 1
	v_mov_b32_dpp v23, v22 quad_perm:[1,0,3,2] row_mask:0xf bank_mask:0xf
	v_perm_b32 v32, v23, v22, v11
	s_cbranch_vccnz .Lrd_f10p
	s_nop 0
	v_mov_b32_dpp v33, v32 row_ror:12 row_mask:0xf bank_mask:0xf
	v_mov_b32_dpp v34, v32 row_ror:8 row_mask:0xf bank_mask:0xf
	v_mov_b32_dpp v35, v32 row_ror:4 row_mask:0xf bank_mask:0xf
	buffer_store_dwordx4 v[32:35], v9, s[20:23], s42 offen sc1
	s_memrealtime s[98:99]
	s_barrier
	ds_read_b32 v21, v19 offset:12
	ds_write2st64_b64 v10, v[132:133], v[132:133] offset0:1 offset1:2
	ds_write_b64 v10, v[132:133] offset:1536
	v_readfirstlane_b32 s49, v1
	s_waitcnt lgkmcnt(2)
	v_readfirstlane_b32 s70, v21
	s_sub_u32 s18, s61, s33
	s_max_u32 s70, s70, s18
	s_add_u32 s70, s70, s33
	s_add_u32 s78, s58, 1
	s_waitcnt lgkmcnt(0)
	s_sub_u32 s76, s98, s72
	s_min_u32 s76, s76, 0x3000
	s_pack_ll_b32_b16 s77, s78, s76
	s_and_b32 s18, s19, 255
	s_min_u32 s18, s18, 1
	s_lshl_b32 s18, s18, 31
	s_or_b32 s77, s77, s18
	v_mov_b32 v36, s77
	s_cmp_eq_u32 s61, s70
	s_cselect_b64 exec, 1, 0
	s_mov_b32 s19, 0
	buffer_store_dword v36, off, s[20:23], s38 sc1
	s_mov_b64 exec, -1
	v_min_u32_dpp v18, v18, v18 row_ror:8 row_mask:0xf bank_mask:0xf
	s_nop 1
	v_min_u32_dpp v18, v18, v18 row_ror:4 row_mask:0xf bank_mask:0xf
	s_nop 1
	v_min_u32_dpp v18, v18, v18 row_ror:2 row_mask:0xf bank_mask:0xf
	s_nop 1
	v_min_u32_dpp v18, v18, v18 row_ror:1 row_mask:0xf bank_mask:0xf
	s_nop 0
	v_readfirstlane_b32 s78, v18
	s_max_u32 s73, s73, s78
	s_cmp_ge_u32 s73, s51
	s_cselect_b32 s48, s37, 0
	s_cmp_ge_u32 s48, 2
	s_cselect_b32 s48, 1, 0
	s_mov_b32 s61, s70
	s_add_u32 s60, s61, s65
	s_add_u32 s82, s60, 12
	s_memrealtime s[98:99]
	s_waitcnt lgkmcnt(0)
	s_sub_u32 s18, s82, s98
	s_cmp_gt_u32 s18, 0xfa0
	s_cbranch_scc1 .Lrd_w24
.Lrd_w23:
	s_memrealtime s[98:99]
	s_waitcnt lgkmcnt(0)
	s_sub_u32 s18, s82, s98
	s_cmp_gt_i32 s18, 0
	s_cbranch_scc1 .Lrd_w23
.Lrd_w24:
.Lrd_f25:
	s_cmp_ge_u32 s73, s51
	s_cbranch_scc1 .Lrd_f26
	s_cmp_eq_u32 s69, 0
	s_cbranch_scc1 .Lrd_f26
	s_sub_u32 s69, s69, 1
	buffer_load_dword v21, v12, s[20:23], 0 offen sc1
	s_waitcnt vmcnt(0)
	s_nop 0
	v_min_u32_dpp v21, v21, v21 row_ror:8 row_mask:0xf bank_mask:0xf
	s_nop 1
	v_min_u32_dpp v21, v21, v21 row_ror:4 row_mask:0xf bank_mask:0xf
	s_nop 1
	v_min_u32_dpp v21, v21, v21 row_ror:2 row_mask:0xf bank_mask:0xf
	s_nop 1
	v_min_u32_dpp v21, v21, v21 row_ror:1 row_mask:0xf bank_mask:0xf
	s_nop 0
	v_readfirstlane_b32 s73, v21
	s_sleep 2
	s_branch .Lrd_f25
.Lrd_f26:
	buffer_load_dwordx4 v[64:67], v5, s[20:23], s52 offen offset:0 sc1
	buffer_load_dwordx4 v[68:71], v5, s[20:23], s52 offen offset:2048 sc1
	buffer_load_dwordx4 v[72:75], v5, s[20:23], s53 offen offset:0 sc1
	buffer_load_dwordx4 v[76:79], v5, s[20:23], s53 offen offset:2048 sc1
	buffer_load_dwordx4 v[80:83], v5, s[20:23], s54 offen offset:0 sc1
	buffer_load_dwordx4 v[84:87], v5, s[20:23], s54 offen offset:2048 sc1
	buffer_load_dwordx4 v[88:91], v5, s[20:23], s55 offen offset:0 sc1
	buffer_load_dwordx4 v[92:95], v5, s[20:23], s55 offen offset:2048 sc1
	buffer_load_dword v18, v12, s[20:23], 0 offen sc1
	s_cmp_ge_u32 s58, 61
	s_cbranch_scc1 .Lrd_l17
	v_permlane16_swap_b32 v96, v97
	v_permlane16_swap_b32 v98, v99
	v_permlane16_swap_b32 v100, v101
	v_permlane16_swap_b32 v102, v103
	v_permlane16_swap_b32 v104, v105
	v_permlane16_swap_b32 v106, v107
	v_permlane16_swap_b32 v108, v109
	v_permlane16_swap_b32 v110, v111
	v_permlane16_swap_b32 v112, v113
	v_permlane16_swap_b32 v114, v115
	v_permlane16_swap_b32 v116, v117
	v_permlane16_swap_b32 v118, v119
	v_permlane16_swap_b32 v120, v121
	v_permlane16_swap_b32 v122, v123
	v_permlane16_swap_b32 v124, v125
	v_permlane16_swap_b32 v126, v127
	s_add_u32 s76, s35, 0
	buffer_store_dwordx4 v[96:99], v0, s[20:23], s76 offen sc1
	buffer_store_dwordx4 v[132:135], v6, s[20:23], s43 offen offset:0 sc1
	s_add_u32 s76, s35, 98304
	buffer_store_dwordx4 v[100:103], v0, s[20:23], s76 offen sc1
	buffer_store_dwordx4 v[132:135], v6, s[20:23], s43 offen offset:2048 sc1
	s_add_u32 s76, s35, 196608
	buffer_store_dwordx4 v[104:107], v0, s[20:23], s76 offen sc1
	buffer_store_dwordx4 v[132:135], v6, s[20:23], s50 offen offset:0 sc1
	s_add_u32 s76, s35, 294912
	buffer_store_dwordx4 v[108:111], v0, s[20:23], s76 offen sc1
	buffer_store_dwordx4 v[132:135], v6, s[20:23], s50 offen offset:2048 sc1
	s_add_u32 s76, s35, 393216
	buffer_store_dwordx4 v[112:115], v0, s[20:23], s76 offen sc1
	buffer_store_dwordx4 v[132:135], v6, s[20:23], s56 offen offset:0 sc1
	s_add_u32 s76, s35, 491520
	buffer_store_dwordx4 v[116:119], v0, s[20:23], s76 offen sc1
	buffer_store_dwordx4 v[132:135], v6, s[20:23], s56 offen offset:2048 sc1
	s_add_u32 s76, s35, 589824
	buffer_store_dwordx4 v[120:123], v0, s[20:23], s76 offen sc1
	buffer_store_dwordx4 v[132:135], v6, s[20:23], s57 offen offset:0 sc1
	s_add_u32 s76, s35, 688128
	buffer_store_dwordx4 v[124:127], v0, s[20:23], s76 offen sc1
	buffer_store_dwordx4 v[132:135], v6, s[20:23], s57 offen offset:2048 sc1
.Lrd_l17:
	s_add_u32 s35, s35, 2097152
	s_xor_b32 s35, s35, 8192
	s_add_u32 s43, s43, 4194304
	s_sub_u32 s18, s43, s36
	s_cmp_eq_u32 s18, 12582912
	s_cselect_b32 s43, s36, s43
	s_and_b32 s65, s49, 1023
	s_add_u32 s61, s61, s65
	s_bfe_u32 s77, s49, 0xa000a
	s_add_u32 s61, s61, s77
	s_add_u32 s58, s58, 1
	s_cmp_lt_u32 s58, 63
	s_cbranch_scc1 .Lrd_step9
	s_branch .Lrd_end8
.Lrd_f10p:
	v_cmp_eq_u32 vcc, -1, v31
	s_cbranch_vccnz .Lrd_f10
.Lrd_f10q:
	s_add_u32 s74, s74, 0x400
	s_cselect_b32 s74, -1, s74
	s_cbranch_scc1 .Lrd_f10c
	ds_read2st64_b64 v[24:27], v10 offset0:1 offset1:2
	ds_read_b64 v[28:29], v10 offset:1536
	s_waitcnt lgkmcnt(0)
	v_max3_u32 v30, v24, v26, v28
	v_cmp_eq_u32 vcc, -1, v30
	s_cbranch_vccnz .Lrd_f10q
	v_cmp_eq_u32 vcc, -2, v30
	s_cbranch_vccnz .Lrd_f10
	v_pk_add_f32 v[20:21], v[24:25], v[26:27]
	v_pk_add_f32 v[20:21], v[20:21], v[28:29]
.Lrd_f10c:
	s_mov_b64 vcc, 0
	s_branch .Lrd_c12
.Lrd_f10y:
	s_cmp_eq_u32 s69, 0
	s_cbranch_scc1 .Lrd_c12y
	s_waitcnt vmcnt(0)
.Lrd_f10:
	s_cmp_eq_u32 s69, 0
	s_cbranch_scc1 .Lrd_f10c
	s_sub_u32 s69, s69, 1
	s_memrealtime s[98:99]
	s_add_u32 s19, s19, 1
	s_add_u32 s64, s64, 1
	v_mov_b32 v20, s64
	ds_write_b32 v19, v20 offset:8
	s_add_u32 s61, s61, 80
	s_sub_u32 s18, s61, s33
	v_mov_b32 v22, s41
	v_mov_b32 v23, s18
	ds_max_u32 v22, v23
	v_mov_b64 v[128:129], 0
	v_mov_b64 v[130:131], 0
	s_waitcnt lgkmcnt(0)
	s_barrier
	ds_write2st64_b64 v10, v[132:133], v[132:133] offset0:1 offset1:2
	ds_write_b64 v10, v[132:133] offset:1536
	s_add_u32 s98, s98, 6
	s_branch .Lrd_b11
.Lrd_b11c:
	s_sub_u32 s18, s98, s61
	s_cmp_gt_i32 s18, 24
	s_cbranch_scc1 .Lrd_b11l
.Lrd_b11d:
	s_sub_u32 s18, s61, s98
	s_sub_u32 s18, s18, 3
	s_cmp_lt_u32 s18, 0xfa0
	s_cbranch_scc1 .Lrd_b11e
	s_cmp_lg_u32 s82, 0
	s_branch .Lrd_b11x
.Lrd_b11e:
	s_memrealtime s[98:99]
	s_waitcnt lgkmcnt(0)
	s_branch .Lrd_b11d
.Lrd_b11l:
	s_mov_b32 s72, s98
	s_add_u32 s19, s19, 1
.Lrd_c27:
	s_add_u32 s61, s61, 80
	s_sub_u32 s18, s61, s98
	s_cmp_lt_i32 s18, -8
	s_cbranch_scc1 .Lrd_c27
	buffer_load_dwordx4 v[96:99], v6, s[20:23], s43 offen offset:0 sc1
	buffer_load_dwordx4 v[100:103], v6, s[20:23], s43 offen offset:2048 sc1
	buffer_load_dwordx4 v[104:107], v6, s[20:23], s50 offen offset:0 sc1
	buffer_load_dwordx4 v[108:111], v6, s[20:23], s50 offen offset:2048 sc1
	buffer_load_dwordx4 v[112:115], v6, s[20:23], s56 offen offset:0 sc1
	buffer_load_dwordx4 v[116:119], v6, s[20:23], s56 offen offset:2048 sc1
	buffer_load_dwordx4 v[120:123], v6, s[20:23], s57 offen offset:0 sc1
	buffer_load_dwordx4 v[124:127], v6, s[20:23], s57 offen offset:2048 sc1
	s_waitcnt lgkmcnt(0)
	s_barrier
	s_sub_u32 s18, s61, s33
	v_mov_b32 v22, s41
	v_mov_b32 v23, s18
	ds_max_u32 v22, v23
	s_branch .Lrd_b11g
.Lrd_h7:
.Lrd_step28:
	s_add_u32 s59, s58, 0
	s_and_b32 s59, s59, 7
	s_lshl_b32 s59, s59, 22
	s_add_u32 s59, s59, s46
	s_add_u32 s71, s59, 67584
	buffer_load_dword v1, off, s[20:23], s71 sc1
.Lrd_b30z:
	v_mov_b64 v[128:129], 0
	v_mov_b64 v[130:131], 0
	s_memrealtime s[98:99]
	s_waitcnt lgkmcnt(0)
.Lrd_b30:
	s_mov_b32 s72, s61
	s_sub_u32 s18, s61, s98
	s_sub_u32 s18, s18, 8
	s_cmp_gt_u32 s18, 0xfa0
	s_cbranch_scc1 .Lrd_b30c
.Lrd_b30r:
	s_barrier
.Lrd_b30g:
	buffer_load_dwordx4 v[32:35], v7, s[20:23], s59 offen offset:0 sc1
	buffer_load_dwordx4 v[36:39], v7, s[20:23], s59 offen offset:1024 sc1
	buffer_load_dwordx4 v[40:43], v7, s[20:23], s59 offen offset:2048 sc1
	buffer_load_dwordx4 v[44:47], v7, s[20:23], s59 offen offset:3072 sc1
	s_add_u32 s78, s58, 1
	s_and_b32 s52, s78, 15
	s_cmp_lt_u32 s78, 8
	s_cselect_b32 s18, 16, 0
	s_add_u32 s52, s52, s18
	s_lshl_b32 s52, s52, 22
	s_add_u32 s52, s52, s32
	s_add_u32 s51, s78, 1
	s_add_u32 s53, s52, 4096
	s_add_u32 s54, s52, 16384
	s_add_u32 s55, s52, 20480
	s_waitcnt vmcnt(3)
	v_smfmac_f32_16x16x64_bf16 v[128:131], v[32:35], v[64:71], v4
	s_waitcnt vmcnt(2)
	v_smfmac_f32_16x16x64_bf16 v[128:131], v[36:39], v[72:79], v4
	v_max_u32 v31, v32, v36
	s_waitcnt vmcnt(1)
	v_smfmac_f32_16x16x64_bf16 v[128:131], v[40:43], v[80:87], v4
	s_waitcnt vmcnt(0)
	v_max3_u32 v31, v40, v44, v31
	v_cmp_eq_u32 vcc, -1, v31
	v_smfmac_f32_16x16x64_bf16 v[128:131], v[44:47], v[88:95], v4
	s_nop 4
	s_cbranch_vccnz .Lrd_f29
	s_nop 1
	v_pk_add_f32 v[128:129], v[128:129], v[130:131]
.Lrd_c31:
	ds_write_b64 v10, v[128:129]
	v_readfirstlane_b32 s49, v1
	s_waitcnt lgkmcnt(0)
	s_barrier
	ds_read_b64 v[20:21], v19 offset:8
	s_waitcnt lgkmcnt(0)
	v_readfirstlane_b32 s18, v20
	s_cmp_lg_u32 s18, s64
	s_cbranch_scc1 .Lrd_f29a
	s_sleep 3
	s_cmp_eq_u32 s45, 0
	s_cbranch_scc1 .Lrd_l38
	buffer_load_dword v16, v15, s[20:23], s46 offen sc1
	buffer_load_dword v17, v13, s[20:23], s46 offen sc1
.Lrd_l38:
	v_min_u32_dpp v18, v18, v18 row_ror:8 row_mask:0xf bank_mask:0xf
	s_nop 1
	v_min_u32_dpp v18, v18, v18 row_ror:4 row_mask:0xf bank_mask:0xf
	s_nop 1
	v_min_u32_dpp v18, v18, v18 row_ror:2 row_mask:0xf bank_mask:0xf
	s_nop 1
	v_min_u32_dpp v18, v18, v18 row_ror:1 row_mask:0xf bank_mask:0xf
	s_nop 0
	v_readfirstlane_b32 s78, v18
	s_max_u32 s73, s73, s78
	s_cmp_ge_u32 s73, s51
	s_cselect_b32 s48, s37, 0
	s_cmp_ge_u32 s48, 2
	s_cselect_b32 s48, 1, 0
	v_readfirstlane_b32 s70, v21
	s_sub_u32 s18, s61, s33
	s_max_u32 s70, s70, s18
	s_add_u32 s70, s70, s33
	s_min_u32 s76, s45, s58
	s_cmp_eq_u32 s76, 0
	s_cbranch_scc1 .Lrd_l35
	v_mov_b32 v21, s67
	s_mov_b32 exec_hi, 0
	v_mov_b32 v21, s66
	s_mov_b32 exec_hi, -1
	v_mov_b32 v22, s68
	s_waitcnt vmcnt(1)
	v_and_b32 v23, 0xffff, v16
	v_cmp_gt_i32 s[76:77], 0, v16
	v_bfe_u32 v16, v16, 16, 15
	v_subrev_u32 v23, s58, v23
	v_cmp_ge_u32 vcc, 1, v23
	v_subrev_u32 v23, 3, v21
	s_nop 0
	v_cndmask_b32 v16, v21, v16, vcc
	v_cndmask_b32 v23, v23, v21, s[76:77]
	v_add_u32 v21, 12, v21
	v_med3_u32 v16, v16, v21, v23
	s_waitcnt vmcnt(0)
	v_and_b32 v23, 0xffff, v17
	v_cmp_gt_i32 s[76:77], 0, v17
	v_bfe_u32 v17, v17, 16, 15
	v_subrev_u32 v23, s58, v23
	v_cmp_ge_u32 vcc, 1, v23
	v_subrev_u32 v23, 3, v22
	s_nop 0
	v_cndmask_b32 v17, v22, v17, vcc
	v_cndmask_b32 v23, v23, v22, s[76:77]
	v_add_u32 v22, 12, v22
	v_med3_u32 v17, v17, v22, v23
	v_add_u32_dpp v16, v16, v16 row_ror:8 row_mask:0xf bank_mask:0xf
	s_nop 0
	v_add_u32_dpp v17, v17, v17 row_ror:8 row_mask:0xf bank_mask:0xf
	v_add_u32_dpp v16, v16, v16 row_ror:4 row_mask:0xf bank_mask:0xf
	s_nop 0
	v_add_u32_dpp v17, v17, v17 row_ror:4 row_mask:0xf bank_mask:0xf
	v_add_u32_dpp v16, v16, v16 row_ror:2 row_mask:0xf bank_mask:0xf
	s_nop 0
	v_add_u32_dpp v17, v17, v17 row_ror:2 row_mask:0xf bank_mask:0xf
	v_add_u32_dpp v16, v16, v16 row_ror:1 row_mask:0xf bank_mask:0xf
	s_nop 0
	v_add_u32_dpp v17, v17, v17 row_ror:1 row_mask:0xf bank_mask:0xf
	v_add_u32_dpp v16, v16, v16 row_bcast:15 row_mask:0xa bank_mask:0xf
	s_nop 0
	v_add_u32_dpp v17, v17, v17 row_bcast:15 row_mask:0xa bank_mask:0xf
	s_nop 1
	v_add_u32_dpp v17, v17, v17 row_bcast:31 row_mask:0xc bank_mask:0xf
	v_readlane_b32 s66, v16, 31
	v_readlane_b32 s67, v16, 63
	v_readlane_b32 s68, v17, 63
	s_add_u32 s66, s66, 16
	s_lshr_b32 s66, s66, 5
	s_min_u32 s66, s66, 900
	s_max_u32 s66, s66, 32
	s_add_u32 s67, s67, 16
	s_lshr_b32 s67, s67, 5
	s_min_u32 s67, s67, 900
	s_max_u32 s67, s67, 32
	s_add_u32 s68, s68, 32
	s_lshr_b32 s68, s68, 6
	s_min_u32 s68, s68, 900
	s_max_u32 s68, s68, 32
	s_add_u32 s77, s67, 11
	s_add_u32 s78, s66, 3
	s_add_u32 s79, s77, 0
	s_max_u32 s78, s78, s79
	s_lshl_b32 s77, s77, 10
	s_or_b32 s77, s77, s78
	s_lshl_b32 s77, s77, 10
	s_add_u32 s77, s77, s68
	s_add_u32 s77, s77, 8
	s_add_u32 s76, s58, 2
	s_and_b32 s76, s76, 7
	s_lshl_b32 s76, s76, 22
	s_add_u32 s76, s76, s46
	s_add_u32 s76, s76, 67584
	v_mov_b32 v22, s77
	s_mov_b64 exec, 1
	buffer_store_dword v22, off, s[20:23], s76 sc1
	s_mov_b64 exec, -1
.Lrd_l35:
	s_cmp_eq_u32 s48, 0
	s_cbranch_scc1 .Lrd_l33
	buffer_load_dwordx4 v[64:67], v5, s[20:23], s52 offen offset:0 sc1
	buffer_load_dwordx4 v[68:71], v5, s[20:23], s52 offen offset:2048 sc1
	buffer_load_dwordx4 v[72:75], v5, s[20:23], s53 offen offset:0 sc1
	buffer_load_dwordx4 v[76:79], v5, s[20:23], s53 offen offset:2048 sc1
	buffer_load_dwordx4 v[80:83], v5, s[20:23], s54 offen offset:0 sc1
	buffer_load_dwordx4 v[84:87], v5, s[20:23], s54 offen offset:2048 sc1
	buffer_load_dwordx4 v[88:91], v5, s[20:23], s55 offen offset:0 sc1
	buffer_load_dwordx4 v[92:95], v5, s[20:23], s55 offen offset:2048 sc1
.Lrd_l33:
	s_mov_b32 s61, s70
	s_add_u32 s60, s61, s65
	s_add_u32 s82, s60, 12
	s_memrealtime s[98:99]
	s_waitcnt lgkmcnt(0)
	s_sub_u32 s18, s82, s98
	s_cmp_gt_u32 s18, 0xfa0
	s_cbranch_scc1 .Lrd_w43
.Lrd_w42:
	s_memrealtime s[98:99]
	s_waitcnt lgkmcnt(0)
	s_sub_u32 s18, s82, s98
	s_cmp_gt_i32 s18, 0
	s_cbranch_scc1 .Lrd_w42
.Lrd_w43:
	s_cmp_lg_u32 s48, 0
	s_cbranch_scc1 .Lrd_l37
.Lrd_f44:
	s_cmp_ge_u32 s73, s51
	s_cbranch_scc1 .Lrd_f45
	s_cmp_eq_u32 s69, 0
	s_cbranch_scc1 .Lrd_f45
	s_sub_u32 s69, s69, 1
	buffer_load_dword v21, v12, s[20:23], 0 offen sc1
	s_waitcnt vmcnt(0)
	s_nop 0
	v_min_u32_dpp v21, v21, v21 row_ror:8 row_mask:0xf bank_mask:0xf
	s_nop 1
	v_min_u32_dpp v21, v21, v21 row_ror:4 row_mask:0xf bank_mask:0xf
	s_nop 1
	v_min_u32_dpp v21, v21, v21 row_ror:2 row_mask:0xf bank_mask:0xf
	s_nop 1
	v_min_u32_dpp v21, v21, v21 row_ror:1 row_mask:0xf bank_mask:0xf
	s_nop 0
	v_readfirstlane_b32 s73, v21
	s_sleep 2
	s_branch .Lrd_f44
.Lrd_f45:
	buffer_load_dwordx4 v[64:67], v5, s[20:23], s52 offen offset:0 sc1
	buffer_load_dwordx4 v[68:71], v5, s[20:23], s52 offen offset:2048 sc1
	buffer_load_dwordx4 v[72:75], v5, s[20:23], s53 offen offset:0 sc1
	buffer_load_dwordx4 v[76:79], v5, s[20:23], s53 offen offset:2048 sc1
	buffer_load_dwordx4 v[80:83], v5, s[20:23], s54 offen offset:0 sc1
	buffer_load_dwordx4 v[84:87], v5, s[20:23], s54 offen offset:2048 sc1
	buffer_load_dwordx4 v[88:91], v5, s[20:23], s55 offen offset:0 sc1
	buffer_load_dwordx4 v[92:95], v5, s[20:23], s55 offen offset:2048 sc1
.Lrd_l37:
	buffer_load_dword v18, v12, s[20:23], 0 offen sc1
	s_cmp_lg_u32 s37, 2
	s_cbranch_scc1 .Lrd_l34
	s_add_u32 s76, s58, 3
	s_and_b32 s76, s76, 7
	s_lshl_b32 s76, s76, 22
	s_add_u32 s76, s76, s46
	buffer_store_dwordx4 v[132:135], v9, s[20:23], s76 offen sc1
	s_cmp_eq_u32 s39, 0
	s_cbranch_scc1 .Lrd_l34
	s_add_u32 s76, s58, 1
	v_mov_b32 v22, s76
	s_mov_b64 exec, 1
	buffer_store_dword v22, off, s[20:23], s44 sc1
	s_mov_b64 exec, -1
.Lrd_l34:
	s_and_b32 s65, s49, 1023
	s_add_u32 s61, s61, s65
	s_bfe_u32 s77, s49, 0xa000a
	s_add_u32 s61, s61, s77
	s_add_u32 s58, s58, 1
	s_cmp_lt_u32 s58, 63
	s_cbranch_scc1 .Lrd_step28
	s_branch .Lrd_end8
.Lrd_f29:
	s_nop 1
	v_mov_b32 v128, -2
	s_branch .Lrd_c31
.Lrd_f29a:
	s_mov_b32 s64, s18
	s_add_u32 s19, s19, 1
	v_readfirstlane_b32 s70, v21
	s_sub_u32 s18, s61, s33
	s_max_u32 s70, s70, s18
	s_add_u32 s70, s70, s33
	s_mov_b32 s61, s70
	s_branch .Lrd_b30z
.Lrd_b30c:
	s_sub_u32 s18, s98, s61
	s_cmp_gt_i32 s18, 24
	s_cbranch_scc0 .Lrd_b30r
.Lrd_b30l:
	s_mov_b32 s72, s98
	s_add_u32 s19, s19, 1
.Lrd_c46:
	s_add_u32 s61, s61, 80
	s_sub_u32 s18, s61, s98
	s_cmp_lt_i32 s18, -8
	s_cbranch_scc1 .Lrd_c46
	s_barrier
	s_sub_u32 s18, s61, s33
	v_mov_b32 v22, s41
	v_mov_b32 v23, s18
	ds_max_u32 v22, v23
	s_branch .Lrd_b30g
.Lrd_end8:
	s_xor_b32 s52, s52, 1024
	s_xor_b32 s53, s53, 1024
	s_xor_b32 s54, s54, 1024
	s_xor_b32 s55, s55, 1024
	buffer_load_dwordx4 v[96:99], v5, s[20:23], s52 offen offset:0 sc1
	buffer_load_dwordx4 v[100:103], v5, s[20:23], s52 offen offset:2048 sc1
	buffer_load_dwordx4 v[104:107], v5, s[20:23], s53 offen offset:0 sc1
	buffer_load_dwordx4 v[108:111], v5, s[20:23], s53 offen offset:2048 sc1
	buffer_load_dwordx4 v[112:115], v5, s[20:23], s54 offen offset:0 sc1
	buffer_load_dwordx4 v[116:119], v5, s[20:23], s54 offen offset:2048 sc1
	buffer_load_dwordx4 v[120:123], v5, s[20:23], s55 offen offset:0 sc1
	buffer_load_dwordx4 v[124:127], v5, s[20:23], s55 offen offset:2048 sc1
	buffer_load_dword v2, v2, s[20:23], 0 offen sc1
	s_add_u32 s33, s61, 16
	s_waitcnt vmcnt(0) lgkmcnt(0)
)ASM" : "+{v[0:15]}"(vi), "+{s[32:47]}"(si), "={v[64:79]}"(u0), "={v[80:95]}"(u1), "={v[96:111]}"(u2), "={v[112:127]}"(u3) : "{s[20:23]}"(rq) : "s18", "s19", "s48", "s49", "s50", "s51", "s52", "s53", "s54", "s55", "s56", "s57", "s58", "s59", "s60", "s61", "s62", "s63", "s64", "s65", "s66", "s67", "s68", "s69", "s70", "s71", "s72", "s73", "s74", "s75", "s76", "s77", "s78", "s79", "s82", "s98", "s99", "v16", "v17", "v18", "v19", "v20", "v21", "v22", "v23", "v24", "v25", "v26", "v27", "v28", "v29", "v30", "v31", "v32", "v33", "v34", "v35", "v36", "v37", "v38", "v39", "v40", "v41", "v42", "v43", "v44", "v45", "v46", "v47", "v48", "v49", "v50", "v51", "v52", "v53", "v54", "v55", "v56", "v57", "v58", "v59", "v60", "v61", "v62", "v63", "v128", "v129", "v130", "v131", "v132", "v133", "v134", "v135", "vcc", "scc", "memory");
  u32x4 U[16];
#define UX(k, x) U[k] = __builtin_shufflevector(x, x, 0, 1, 2, 3); U[k + 2] = __builtin_shufflevector(x, x, 4, 5, 6, 7); U[k + 4] = __builtin_shufflevector(x, x, 8, 9, 10, 11); U[k + 6] = __builtin_shufflevector(x, x, 12, 13, 14, 15);
  UX(0, u0) UX(8, u1) UX(1, u2) UX(9, u3)
  KArgs& b = reload(&a);
  const int w2 = si[5], r2 = si[2];
  rsrc_t rs = mkrs(b.ws, WS_RANGE);
  unsigned prog = min16(vi[2]);
  bool dead = false;
  wait_flag(rs, flag2(si[8], 32 + 2 * w2 + (lane & 1)), NSEG, prog, dead);
  memfence();
  tok_core<1>(b, rs, lds, U, 8 * ((w2 + r2) & 3), NSEG - 1, r2 >> 1, r2 & 1, r2 & 1, si[8], si[1], prog, lane, w2, dead);
  if (DIAG && r2 == 0) { const unsigned t = rtl(), d = DIAG == 4 ? 2000u : DIAG == 5 ? min(4 * (si[0] - rfl(*(LAS unsigned*)(lds + 100000))), 10000u) : min(si[0], 10000u); for (int i = 1; i <= 4; ++i) wait_t(t + d * i / 4); }
  if (r2 == 0 && w2 == 0) { const unsigned n = calltag(rs); st1(n, rs, lane ? OOB : OFF_CALL, 0); }
  return 0;
}

__global__ void __launch_bounds__(256, 1) k_main(Args a) {
  extern __shared__ __attribute__((aligned(16))) unsigned char smem[];
  ldsp lds = (ldsp)smem;
  if (DIAG == 5) sts1(lds + 100000, rtl());
  int wv = rfl(threadIdx.x >> 6), lane;
  unsigned bid = blockIdx.x, att = FALLB ? rfl(bid == 77) : 0;
  KArgs* ap = (KArgs*)__builtin_amdgcn_kernarg_segment_ptr();
  SB();
  asm volatile(".Lagain:\n\tv_mbcnt_lo_u32_b32 %0, -1, 0\n\ts_getpc_b64 s[16:17]\n.Lpc:\n\tv_mbcnt_hi_u32_b32 %0, -1, %0\n\ts_lshr_b32 s18, s9, 3\n\ts_lshl2_add_u32 s18, s18, s8\n\ts_lshl_b32 s19, s18, 7\n\t"
               "v_lshl_add_u32 v255, %0, 13, s19\n\ts_and_b32 s17, s17, 0xffff\n\ts_cmp_lt_u32 s18, 64\n\ts_cselect_b32 s18, .Lfin-.Lpc, 0\n\ts_mov_b32 s19, 0x20000\n\tbuffer_load_dword v255, v255, s[16:19], 0 offen"
               : "=v"(lane), "+{s8}"(wv), "+{s9}"(bid), "+{s10}"(att), "+{s[12:13]}"(ap)::"memory", "scc", "s16", "s17", "s18", "s19", "v255");
  SB();
  const int X = att ? bid & 7 : xcc_id(), t = bid >> 3;
  const unsigned hp = ((X - bid) & 7u) | 24u | ((FALLB ? att > (bid == 77) : att) * 1024 + bid * 4 + (wv ? 2048 : 0)) << 8;
  const int G = X & 3, u = X < 4 ? t - 26 : MAP == 2 && t >= 28 ? t - 24 : t - 2;
  const int b = X < 4 || (t >= 8 && t < 18) ? (u & 1) * 32 + 8 * G + (0x75420631u >> (u >> 1) * 4 & 7) : t < 8 ? 64 + 8 * G + t : t < 20 ? 96 + 2 * G + t - 18 : t < 28 ? 104 + 8 * G + t - 20 : t < 30 ? 136 + 2 * G + t - 28 : 144 + 2 * G + t - 30;
  unsigned sd = 2;
  if (X < 4 && t < 8) role_qk(reload(ap), 8 * X + t, hp, lds, lane, wv);
  else if (X < 4 && t < 16) role_vo(reload(ap), 8 * X + t - 8, hp, lds, lane, wv);
  else {
    const bool red = X < 4 && t < 26;
    unsigned exp = 0;
    if (red || b >= 144) exp = helper(reload(ap), red ? 10 * X + t - 16 : b - 104, hp, lds, lane, wv);
    if (red) sd = role_red(reload(ap), 10 * X + t - 16, hp, exp, lds, lane, wv);
    else if (b < 104) sd = role_t1(reload(ap), b, hp, lds, lane, wv);
    else if (b < 144) sd = role_t2(reload(ap), b - 104, hp, lds, lane, wv);
    else { sd = rfl(hs_wait(mkrs(ap->ws, WS_RANGE), OFF_ARR + ((hp >> 8) & 1024u), exp, lane)); sd = sd == 1 ? 2 : sd >> 13 & 1;
      if (PF && sd != 2 && !(b & 1) && wv == 0) {
        bool dead = false;
        unsigned seen = 0;
        for (int k = PF; k >= PG; k -= PH) {
          wait_flag(mkrs(ap->ws, WS_RANGE), flag2(sd, 8 * G + (lane & 7)), PA - k, seen, dead);
          asm volatile("buffer_wbl2 sc1\n\ts_waitcnt vmcnt(0)" ::: "memory");
        }
      }
    }
  }
  if (sd == 2) {
    asm volatile("s_waitcnt vmcnt(0) lgkmcnt(0)\n\ts_barrier\n\ts_branch .Lagain\n.Lfin:" ::"{s8}"(wv), "{s9}"(bid), "{s10}"(att + 1), "{s[12:13]}"(ap) : "memory");
    __builtin_unreachable();
  }
  asm volatile("v_mbcnt_lo_u32_b32 %0, -1, 0\n\tv_mbcnt_hi_u32_b32 %0, -1, %0" : "=v"(lane));
  if (X >= 4) role_tok(reload(ap), sd, lds, lane, wv);
}

extern "C" void kernel_launch(void* const* d_in, const int* in_sizes, int n_in, void* d_out, int out_size, void* d_ws, size_t ws_size, hipStream_t stream) {
  Args a;
  a.x = (const u16*)d_in[0]; a.mem0 = (const u16*)d_in[1];
  a.Wq = (const u16*)d_in[2]; a.Wk = (const u16*)d_in[3]; a.Wv = (const u16*)d_in[4]; a.Wo = (const u16*)d_in[5];
  a.w1 = (const u16*)d_in[8]; a.b1 = (const u16*)d_in[9]; a.w2 = (const u16*)d_in[10]; a.b2 = (const u16*)d_in[11];
  a.out = (u16*)d_out; a.ws = (unsigned char*)d_ws;
  (void)hipFuncSetAttribute((const void*)k_main, hipFuncAttributeMaxDynamicSharedMemorySize, LDS_B);
  hipLaunchKernelGGL(k_main, dim3(256), dim3(256), LDS_B, stream, a);
}
```
